```python
import torch

def _softmax(x, axis=-1):
    return torch.softmax(x, dim=axis)

def _gelu(x):
    return torch.nn.functional.gelu(x, approximate='tanh')

def _silu(x):
    return torch.nn.functional.silu(x)

def _sigmoid(x):
    return torch.sigmoid(x)

def _layernorm(x, axis=-1, eps=1e-05):
    m = x.mean(dim=axis, keepdim=True)
    v = x.var(dim=axis, keepdim=True, unbiased=False)
    return (x - m) / torch.sqrt(v + eps)

def _scatter_add(tgt, idx, src, axis=0):
    return tgt.clone().index_add_(axis, idx, src)

def _segment_sum(src, seg, n):
    out = torch.zeros((n,) + src.shape[1:], dtype=src.dtype)
    return out.index_add_(0, seg, src)

def _pad(x, pw):
    flat = tuple((v for pair in reversed(pw) for v in pair))
    return torch.nn.functional.pad(x, flat)
kernel_name = 'adaLN_cond__ssm_conv_s4__glu_gated_reglu__prompt'

def setup_inputs():

    _w = lambda a: (lambda t: t.to(torch.bfloat16) if t.is_floating_point() else t)(torch.as_tensor(a))

    def _A_setup():
        B, L, D, N = (1, 2048, 576, 128)
        A_re = -(lambda _x: torch.exp(torch.as_tensor(_x)))((lambda *s: torch.randn(*s, dtype=torch.bfloat16) * 3.2)(D, N))
        A_im = torch.pi * (lambda *s: torch.randn(*s, dtype=torch.bfloat16) * 0.05)(D, N)
        B_re = (lambda *s: torch.randn(*s, dtype=torch.bfloat16) * 0.1)(D, N)
        B_im = (lambda *s: torch.randn(*s, dtype=torch.bfloat16) * 0.1)(D, N)
        C_re = (lambda *s: torch.randn(*s, dtype=torch.bfloat16) * 0.1)(D, N)
        C_im = (lambda *s: torch.randn(*s, dtype=torch.bfloat16) * 0.1)(D, N)
        log_dt = (lambda *s: torch.randn(*s, dtype=torch.bfloat16) * 0.05)(D) * 0.5 - 4.0
        Dskip = (lambda *s: torch.randn(*s, dtype=torch.bfloat16) * 0.05)(D)
        return {'u': _w((lambda *s: torch.randn(*s, dtype=torch.bfloat16) * 0.05)(B, L, D)), 'A_re': _w(A_re), 'A_im': _w(A_im), 'B_re': _w(B_re), 'B_im': _w(B_im), 'C_re': _w(C_re), 'C_im': _w(C_im), 'log_dt': _w(log_dt), 'Dskip': _w(Dskip)}
    _A_in = _A_setup()

    def _B_setup():
        B, S, D, F = (1, 2048, 576, 4608)
        return {'x': _w((lambda *s: torch.randn(*s, dtype=torch.bfloat16) * 0.05)(B, S, D)), 'W_gate': _w((lambda *s: torch.randn(*s, dtype=torch.bfloat16) * 0.8)(D, F) * (2.0 / D) ** 0.5), 'W_up': _w((lambda *s: torch.randn(*s, dtype=torch.bfloat16) * 0.8)(D, F) * (2.0 / D) ** 0.5), 'W_down': _w((lambda *s: torch.randn(*s, dtype=torch.bfloat16) * 0.8)(F, D) * (1.0 / F) ** 0.5)}
    _B_in = _B_setup()
    return {'A_u': _A_in['u'], 'A_A_re': _A_in['A_re'], 'A_A_im': _A_in['A_im'], 'A_B_re': _A_in['B_re'], 'A_B_im': _A_in['B_im'], 'A_C_re': _A_in['C_re'], 'A_C_im': _A_in['C_im'], 'A_log_dt': _A_in['log_dt'], 'A_Dskip': _A_in['Dskip'], 'B_x': _B_in['x'], 'B_W_gate': _B_in['W_gate'], 'B_W_up': _B_in['W_up'], 'B_W_down': _B_in['W_down'], 'c': _w((lambda *s: torch.randn(*s, dtype=torch.bfloat16) * 0.2)(1, 576) * 8.0), 'w_mod': _w((lambda *s: torch.randn(*s, dtype=torch.bfloat16) * 0.2)(576, 6 * 576) * 576 ** (-0.5) * 8.0)}

def reference(A_u, A_A_re, A_A_im, A_B_re, A_B_im, A_C_re, A_C_im, A_log_dt, A_Dskip, B_x, B_W_gate, B_W_up, B_W_down, c, w_mod):

    def _A_ref(u, A_re, A_im, B_re, B_im, C_re, C_im, log_dt, Dskip):
        Bsz, L, D = u.shape
        dt = torch.exp(log_dt)[:, None]
        A = A_re + 1j * A_im
        Bc = B_re + 1j * B_im
        Cc = C_re + 1j * C_im
        logdA = dt * A
        dB = dt * Bc
        l = (lambda x, dt: x.to(dt))(torch.arange(L), A_re.dtype)
        P = torch.exp(logdA[:, :, None] * l[None, None, :])
        K = torch.einsum('dn,dnl,dn->dl', Cc, P, dB).real
        L2 = 2 * L
        Kp = _pad(K, ((0, 0), (0, L)))
        up = (lambda x, *d: x.permute(*d))(u, 0, 2, 1)
        up = _pad(up, ((0, 0), (0, 0), (0, L)))
        Kf = (lambda x, axes=(-2, -1): torch.fft.rfft2(x.float(), dim=axes))(Kp[:, None, :], axes=(-2, -1))
        Uf = (lambda x, axes=(-2, -1): torch.fft.rfft2(x.float(), dim=axes))(up[:, :, None, :], axes=(-2, -1))
        Yf = Uf * Kf[None, :, :, :]
        y = (lambda x, s, axes=(-2, -1): torch.fft.irfft2(x, s=s, dim=axes).to(torch.bfloat16))(Yf, (1, L2), axes=(-2, -1))[:, :, 0, :L]
        y = (lambda x, *d: x.permute(*d))(y, 0, 2, 1) + u * Dskip[None, None, :]
        return y

    def _B_ref(x, W_gate, W_up, W_down):
        gate = torch.einsum('bsd,df->bsf', x, W_gate)
        up = torch.einsum('bsd,df->bsf', x, W_up)
        gate = gate * (lambda x, dt: x.to(dt))(gate > 0, gate.dtype)
        h = gate * up
        return torch.einsum('bsf,fd->bsd', h, W_down)
    x = A_u
    mod = (_silu(c) @ w_mod).reshape(c.shape[0], 6, 1, -1)
    sa, ca, ga, sb, cb, gb = (mod[:, 0].clone(), mod[:, 1].clone(), mod[:, 2].clone(), mod[:, 3].clone(), mod[:, 4].clone(), mod[:, 5].clone())
    h = _layernorm(x, axis=-1) * (1 + ca) + sa
    x = x + ga * _A_ref(h, A_A_re, A_A_im, A_B_re, A_B_im, A_C_re, A_C_im, A_log_dt, A_Dskip)
    h = _layernorm(x, axis=-1) * (1 + cb) + sb
    x = x + gb * _B_ref(h, B_W_gate, B_W_up, B_W_down)
    return x
```

```cpp
#include <hip/hip_runtime.h>
#include <hip/hip_bf16.h>
#include <stdint.h>

typedef unsigned short u16;
constexpr int L = 2048, D = 576, NS = 128, F = 4608;

__device__ __forceinline__ float bf2f(u16 v) { return __uint_as_float(((unsigned)v) << 16); }
__device__ __forceinline__ u16 f2bf(float f) { __hip_bfloat16 b = __float2bfloat16(f); return *(u16*)&b; }
__device__ __forceinline__ float rbf(float f) { return bf2f(f2bf(f)); }

__global__ void k_mod(const u16* c, const u16* w, float* mod) {
    int j = blockIdx.x * blockDim.x + threadIdx.x;
    if (j >= 6 * D) return;
    float acc = 0.f;
    for (int k = 0; k < D; ++k) {
        float cv = bf2f(c[k]);
        float s = rbf(cv / (1.f + __expf(-cv)));
        acc += s * bf2f(w[(size_t)k * 6 * D + j]);
    }
    mod[j] = rbf(acc);
}

__global__ void k_ln(const float* xf, const u16* xb, const float* shift, const float* scale, float* h) {
    int t = blockIdx.x;
    __shared__ float red[256];
    float v[3];
    float s = 0.f;
    for (int i = 0; i < 3; ++i) {
        int d = threadIdx.x + i * 256;
        v[i] = d < D ? (xf ? xf[t * D + d] : bf2f(xb[t * D + d])) : 0.f;
        s += v[i];
    }
    red[threadIdx.x] = s; __syncthreads();
    for (int o = 128; o > 0; o >>= 1) { if (threadIdx.x < o) red[threadIdx.x] += red[threadIdx.x + o]; __syncthreads(); }
    float mean = red[0] / D; __syncthreads();
    float q = 0.f;
    for (int i = 0; i < 3; ++i) { int d = threadIdx.x + i * 256; if (d < D) q += (v[i] - mean) * (v[i] - mean); }
    red[threadIdx.x] = q; __syncthreads();
    for (int o = 128; o > 0; o >>= 1) { if (threadIdx.x < o) red[threadIdx.x] += red[threadIdx.x + o]; __syncthreads(); }
    float rs = rsqrtf(red[0] / D + 1e-5f);
    for (int i = 0; i < 3; ++i) { int d = threadIdx.x + i * 256; if (d < D) h[t * D + d] = rbf((v[i] - mean) * rs * (1.f + scale[d]) + shift[d]); }
}

__global__ void k_kgen(const u16* Are, const u16* Aim, const u16* Bre, const u16* Bim, const u16* Cre, const u16* Cim, const u16* logdt, float* K) {
    int d = blockIdx.y;
    int l = blockIdx.x * blockDim.x + threadIdx.x;
    float dt = rbf(expf(bf2f(logdt[d])));
    float lf = rbf((float)l);
    float acc = 0.f;
    for (int n = 0; n < NS; ++n) {
        int i = d * NS + n;
        float zr = dt * bf2f(Are[i]), zi = dt * bf2f(Aim[i]);
        float br = dt * bf2f(Bre[i]), bi = dt * bf2f(Bim[i]);
        float cr = bf2f(Cre[i]), ci = bf2f(Cim[i]);
        float wr = cr * br - ci * bi, wi = cr * bi + ci * br;
        float e = expf(zr * lf), s, co;
        sincosf(zi * lf, &s, &co);
        acc += e * (wr * co - wi * s);
    }
    K[d * L + l] = acc;
}

__global__ void k_conv(const float* K, const float* h, const u16* x, const u16* Dskip, const float* ga, float* x2) {
    int d = blockIdx.y;
    int t = blockIdx.x * blockDim.x + threadIdx.x;
    float acc = 0.f;
    for (int j = 0; j <= t; ++j) acc += K[d * L + j] * h[(t - j) * D + d];
    float y = rbf(acc) + h[t * D + d] * bf2f(Dskip[d]);
    x2[t * D + d] = rbf(bf2f(x[t * D + d]) + ga[d] * y);
}

__global__ void k_ffn1(const float* h, const u16* Wg, const u16* Wu, float* hh) {
    int f = blockIdx.x * blockDim.x + threadIdx.x;
    int t = blockIdx.y;
    float g = 0.f, u = 0.f;
    for (int k = 0; k < D; ++k) { float a = h[t * D + k]; g += a * bf2f(Wg[(size_t)k * F + f]); u += a * bf2f(Wu[(size_t)k * F + f]); }
    hh[(size_t)t * F + f] = rbf(fmaxf(g, 0.f) * u);
}

__global__ void k_ffn2(const float* hh, const u16* Wd, const float* x2, const float* gb, u16* out) {
    int d = blockIdx.x * blockDim.x + threadIdx.x;
    int t = blockIdx.y;
    if (d >= D) return;
    float a = 0.f;
    for (int k = 0; k < F; ++k) a += hh[(size_t)t * F + k] * bf2f(Wd[(size_t)k * D + d]);
    out[t * D + d] = f2bf(x2[t * D + d] + gb[d] * a);
}

extern "C" void kernel_launch(void* const* d_in, const int* in_sizes, int n_in,
                              void* d_out, int out_size, void* d_ws, size_t ws_size,
                              hipStream_t stream) {
    const u16* x = (const u16*)d_in[0];
    const u16 *Are = (const u16*)d_in[1], *Aim = (const u16*)d_in[2], *Bre = (const u16*)d_in[3], *Bim = (const u16*)d_in[4];
    const u16 *Cre = (const u16*)d_in[5], *Cim = (const u16*)d_in[6], *logdt = (const u16*)d_in[7], *Dskip = (const u16*)d_in[8];
    const u16 *Wg = (const u16*)d_in[10], *Wu = (const u16*)d_in[11], *Wd = (const u16*)d_in[12];
    const u16 *c = (const u16*)d_in[13], *wmod = (const u16*)d_in[14];
    float* ws = (float*)d_ws;
    float* mod = ws; ws += 4096;
    float* h1 = ws; ws += L * D;
    float* K = ws; ws += L * D;
    float* x2 = ws; ws += L * D;
    float* h2 = ws; ws += L * D;
    float* hh = ws;
    k_mod<<<(6 * D + 63) / 64, 64, 0, stream>>>(c, wmod, mod);
    k_ln<<<L, 256, 0, stream>>>(nullptr, x, mod + 0 * D, mod + 1 * D, h1);
    k_kgen<<<dim3(L / 256, D), 256, 0, stream>>>(Are, Aim, Bre, Bim, Cre, Cim, logdt, K);
    k_conv<<<dim3(L / 256, D), 256, 0, stream>>>(K, h1, x, Dskip, mod + 2 * D, x2);
    k_ln<<<L, 256, 0, stream>>>(x2, nullptr, mod + 3 * D, mod + 4 * D, h2);
    k_ffn1<<<dim3(F / 256, L), 256, 0, stream>>>(h2, Wg, Wu, hh);
    k_ffn2<<<dim3(3, L), 256, 0, stream>>>(hh, Wd, x2, mod + 5 * D, (u16*)d_out);
}
```

```cpp
#include <hip/hip_runtime.h>

static __device__ const unsigned short k_pos[1312] __attribute__((aligned(128))) = {2048, 2048, 2048, 2048, 0, 2, 256, 258, 64, 66, 320, 322, 4, 6, 260, 262, 68, 70, 324, 326, 8, 10, 264, 266, 72, 74, 328, 330, 12, 14, 268, 270, 76, 78, 332, 334, 16, 18, 272, 274, 80, 82, 336, 338, 20, 22, 276, 278, 84, 86, 340, 342, 24, 26, 280, 282, 88, 90, 344, 346, 28, 30, 284, 286, 92, 94, 348, 350, 32, 34, 288, 290, 96, 98, 352, 354, 36, 38, 292, 294, 100, 102, 356, 358, 40, 42, 296, 298, 104, 106, 360, 362, 44, 46, 300, 302, 108, 110, 364, 366, 48, 50, 304, 306, 112, 114, 368, 370, 52, 54, 308, 310, 116, 118, 372, 374, 56, 58, 312, 314, 120, 122, 376, 378, 60, 62, 316, 318, 124, 126, 380, 382, 512, 514, 768, 770, 576, 578, 832, 834, 516, 518, 772, 774, 580, 582, 836, 838, 520, 522, 776, 778, 584, 586, 840, 842, 524, 526, 780, 782, 588, 590, 844, 846, 528, 530, 784, 786, 592, 594, 848, 850, 532, 534, 788, 790, 596, 598, 852, 854, 536, 538, 792, 794, 600, 602, 856, 858, 540, 542, 796, 798, 604, 606, 860, 862, 544, 546, 800, 802, 608, 610, 864, 866, 548, 550, 804, 806, 612, 614, 868, 870, 552, 554, 808, 810, 616, 618, 872, 874, 556, 558, 812, 814, 620, 622, 876, 878, 560, 562, 816, 818, 624, 626, 880, 882, 564, 566, 820, 822, 628, 630, 884, 886, 568, 570, 824, 826, 632, 634, 888, 890, 572, 574, 828, 830, 636, 638, 892, 894, 128, 128, 130, 384, 384, 384, 386, 132, 132, 132, 134, 388, 388, 388, 390, 136, 136, 136, 138, 392, 392, 392, 394, 140, 140, 140, 142, 396, 396, 396, 398, 144, 144, 144, 146, 400, 400, 400, 402, 148, 148, 148, 150, 404, 404, 404, 406, 152, 152, 152, 154, 408, 408, 408, 410, 156, 156, 156, 158, 412, 412, 412, 414, 160, 160, 160, 162, 416, 416, 416, 418, 164, 164, 164, 166, 420, 420, 420, 422, 168, 168, 168, 170, 424, 424, 424, 426, 172, 172, 172, 174, 428, 428, 428, 430, 176, 176, 176, 178, 432, 432, 432, 434, 180, 180, 180, 182, 436, 436, 436, 438, 184, 184, 184, 186, 440, 440, 440, 442, 188, 188, 188, 190, 444, 444, 444, 446, 640, 640, 640, 642, 896, 896, 896, 898, 644, 644, 644, 646, 900, 900, 900, 902, 648, 648, 648, 650, 904, 904, 904, 906, 652, 652, 652, 654, 908, 908, 908, 910, 656, 656, 656, 658, 912, 912, 912, 914, 660, 660, 660, 662, 916, 916, 916, 918, 664, 664, 664, 666, 920, 920, 920, 922, 668, 668, 668, 670, 924, 924, 924, 926, 672, 672, 672, 674, 928, 928, 928, 930, 676, 676, 676, 678, 932, 932, 932, 934, 680, 680, 680, 682, 936, 936, 936, 938, 684, 684, 684, 686, 940, 940, 940, 942, 688, 688, 688, 690, 944, 944, 944, 946, 692, 692, 692, 694, 948, 948, 948, 950, 696, 696, 696, 2048, 2048, 2048, 2048, 2048, 2048, 2048, 2048, 2048, 2048, 2048, 2048, 2048, 2048, 2048, 2048, 2048, 2048, 2048, 2048, 2048, 2048, 2048, 2048, 2048, 2048, 2048, 2048, 2048, 2048, 2048, 2048, 2048, 2048, 2048, 2048, 2048, 2048, 2048, 2048, 2048, 2048, 194, 196, 198, 200, 202, 204, 206, 208, 210, 212, 214, 216, 218, 220, 222, 224, 226, 228, 230, 232, 234, 236, 238, 240, 242, 244, 246, 248, 250, 252, 254, 704, 706, 708, 710, 712, 714, 716, 718, 720, 722, 724, 726, 728, 730, 732, 734, 736, 738, 740, 742, 744, 746, 748, 750, 752, 754, 756, 758, 760, 762, 764, 766, 448, 450, 452, 454, 456, 458, 460, 462, 464, 466, 468, 470, 472, 474, 476, 478, 480, 482, 484, 486, 488, 490, 492, 494, 496, 498, 500, 502, 504, 506, 508, 510, 960, 962, 964, 966, 968, 970, 972, 974, 976, 978, 980, 982, 984, 986, 988, 990, 992, 994, 996, 998, 1000, 1002, 1004, 1006, 1008, 1010, 1012, 1014, 1016, 1018, 1020, 1022, 1024, 1028, 1032, 1036, 1040, 1044, 1048, 1052, 1056, 1060, 1064, 1068, 1072, 1076, 1080, 1084, 1536, 1540, 1544, 1548, 1552, 1556, 1560, 1564, 1568, 1572, 1576, 1580, 1584, 1588, 1592, 1596, 1026, 1030, 1034, 1038, 1042, 1046, 1050, 1054, 1058, 1062, 1066, 1070, 1074, 1078, 1082, 1086, 1538, 1542, 1546, 1550, 1554, 1558, 1562, 1566, 1570, 1574, 1578, 1582, 1586, 1590, 1594, 1598, 1280, 1284, 1288, 1292, 1296, 1300, 1304, 1308, 1312, 1316, 1320, 1324, 1328, 1332, 1336, 1340, 1792, 1796, 1800, 1804, 1808, 1812, 1816, 1820, 1824, 1828, 1832, 1836, 1840, 1844, 1848, 1852, 1282, 1286, 1290, 1294, 1298, 1302, 1306, 1310, 1314, 1318, 1322, 1326, 1330, 1334, 1338, 1342, 1794, 1798, 1802, 1806, 1810, 1814, 1818, 1822, 1826, 1830, 1834, 1838, 1842, 1846, 1850, 1854, 1088, 192, 192, 192, 192, 958, 956, 956, 956, 2048, 192, 192, 192, 192, 958, 956, 956, 2048, 2048, 192, 192, 192, 192, 958, 956, 2048, 2048, 2048, 192, 192, 192, 192, 958, 2048, 2048, 2048, 2048, 192, 192, 192, 192, 2048, 2048, 2048, 2048, 2048, 192, 192, 192, 2048, 2048, 2048, 2048, 2048, 2048, 192, 192, 2048, 2048, 2048, 2048, 2048, 2048, 2048, 192, 2048, 2048, 2048, 2048, 2048, 2048, 2048, 2048, 2048, 2048, 2048, 2048, 2048, 2048, 2048, 2048, 2048, 2048, 2048, 2048, 2048, 2048, 2048, 2048, 2048, 2048, 2048, 2048, 2048, 2048, 2048, 2048, 2048, 2048, 2048, 2048, 2048, 2048, 2048, 2048, 2048, 2048, 2048, 2048, 2048, 2048, 2048, 2048, 2048, 2048, 2048, 2048, 2048, 2048, 2048, 2048, 2048, 2048, 2048, 2048, 2048, 2048, 2048, 2048, 702, 700, 700, 700, 954, 952, 952, 952, 956, 702, 700, 700, 700, 954, 952, 952, 956, 956, 702, 700, 700, 700, 954, 952, 956, 956, 956, 702, 700, 700, 700, 954, 958, 956, 956, 956, 702, 700, 700, 700, 192, 958, 956, 956, 956, 702, 700, 700, 192, 192, 958, 956, 956, 956, 702, 700, 192, 192, 192, 958, 956, 956, 956, 702, 192, 192, 192, 192, 958, 956, 956, 956, 2048, 192, 192, 192, 192, 958, 956, 956, 2048, 2048, 192, 192, 192, 192, 958, 956, 2048, 2048, 2048, 192, 192, 192, 192, 958, 2048, 2048, 2048, 2048, 192, 192, 192, 192, 2048, 2048, 2048, 2048, 2048, 192, 192, 192, 2048, 2048, 2048, 2048, 2048, 2048, 192, 192, 2048, 2048, 2048, 2048, 2048, 2048, 2048, 192, 698, 696, 2048, 2048, 2048, 2048, 2048, 2048, 952, 698, 2048, 2048, 2048, 2048, 2048, 2048, 952, 952, 698, 2048, 2048, 2048, 2048, 2048, 952, 952, 952, 698, 2048, 2048, 2048, 2048, 954, 952, 952, 952, 698, 2048, 2048, 2048, 700, 954, 952, 952, 952, 698, 2048, 2048, 700, 700, 954, 952, 952, 952, 698, 2048, 700, 700, 700, 954, 952, 952, 952, 698, 702, 700, 700, 700, 954, 952, 952, 952, 956, 702, 700, 700, 700, 954, 952, 952, 956, 956, 702, 700, 700, 700, 954, 952, 956, 956, 956, 702, 700, 700, 700, 954, 958, 956, 956, 956, 702, 700, 700, 700, 192, 958, 956, 956, 956, 702, 700, 700, 192, 192, 958, 956, 956, 956, 702, 700, 192, 192, 192, 958, 956, 956, 956, 702, 2048, 2048, 2048, 2048, 2048, 2048, 2048, 2048, 2048, 2048, 2048, 2048, 2048, 2048, 2048, 2048, 2048, 2048, 2048, 2048, 2048, 2048, 2048, 2048, 2048, 2048, 2048, 2048, 2048, 2048, 2048, 2048, 2048, 2048, 2048, 2048, 2048, 2048, 2048, 2048, 2048, 2048, 2048, 2048, 2048, 2048, 2048, 2048, 2048, 2048, 2048, 2048, 2048, 2048, 2048, 2048, 2048, 2048, 2048, 2048, 2048, 2048, 2048, 2048, 698, 2048, 2048, 2048, 2048, 2048, 2048, 2048, 952, 698, 2048, 2048, 2048, 2048, 2048, 2048, 952, 952, 698, 2048, 2048, 2048, 2048, 2048, 952, 952, 952, 698, 2048, 2048, 2048, 2048, 954, 952, 952, 952, 698, 2048, 2048, 2048, 700, 954, 952, 952, 952, 698, 2048, 2048, 700, 700, 954, 952, 952, 952, 698, 2048, 700, 700, 700, 954, 952, 952, 952, 698};
__global__ __launch_bounds__(256, 1) void k_block(const void* x, const void* Are, const void* Aim, const void* Bre, const void* Bim, const void* Cre, const void* Cim, const void* logdt,
                                                 const void* Dsk, const void* cvec, const void* wmod, const void* Wg, const void* Wu, const void* Wd, void* ws, void* out) {
    asm volatile(
"s_load_dwordx16 s[68:83], %[ka], 0x0\n"
"s_load_dwordx16 s[84:99], %[ka], 0x40\n"
"s_getpc_b64 s[18:19]\n"
"FPC_%=:\n"
"s_lshr_b32 s37, %[blk], 3\n"
"s_lshl_b32 s37, s37, 3\n"
"v_lshrrev_b32 v2, 6, %[tid]\n"
"v_and_b32 v3, 15, %[tid]\n"
"v_lshl_add_u32 v2, v2, 1, v3\n"
"v_add_u32 v2, s37, v2\n"
"v_and_b32 v2, 255, v2\n"
"v_lshlrev_b32 v2, 7, v2\n"
"global_load_dword v14, v2, s[18:19]\n"
"v_lshrrev_b32 v3, 6, %[tid]\n"
"v_and_b32 v1, 63, %[tid]\n"
"v_readfirstlane_b32 s33, v3\n"
"v_lshlrev_b32 v2, 4, v1\n"
"v_lshrrev_b32 v3, 5, v1\n"
"v_mul_u32_u24 v3, 1152-128, v3\n"
"v_lshl_add_u32 v3, v1, 2, v3\n"
"s_lshl_b32 s37, %[blk], 2\n"
"s_add_u32 s37, s37, s33\n"
"s_mul_i32 s38, s37, 2304\n"
"v_mov_b32 v15, 0\n"
"v_mov_b32 v16, 0\n"
"v_mov_b32 v17, 0\n"
"v_mov_b32 v18, 0\n"
"v_mov_b32 v19, 0\n"
"v_mov_b32 v20, 0\n"
"v_mov_b32 v21, 0\n"
"v_mov_b32 v22, 0\n"
"v_mov_b32 v23, 0\n"
"v_mov_b32 v24, 0\n"
"v_mov_b32 v25, 0\n"
"v_mov_b32 v26, 0\n"
"v_mov_b32 v27, 0\n"
"v_mov_b32 v28, 0\n"
"v_mov_b32 v29, 0\n"
"v_mov_b32 v30, 0\n"
"v_mov_b32 v31, 0\n"
"v_mov_b32 v32, 0\n"
"v_mov_b32 v33, 0\n"
"s_and_b32 s39, %[blk], 7\n"
"s_lshr_b32 s40, %[blk], 3\n"
"s_waitcnt lgkmcnt(0)\n"
"s_add_u32 s20, s68, s38\n"
"s_addc_u32 s21, s69, 0\n"
"global_load_dwordx4 v[4:7], v2, s[20:21]\n"
"global_load_dwordx4 v[8:11], v2, s[20:21] offset:1152\n"
"global_load_dword v12, v3, s[20:21] offset:1024\n"
"global_load_dword v13, v15, s[96:97] sc1\n"
"s_cmp_ge_u32 s40, 24\n"
"s_cbranch_scc1 FAUX_%=\n"
"s_mul_i32 s34, s39, 24\n"
"s_add_u32 s34, s34, s40\n"
"s_mul_i32 s41, s34, 768\n"
"v_and_b32 v2, 127, %[tid]\n"
"v_lshl_add_u32 v2, v2, 1, s41\n"
"s_mul_i32 s42, s34, 6\n"
"s_and_b32 s42, s42, -4\n"
"global_load_short_d16_hi v16, v2, s[70:71]\n"
"global_load_short_d16_hi v17, v2, s[72:73]\n"
"global_load_short_d16_hi v18, v2, s[74:75]\n"
"global_load_short_d16_hi v19, v2, s[76:77]\n"
"global_load_short_d16_hi v20, v2, s[78:79]\n"
"global_load_short_d16_hi v21, v2, s[80:81]\n"
"s_load_dwordx2 s[22:23], s[82:83], s42\n"
"s_load_dwordx2 s[24:25], s[84:85], s42\n"
"global_load_short_d16_hi v22, v2, s[70:71] offset:256\n"
"global_load_short_d16_hi v23, v2, s[72:73] offset:256\n"
"global_load_short_d16_hi v24, v2, s[74:75] offset:256\n"
"global_load_short_d16_hi v25, v2, s[76:77] offset:256\n"
"global_load_short_d16_hi v26, v2, s[78:79] offset:256\n"
"global_load_short_d16_hi v27, v2, s[80:81] offset:256\n"
"global_load_short_d16_hi v28, v2, s[70:71] offset:512\n"
"global_load_short_d16_hi v29, v2, s[72:73] offset:512\n"
"global_load_short_d16_hi v30, v2, s[74:75] offset:512\n"
"global_load_short_d16_hi v31, v2, s[76:77] offset:512\n"
"global_load_short_d16_hi v32, v2, s[78:79] offset:512\n"
"global_load_short_d16_hi v33, v2, s[80:81] offset:512\n"
"v_lshlrev_b32 v236, 4, v1\n"
"global_load_dwordx2 v[62:63], v236, %[kt]\n"
"global_load_dwordx4 v[64:67], v236, %[kt] offset:8\n"
"s_and_b32 s26, s34, 1\n"
"s_lshl_b32 s26, s26, 3\n"
"s_sub_u32 s26, s26, 4\n"
"s_and_b32 s27, s42, 127\n"
"s_cmp_eq_u32 s27, 124\n"
"s_cselect_b32 s26, s26, 0\n"
"s_add_u32 s26, s26, s42\n"
"s_add_u32 s26, s68, s26\n"
"s_addc_u32 s27, s69, 0\n"
"v_mul_u32_u24 v248, 9216, %[tid]\n"
"v_add_u32 v249, 4608, v248\n"
"global_load_dwordx2 v[34:35], v248, s[26:27]\n"
"global_load_dwordx2 v[36:37], v248, s[26:27] offset:1152\n"
"s_cmp_eq_u32 s33, 3\n"
"s_cselect_b32 s44, 512, 0\n"
"s_cselect_b32 s45, 4, 3\n"
"v_lshlrev_b32 v70, s45, v1\n"
"v_add_u32 v70, s44, v70\n"
"global_load_dwordx4 a[204:207], v70, %[kt] offset:1088\n"
"s_mul_i32 s35, s34, 3\n"
"s_and_b32 s36, s34, 1\n"
"s_lshl_b32 s36, s36, 4\n"
"v_and_b32 v70, 15, v1\n"
"v_lshrrev_b32 v71, 4, v1\n"
"s_and_b32 s43, s33, 1\n"
"s_min_u32 s45, s33, 2\n"
"s_mul_i32 s45, s45, 28832\n"
"s_add_u32 s45, s45, 6272\n"
"v_mul_u32_u24 v68, 544, v70\n"
"v_lshl_add_u32 v68, v71, 4, v68\n"
"v_add_u32 v68, s45, v68\n"
"s_lshl_b32 s41, s33, 12\n"
"s_add_u32 s41, s41, 32063488\n"
"v_lshl_add_u32 v243, v1, 4, s41\n"
"v_and_b32 v84, 3, v1\n"
"v_min_u32 v84, 2, v84\n"
"v_mul_u32_u24 v84, 3456, v84\n"
"v_bfe_u32 v85, v1, 2, 2\n"
"v_min_u32 v85, 2, v85\n"
"v_lshrrev_b32 v86, 4, v1\n"
"v_cmp_eq_u32 vcc, 3, v86\n"
"v_add3_u32 v84, v84, v85, s35\n"
"s_nop 0\n"
"v_addc_co_u32 v86, vcc, 0, v86, vcc\n"
"v_mul_u32_u24 v86, 576, v86\n"
"v_add_u32 v84, v84, v86\n"
"v_lshlrev_b32 v84, 3, v84\n"
"v_add_u32 v242, 36372480, v84\n"
"v_lshrrev_b32 v146, 4, v1\n"
"v_bfe_u32 v147, v1, 2, 2\n"
"v_and_b32 v148, 3, v1\n"
"v_and_b32 v149, 15, v1\n"
"v_lshl_add_u32 v147, v146, 3, v147\n"
"v_sub_u32 v147, v148, v147\n"
"v_lshlrev_b32 v147, 3, v147\n"
"v_add_u32 v237, 119488, v147\n"
"v_lshl_add_u32 v147, v146, 1, v149\n"
"v_lshlrev_b32 v147, 3, v147\n"
"v_add_u32 v238, 92776, v147\n"
"v_and_b32 v147, 1, v149\n"
"v_mul_u32_u24 v147, 86392, v147\n"
"v_add_u32 v238, v147, v238\n"
"v_subrev_u32 v238, 86392, v238\n"
"v_lshrrev_b32 v147, 1, v146\n"
"v_and_b32 v148, 1, v146\n"
"v_lshlrev_b32 v150, 11, v147\n"
"v_lshl_add_u32 v150, v149, 4, v150\n"
"v_lshl_add_u32 v150, v148, 3, v150\n"
"v_add_u32 v239, 106976, v150\n"
"v_lshrrev_b32 v150, 3, v149\n"
"v_mul_u32_u24 v150, 147456, v150\n"
"v_and_b32 v151, 7, v149\n"
"v_lshl_add_u32 v151, v151, 1, v147\n"
"v_lshl_add_u32 v150, v151, 4, v150\n"
"s_lshl_b32 s41, s35, 8\n"
"s_add_u32 s41, s41, 2424832\n"
"v_add_u32 v150, s41, v150\n"
"v_lshrrev_b32 v151, 1, v149\n"
"v_mul_u32_u24 v151, 36864, v151\n"
"v_and_b32 v152, 1, v149\n"
"v_lshl_add_u32 v152, v152, 1, v147\n"
"v_lshl_add_u32 v147, v152, 4, v151\n"
"s_lshl_b32 s41, s35, 6\n"
"s_add_u32 s41, s41, 65536\n"
"v_add_u32 v147, s41, v147\n"
"v_mul_u32_u24 v148, 294912, v148\n"
"v_add_u32 v240, v147, v148\n"
"v_add_u32 v244, v150, v148\n"
"s_cmp_eq_u32 s33, 3\n"
"s_cselect_b32 s45, 0, s33\n"
"s_mul_i32 s41, s45, 4224\n"
"v_add_u32 v237, s41, v237\n"
"s_mul_i32 s41, s45, 4736\n"
"v_add_u32 v238, s41, v238\n"
"s_lshl_b32 s41, s45, 12\n"
"v_add_u32 v239, s41, v239\n"
"s_lshl_b32 s41, s45, 6\n"
"v_add_u32 v240, s41, v240\n"
"s_lshl_b32 s41, s45, 8\n"
"v_add_u32 v244, s41, v244\n"
"s_mul_i32 s41, s33, 4224\n"
"s_add_u32 s41, s41, 119296\n"
"s_cmp_eq_u32 s33, 3\n"
"s_cselect_b32 s41, 148448, s41\n"
"v_add_u32 v236, s41, v236\n"
"v_lshrrev_b32 v146, 1, %[tid]\n"
"v_and_b32 v145, 1, %[tid]\n"
"v_lshlrev_b32 v254, 4, v146\n"
"v_lshl_add_u32 v254, v145, 11, v254\n"
"v_add_u32 v254, 106976, v254\n"
"s_cmp_lt_u32 s39, 4\n"
"s_cselect_b32 s60, s90, s92\n"
"s_cselect_b32 s61, s91, s93\n"
"s_and_b32 s44, s39, 3\n"
"s_mul_i32 s45, s44, 2304\n"
"s_mul_i32 s46, s40, 3\n"
"s_mul_i32 s47, s33, 144\n"
"s_add_u32 s46, s46, s47\n"
"s_add_u32 s46, s46, 72\n"
"s_mul_i32 s46, s46, 9216\n"
"s_add_u32 s45, s45, s46\n"
"s_add_u32 s60, s60, s45\n"
"s_addc_u32 s61, s61, 0\n"
"s_sub_u32 s62, s60, 663552\n"
"s_subb_u32 s63, s61, 0\n"
"v_min_u32 v70, 53, v1\n"
"v_mul_u32_u24 v71, 3641, v70\n"
"v_lshrrev_b32 v71, 16, v71\n"
"v_mul_u32_u24 v71, 6912, v71\n"
"v_lshl_add_u32 v250, v70, 7, v71\n"
"s_mul_i32 s44, s40, 43\n"
"s_lshr_b32 s44, s44, 8\n"
"s_mul_i32 s45, s44, 6\n"
"s_sub_u32 s45, s40, s45\n"
"s_and_b32 s46, s39, 3\n"
"s_lshl_b32 s46, s46, 2\n"
"s_add_u32 s44, s44, s46\n"
"s_mul_i32 s45, s45, 96\n"
"s_lshl_b32 s46, s33, 5\n"
"s_add_u32 s45, s45, s46\n"
"s_lshr_b32 s46, s39, 2\n"
"s_lshl_b32 s47, s44, 1\n"
"s_add_u32 s46, s46, s47\n"
"s_mul_i32 s46, s46, 576\n"
"s_add_u32 s46, s46, s45\n"
"s_lshl_b32 s46, s46, 6\n"
"s_add_u32 s46, s46, 34144256\n"
"v_lshl_add_u32 v251, v1, 4, s46\n"
"s_mul_i32 s45, s45, 9216\n"
"s_mul_i32 s46, s44, 576\n"
"s_add_u32 s45, s45, s46\n"
"s_and_b32 s46, s44, 1\n"
"s_xor_b32 s46, s46, 1\n"
"s_lshl_b32 s46, s46, 9\n"
"s_add_u32 s45, s45, s46\n"
"s_cmp_lt_u32 s39, 4\n"
"s_cselect_b32 s66, s90, s92\n"
"s_cselect_b32 s67, s91, s93\n"
"s_add_u32 s66, s66, s45\n"
"s_addc_u32 s67, s67, 0\n"
"v_lshrrev_b32 v70, 2, v1\n"
"v_mul_u32_u24 v70, 9216, v70\n"
"v_and_b32 v71, 3, v1\n"
"v_lshl_add_u32 v252, v71, 4, v70\n"
"s_mul_i32 s41, s34, 128\n"
"s_add_u32 s41, s41, 36586752\n"
"v_mov_b32 v70, s41\n"
"v_mov_b32 v253, 2147483632\n"
"v_cmp_eq_u32 vcc, 0, %[tid]\n"
"s_nop 1\n"
"v_cndmask_b32 v253, v253, v70, vcc\n"
"v_mov_b32 v70, 0\n"
"v_mov_b32 v71, 0\n"
"v_mov_b32 v72, 0\n"
"v_mov_b32 v73, 0\n"
"v_lshrrev_b32 v74, 3, %[tid]\n"
"v_mul_u32_u24 v74, 1184, v74\n"
"v_and_b32 v75, 7, %[tid]\n"
"v_lshl_add_u32 v74, v75, 4, v74\n"
"v_add_u32 v74, 92768, v74\n"
"v_mul_u32_u24 v75, 2064, %[tid]\n"
"v_cmp_gt_u32 vcc, 96, %[tid]\n"
"s_nop 1\n"
"s_mov_b64 exec, vcc\n"
"ds_write_b128 v74, v[70:73]\n"
"s_mov_b64 exec, -1\n"
"v_cmp_gt_u32 vcc, 3, %[tid]\n"
"s_nop 1\n"
"s_mov_b64 exec, vcc\n"
"ds_write_b32 v75, v70 offset:2048\n"
"s_mov_b64 exec, -1\n"
"s_mov_b32 s48, 0x80000080\n"
"s_mov_b32 s49, 0x00800000\n"
"s_and_b32 s58, %[blk], 63\n"
"s_lshl_b32 s58, s58, 4\n"
"s_lshr_b32 s59, %[blk], 6\n"
"s_lshl_b32 s59, s59, 2\n"
"s_add_u32 s59, s59, s33\n"
"s_lshl_b32 s59, s59, 10\n"
"s_add_u32 s58, s58, s59\n"
"s_add_u32 s58, s58, 32047104\n"
"s_mov_b64 s[50:51], 1\n"
"s_mov_b64 s[64:65], 2\n"
"v_mov_b32 v157, 0x05040100\n"
"v_mov_b32 v158, 0x07060302\n"
"v_cndmask_b32 v157, v157, v158, s[48:49]\n"
"v_mov_b32 v140, 0\n"
"v_mov_b32 v141, 0\n"
"v_mov_b32 v142, 0\n"
"v_mov_b32 v143, 0\n"
"s_waitcnt vmcnt(26)\n"
"v_dot2c_f32_bf16 v140, 0x3f803f80, v4\n"
"v_dot2c_f32_bf16 v141, v4, v4\n"
"s_waitcnt vmcnt(25)\n"
"v_dot2c_f32_bf16 v142, 0x3f803f80, v8\n"
"v_dot2c_f32_bf16 v143, v8, v8\n"
"v_dot2c_f32_bf16 v140, 0x3f803f80, v5\n"
"v_dot2c_f32_bf16 v141, v5, v5\n"
"v_dot2c_f32_bf16 v142, 0x3f803f80, v9\n"
"v_dot2c_f32_bf16 v143, v9, v9\n"
"v_dot2c_f32_bf16 v140, 0x3f803f80, v6\n"
"v_dot2c_f32_bf16 v141, v6, v6\n"
"v_dot2c_f32_bf16 v142, 0x3f803f80, v10\n"
"v_dot2c_f32_bf16 v143, v10, v10\n"
"v_dot2c_f32_bf16 v140, 0x3f803f80, v7\n"
"v_dot2c_f32_bf16 v141, v7, v7\n"
"v_dot2c_f32_bf16 v142, 0x3f803f80, v11\n"
"v_dot2c_f32_bf16 v143, v11, v11\n"
"s_nop 1\n"
"v_permlane32_swap_b32 v140, v142\n"
"v_permlane32_swap_b32 v141, v143\n"
"v_add_f32 v140, v140, v142\n"
"v_add_f32 v141, v141, v143\n"
"s_waitcnt vmcnt(24)\n"
"v_dot2c_f32_bf16 v140, 0x3f803f80, v12\n"
"v_dot2c_f32_bf16 v141, v12, v12\n"
"s_waitcnt vmcnt(23)\n"
"v_add_u32 v13, 1, v13\n"
"s_nop 0\n"
"v_permlane16_swap_b32 v140, v141\n"
"v_add_f32 v140, v140, v141\n"
"v_cndmask_b32 v156, v4, v7, s[48:49]\n"
"v_mov_b32 v154, v12\n"
"v_add_f32_dpp v140, v140, v140 row_ror:8 row_mask:0xf bank_mask:0xf bound_ctrl:1\n"
"v_cndmask_b32 v152, v8, v11, s[48:49]\n"
"v_lshl_add_u32 v145, v1, 14, s58\n"
"v_add_f32_dpp v140, v140, v140 row_ror:4 row_mask:0xf bank_mask:0xf bound_ctrl:1\n"
"v_permlane32_swap_b32 v154, v155\n"
"v_cndmask_b32 v156, v156, v12, s[50:51]\n"
"v_add_f32_dpp v140, v140, v140 row_ror:2 row_mask:0xf bank_mask:0xf bound_ctrl:1\n"
"s_nop 0\n"
"v_cndmask_b32 v152, v152, v155, s[50:51]\n"
"v_add_f32_dpp v140, v140, v140 row_ror:1 row_mask:0xf bank_mask:0xf bound_ctrl:1\n"
"v_perm_b32 v152, v152, v156, v157\n"
"v_mul_f32 v140, 0x3ae38e39, v140\n"
"v_mov_b32 v141, v140\n"
"s_nop 1\n"
"v_permlane16_swap_b32 v140, v141\n"
"v_fma_f32 v141, -v140, v140, v141\n"
"v_max_f32 v141, 0, v141\n"
"v_cvt_pk_bf16_f32 v141, v15, v141\n"
"v_add_f32 v141, 0x3727c5ac, v141\n"
"v_cvt_pk_bf16_f32 v141, v15, v141\n"
"v_sqrt_f32 v141, v141\n"
"s_nop 0\n"
"v_cvt_pk_bf16_f32 v150, v140, v141\n"
"v_mov_b32 v151, v150\n"
"s_nop 1\n"
"v_permlane32_swap_b32 v150, v151\n"
"v_bitop3_b32 v153, v150, v151, v13 bitop3:0x96\n"
"s_mov_b32 exec_lo, 0x80010083\n"
"s_mov_b32 exec_hi, 0x00800100\n"
"global_store_dwordx4 v145, v[150:153], s[96:97] sc1\n"
"s_mov_b64 exec, -1\n"
"s_waitcnt lgkmcnt(0)\n"
"s_lshr_b64 s[22:23], s[22:23], s36\n"
"s_lshr_b64 s[24:25], s[24:25], s36\n"
"s_lshl_b32 s28, s22, 16\n"
"s_and_b32 s29, s22, 0xffff0000\n"
"s_lshl_b32 s30, s23, 16\n"
"s_lshl_b32 s22, s24, 16\n"
"s_and_b32 s23, s24, 0xffff0000\n"
"s_lshl_b32 s24, s25, 16\n"
"s_lshl_b32 s41, s43, 8\n"
"s_add_u32 m0, s41, 6272\n"
"v_and_b32 v70, 127, %[tid]\n"
"v_lshlrev_b32 v70, 2, v70\n"
"v_add_u32 v70, 63936, v70\n"
"v_cmp_eq_u32 vcc, 0, v1\n"
"s_cmp_eq_u32 s33, 1\n"
"s_cselect_b32 s22, s23, s22\n"
"s_cmp_eq_u32 s33, 2\n"
"s_cselect_b32 s22, s24, s22\n"
"v_mov_b32 v71, s22\n"
"v_cndmask_b32 v61, v15, v71, vcc\n"
"s_cmp_ge_u32 s33, 2\n"
"s_cbranch_scc1 FR1_%=\n"
"global_load_dwordx2 v[38:39], v248, s[26:27] offset:2304\n"
"v_mov_b32 v120, 0x3fb8aa3b\n"
"v_mov_b32 v174, 0x3fb8aa3b\n"
"v_mov_b32 v228, 0x3fb8aa3b\n"
"v_mul_f32 v120, s28, v120\n"
"v_mul_f32 v174, s29, v174\n"
"v_mul_f32 v228, s30, v228\n"
"v_exp_f32 v120, v120\n"
"v_exp_f32 v174, v174\n"
"v_exp_f32 v228, v228\n"
"v_cvt_pk_bf16_f32 v120, v15, v120\n"
"v_cvt_pk_bf16_f32 v174, v15, v174\n"
"v_cvt_pk_bf16_f32 v228, v15, v228\n"
"v_mul_f32 v122, 0x3fb8aa3b, v120\n"
"v_mul_f32 v176, 0x3fb8aa3b, v174\n"
"v_mul_f32 v230, 0x3fb8aa3b, v228\n"
"v_mul_f32 v123, 0x3e22f983, v120\n"
"v_mul_f32 v177, 0x3e22f983, v174\n"
"v_mul_f32 v231, 0x3e22f983, v228\n"
"s_waitcnt vmcnt(23)\n"
"v_pk_mul_f32 v[122:123], v[122:123], v[16:17]\n"
"s_waitcnt vmcnt(17)\n"
"v_pk_mul_f32 v[176:177], v[176:177], v[22:23]\n"
"s_waitcnt vmcnt(11)\n"
"v_pk_mul_f32 v[230:231], v[230:231], v[28:29]\n"
"v_mul_f32 v112, 0x41000000, v122\n"
"v_mul_f32 v166, 0x41000000, v176\n"
"v_mul_f32 v220, 0x41000000, v230\n"
"v_mul_f32 v113, 0x41000000, v123\n"
"v_mul_f32 v167, 0x41000000, v177\n"
"v_mul_f32 v221, 0x41000000, v231\n"
"v_exp_f32 v112, v112\n"
"v_exp_f32 v166, v166\n"
"v_exp_f32 v220, v220\n"
"v_fract_f32 v113, v113\n"
"v_fract_f32 v167, v167\n"
"v_fract_f32 v221, v221\n"
"v_cos_f32 v72, v113\n"
"v_cos_f32 v126, v167\n"
"v_cos_f32 v180, v221\n"
"v_sin_f32 v73, v113\n"
"v_sin_f32 v127, v167\n"
"v_sin_f32 v181, v221\n"
"v_pk_mul_f32 v[72:73], v[72:73], v[112:113] op_sel_hi:[1,0]\n"
"v_pk_mul_f32 v[126:127], v[126:127], v[166:167] op_sel_hi:[1,0]\n"
"v_pk_mul_f32 v[180:181], v[180:181], v[220:221] op_sel_hi:[1,0]\n"
"v_mov_b32 v116, 0x3f80\n"
"v_mov_b32 v170, 0x3f80\n"
"v_mov_b32 v224, 0x3f80\n"
"ds_write_addtid_b32 v116 offset:11424\n"
"ds_write_addtid_b32 v170 offset:40256\n"
"ds_write_b32 v70, v224 offset:11424\n"
"v_cvt_pk_bf16_f32 v117, v72, v73\n"
"v_cvt_pk_bf16_f32 v171, v126, v127\n"
"v_cvt_pk_bf16_f32 v225, v180, v181\n"
"ds_write_addtid_b32 v117 offset:11968\n"
"ds_write_addtid_b32 v171 offset:40800\n"
"ds_write_b32 v70, v225 offset:11968\n"
"v_pk_mul_f32 v[112:113], v[72:73], v[72:73] op_sel:[0,0] op_sel_hi:[0,1]\n"
"v_pk_mul_f32 v[166:167], v[126:127], v[126:127] op_sel:[0,0] op_sel_hi:[0,1]\n"
"v_pk_mul_f32 v[220:221], v[180:181], v[180:181] op_sel:[0,0] op_sel_hi:[0,1]\n"
"v_pk_fma_f32 v[74:75], v[72:73], v[72:73], v[112:113] op_sel:[1,1,0] op_sel_hi:[1,0,1] neg_lo:[0,1,0]\n"
"v_pk_fma_f32 v[128:129], v[126:127], v[126:127], v[166:167] op_sel:[1,1,0] op_sel_hi:[1,0,1] neg_lo:[0,1,0]\n"
"v_pk_fma_f32 v[182:183], v[180:181], v[180:181], v[220:221] op_sel:[1,1,0] op_sel_hi:[1,0,1] neg_lo:[0,1,0]\n"
"v_cvt_pk_bf16_f32 v118, v74, v75\n"
"v_cvt_pk_bf16_f32 v172, v128, v129\n"
"v_cvt_pk_bf16_f32 v226, v182, v183\n"
"ds_write_addtid_b32 v118 offset:12512\n"
"ds_write_addtid_b32 v172 offset:41344\n"
"ds_write_b32 v70, v226 offset:12512\n"
"v_pk_mul_f32 v[114:115], v[72:73], v[74:75] op_sel:[0,0] op_sel_hi:[0,1]\n"
"v_pk_mul_f32 v[168:169], v[126:127], v[128:129] op_sel:[0,0] op_sel_hi:[0,1]\n"
"v_pk_mul_f32 v[222:223], v[180:181], v[182:183] op_sel:[0,0] op_sel_hi:[0,1]\n"
"v_pk_fma_f32 v[76:77], v[72:73], v[74:75], v[114:115] op_sel:[1,1,0] op_sel_hi:[1,0,1] neg_lo:[0,1,0]\n"
"v_pk_fma_f32 v[130:131], v[126:127], v[128:129], v[168:169] op_sel:[1,1,0] op_sel_hi:[1,0,1] neg_lo:[0,1,0]\n"
"v_pk_fma_f32 v[184:185], v[180:181], v[182:183], v[222:223] op_sel:[1,1,0] op_sel_hi:[1,0,1] neg_lo:[0,1,0]\n"
"v_cvt_pk_bf16_f32 v119, v76, v77\n"
"v_cvt_pk_bf16_f32 v173, v130, v131\n"
"v_cvt_pk_bf16_f32 v227, v184, v185\n"
"global_load_dwordx2 v[40:41], v248, s[26:27] offset:3456\n"
"ds_write_addtid_b32 v119 offset:13056\n"
"ds_write_addtid_b32 v173 offset:41888\n"
"ds_write_b32 v70, v227 offset:13056\n"
"v_pk_mul_f32 v[112:113], v[74:75], v[74:75] op_sel:[0,0] op_sel_hi:[0,1]\n"
"v_pk_mul_f32 v[166:167], v[128:129], v[128:129] op_sel:[0,0] op_sel_hi:[0,1]\n"
"v_pk_mul_f32 v[220:221], v[182:183], v[182:183] op_sel:[0,0] op_sel_hi:[0,1]\n"
"v_pk_fma_f32 v[78:79], v[74:75], v[74:75], v[112:113] op_sel:[1,1,0] op_sel_hi:[1,0,1] neg_lo:[0,1,0]\n"
"v_pk_fma_f32 v[132:133], v[128:129], v[128:129], v[166:167] op_sel:[1,1,0] op_sel_hi:[1,0,1] neg_lo:[0,1,0]\n"
"v_pk_fma_f32 v[186:187], v[182:183], v[182:183], v[220:221] op_sel:[1,1,0] op_sel_hi:[1,0,1] neg_lo:[0,1,0]\n"
"v_cvt_pk_bf16_f32 v116, v78, v79\n"
"v_cvt_pk_bf16_f32 v170, v132, v133\n"
"v_cvt_pk_bf16_f32 v224, v186, v187\n"
"ds_write_addtid_b32 v116 offset:13600\n"
"ds_write_addtid_b32 v170 offset:42432\n"
"ds_write_b32 v70, v224 offset:13600\n"
"v_pk_mul_f32 v[114:115], v[74:75], v[76:77] op_sel:[0,0] op_sel_hi:[0,1]\n"
"v_pk_mul_f32 v[168:169], v[128:129], v[130:131] op_sel:[0,0] op_sel_hi:[0,1]\n"
"v_pk_mul_f32 v[222:223], v[182:183], v[184:185] op_sel:[0,0] op_sel_hi:[0,1]\n"
"v_pk_fma_f32 v[80:81], v[74:75], v[76:77], v[114:115] op_sel:[1,1,0] op_sel_hi:[1,0,1] neg_lo:[0,1,0]\n"
"v_pk_fma_f32 v[134:135], v[128:129], v[130:131], v[168:169] op_sel:[1,1,0] op_sel_hi:[1,0,1] neg_lo:[0,1,0]\n"
"v_pk_fma_f32 v[188:189], v[182:183], v[184:185], v[222:223] op_sel:[1,1,0] op_sel_hi:[1,0,1] neg_lo:[0,1,0]\n"
"v_cvt_pk_bf16_f32 v117, v80, v81\n"
"v_cvt_pk_bf16_f32 v171, v134, v135\n"
"v_cvt_pk_bf16_f32 v225, v188, v189\n"
"ds_write_addtid_b32 v117 offset:14144\n"
"ds_write_addtid_b32 v171 offset:42976\n"
"ds_write_b32 v70, v225 offset:14144\n"
"v_pk_mul_f32 v[112:113], v[76:77], v[76:77] op_sel:[0,0] op_sel_hi:[0,1]\n"
"v_pk_mul_f32 v[166:167], v[130:131], v[130:131] op_sel:[0,0] op_sel_hi:[0,1]\n"
"v_pk_mul_f32 v[220:221], v[184:185], v[184:185] op_sel:[0,0] op_sel_hi:[0,1]\n"
"v_pk_fma_f32 v[82:83], v[76:77], v[76:77], v[112:113] op_sel:[1,1,0] op_sel_hi:[1,0,1] neg_lo:[0,1,0]\n"
"v_pk_fma_f32 v[136:137], v[130:131], v[130:131], v[166:167] op_sel:[1,1,0] op_sel_hi:[1,0,1] neg_lo:[0,1,0]\n"
"v_pk_fma_f32 v[190:191], v[184:185], v[184:185], v[220:221] op_sel:[1,1,0] op_sel_hi:[1,0,1] neg_lo:[0,1,0]\n"
"v_cvt_pk_bf16_f32 v118, v82, v83\n"
"v_cvt_pk_bf16_f32 v172, v136, v137\n"
"v_cvt_pk_bf16_f32 v226, v190, v191\n"
"ds_write_addtid_b32 v118 offset:14688\n"
"ds_write_addtid_b32 v172 offset:43520\n"
"ds_write_b32 v70, v226 offset:14688\n"
"v_pk_mul_f32 v[114:115], v[76:77], v[78:79] op_sel:[0,0] op_sel_hi:[0,1]\n"
"v_pk_mul_f32 v[168:169], v[130:131], v[132:133] op_sel:[0,0] op_sel_hi:[0,1]\n"
"v_pk_mul_f32 v[222:223], v[184:185], v[186:187] op_sel:[0,0] op_sel_hi:[0,1]\n"
"v_pk_fma_f32 v[84:85], v[76:77], v[78:79], v[114:115] op_sel:[1,1,0] op_sel_hi:[1,0,1] neg_lo:[0,1,0]\n"
"v_pk_fma_f32 v[138:139], v[130:131], v[132:133], v[168:169] op_sel:[1,1,0] op_sel_hi:[1,0,1] neg_lo:[0,1,0]\n"
"v_pk_fma_f32 v[192:193], v[184:185], v[186:187], v[222:223] op_sel:[1,1,0] op_sel_hi:[1,0,1] neg_lo:[0,1,0]\n"
"v_cvt_pk_bf16_f32 v119, v84, v85\n"
"v_cvt_pk_bf16_f32 v173, v138, v139\n"
"v_cvt_pk_bf16_f32 v227, v192, v193\n"
"ds_write_addtid_b32 v119 offset:15232\n"
"ds_write_addtid_b32 v173 offset:44064\n"
"ds_write_b32 v70, v227 offset:15232\n"
"v_pk_mul_f32 v[112:113], v[78:79], v[78:79] op_sel:[0,0] op_sel_hi:[0,1]\n"
"v_pk_mul_f32 v[166:167], v[132:133], v[132:133] op_sel:[0,0] op_sel_hi:[0,1]\n"
"v_pk_mul_f32 v[220:221], v[186:187], v[186:187] op_sel:[0,0] op_sel_hi:[0,1]\n"
"v_pk_fma_f32 v[86:87], v[78:79], v[78:79], v[112:113] op_sel:[1,1,0] op_sel_hi:[1,0,1] neg_lo:[0,1,0]\n"
"v_pk_fma_f32 v[140:141], v[132:133], v[132:133], v[166:167] op_sel:[1,1,0] op_sel_hi:[1,0,1] neg_lo:[0,1,0]\n"
"v_pk_fma_f32 v[194:195], v[186:187], v[186:187], v[220:221] op_sel:[1,1,0] op_sel_hi:[1,0,1] neg_lo:[0,1,0]\n"
"v_cvt_pk_bf16_f32 v116, v86, v87\n"
"v_cvt_pk_bf16_f32 v170, v140, v141\n"
"v_cvt_pk_bf16_f32 v224, v194, v195\n"
"ds_write_addtid_b32 v116 offset:15776\n"
"ds_write_addtid_b32 v170 offset:44608\n"
"ds_write_b32 v70, v224 offset:15776\n"
"v_pk_mul_f32 v[114:115], v[78:79], v[80:81] op_sel:[0,0] op_sel_hi:[0,1]\n"
"v_pk_mul_f32 v[168:169], v[132:133], v[134:135] op_sel:[0,0] op_sel_hi:[0,1]\n"
"v_pk_mul_f32 v[222:223], v[186:187], v[188:189] op_sel:[0,0] op_sel_hi:[0,1]\n"
"v_pk_fma_f32 v[88:89], v[78:79], v[80:81], v[114:115] op_sel:[1,1,0] op_sel_hi:[1,0,1] neg_lo:[0,1,0]\n"
"v_pk_fma_f32 v[142:143], v[132:133], v[134:135], v[168:169] op_sel:[1,1,0] op_sel_hi:[1,0,1] neg_lo:[0,1,0]\n"
"v_pk_fma_f32 v[196:197], v[186:187], v[188:189], v[222:223] op_sel:[1,1,0] op_sel_hi:[1,0,1] neg_lo:[0,1,0]\n"
"v_cvt_pk_bf16_f32 v117, v88, v89\n"
"v_cvt_pk_bf16_f32 v171, v142, v143\n"
"v_cvt_pk_bf16_f32 v225, v196, v197\n"
"ds_write_addtid_b32 v117 offset:16320\n"
"ds_write_addtid_b32 v171 offset:45152\n"
"ds_write_b32 v70, v225 offset:16320\n"
"v_pk_mul_f32 v[112:113], v[80:81], v[80:81] op_sel:[0,0] op_sel_hi:[0,1]\n"
"global_load_dwordx2 v[42:43], v249, s[26:27]\n"
"v_pk_mul_f32 v[166:167], v[134:135], v[134:135] op_sel:[0,0] op_sel_hi:[0,1]\n"
"v_pk_mul_f32 v[220:221], v[188:189], v[188:189] op_sel:[0,0] op_sel_hi:[0,1]\n"
"v_pk_fma_f32 v[90:91], v[80:81], v[80:81], v[112:113] op_sel:[1,1,0] op_sel_hi:[1,0,1] neg_lo:[0,1,0]\n"
"v_pk_fma_f32 v[144:145], v[134:135], v[134:135], v[166:167] op_sel:[1,1,0] op_sel_hi:[1,0,1] neg_lo:[0,1,0]\n"
"v_pk_fma_f32 v[198:199], v[188:189], v[188:189], v[220:221] op_sel:[1,1,0] op_sel_hi:[1,0,1] neg_lo:[0,1,0]\n"
"v_cvt_pk_bf16_f32 v118, v90, v91\n"
"v_cvt_pk_bf16_f32 v172, v144, v145\n"
"v_cvt_pk_bf16_f32 v226, v198, v199\n"
"ds_write_addtid_b32 v118 offset:16864\n"
"ds_write_addtid_b32 v172 offset:45696\n"
"ds_write_b32 v70, v226 offset:16864\n"
"v_pk_mul_f32 v[114:115], v[80:81], v[82:83] op_sel:[0,0] op_sel_hi:[0,1]\n"
"v_pk_mul_f32 v[168:169], v[134:135], v[136:137] op_sel:[0,0] op_sel_hi:[0,1]\n"
"v_pk_mul_f32 v[222:223], v[188:189], v[190:191] op_sel:[0,0] op_sel_hi:[0,1]\n"
"v_pk_fma_f32 v[92:93], v[80:81], v[82:83], v[114:115] op_sel:[1,1,0] op_sel_hi:[1,0,1] neg_lo:[0,1,0]\n"
"v_pk_fma_f32 v[146:147], v[134:135], v[136:137], v[168:169] op_sel:[1,1,0] op_sel_hi:[1,0,1] neg_lo:[0,1,0]\n"
"v_pk_fma_f32 v[200:201], v[188:189], v[190:191], v[222:223] op_sel:[1,1,0] op_sel_hi:[1,0,1] neg_lo:[0,1,0]\n"
"v_cvt_pk_bf16_f32 v119, v92, v93\n"
"v_cvt_pk_bf16_f32 v173, v146, v147\n"
"v_cvt_pk_bf16_f32 v227, v200, v201\n"
"ds_write_addtid_b32 v119 offset:17408\n"
"ds_write_addtid_b32 v173 offset:46240\n"
"ds_write_b32 v70, v227 offset:17408\n"
"v_pk_mul_f32 v[112:113], v[82:83], v[82:83] op_sel:[0,0] op_sel_hi:[0,1]\n"
"v_pk_mul_f32 v[166:167], v[136:137], v[136:137] op_sel:[0,0] op_sel_hi:[0,1]\n"
"v_pk_mul_f32 v[220:221], v[190:191], v[190:191] op_sel:[0,0] op_sel_hi:[0,1]\n"
"v_pk_fma_f32 v[94:95], v[82:83], v[82:83], v[112:113] op_sel:[1,1,0] op_sel_hi:[1,0,1] neg_lo:[0,1,0]\n"
"v_pk_fma_f32 v[148:149], v[136:137], v[136:137], v[166:167] op_sel:[1,1,0] op_sel_hi:[1,0,1] neg_lo:[0,1,0]\n"
"v_pk_fma_f32 v[202:203], v[190:191], v[190:191], v[220:221] op_sel:[1,1,0] op_sel_hi:[1,0,1] neg_lo:[0,1,0]\n"
"v_cvt_pk_bf16_f32 v116, v94, v95\n"
"v_cvt_pk_bf16_f32 v170, v148, v149\n"
"v_cvt_pk_bf16_f32 v224, v202, v203\n"
"ds_write_addtid_b32 v116 offset:17952\n"
"ds_write_addtid_b32 v170 offset:46784\n"
"ds_write_b32 v70, v224 offset:17952\n"
"v_pk_mul_f32 v[114:115], v[82:83], v[84:85] op_sel:[0,0] op_sel_hi:[0,1]\n"
"v_pk_mul_f32 v[168:169], v[136:137], v[138:139] op_sel:[0,0] op_sel_hi:[0,1]\n"
"v_pk_mul_f32 v[222:223], v[190:191], v[192:193] op_sel:[0,0] op_sel_hi:[0,1]\n"
"v_pk_fma_f32 v[96:97], v[82:83], v[84:85], v[114:115] op_sel:[1,1,0] op_sel_hi:[1,0,1] neg_lo:[0,1,0]\n"
"v_pk_fma_f32 v[150:151], v[136:137], v[138:139], v[168:169] op_sel:[1,1,0] op_sel_hi:[1,0,1] neg_lo:[0,1,0]\n"
"v_pk_fma_f32 v[204:205], v[190:191], v[192:193], v[222:223] op_sel:[1,1,0] op_sel_hi:[1,0,1] neg_lo:[0,1,0]\n"
"v_cvt_pk_bf16_f32 v117, v96, v97\n"
"v_cvt_pk_bf16_f32 v171, v150, v151\n"
"v_cvt_pk_bf16_f32 v225, v204, v205\n"
"ds_write_addtid_b32 v117 offset:18496\n"
"ds_write_addtid_b32 v171 offset:47328\n"
"ds_write_b32 v70, v225 offset:18496\n"
"v_pk_mul_f32 v[112:113], v[84:85], v[84:85] op_sel:[0,0] op_sel_hi:[0,1]\n"
"v_pk_mul_f32 v[166:167], v[138:139], v[138:139] op_sel:[0,0] op_sel_hi:[0,1]\n"
"v_pk_mul_f32 v[220:221], v[192:193], v[192:193] op_sel:[0,0] op_sel_hi:[0,1]\n"
"v_pk_fma_f32 v[98:99], v[84:85], v[84:85], v[112:113] op_sel:[1,1,0] op_sel_hi:[1,0,1] neg_lo:[0,1,0]\n"
"v_pk_fma_f32 v[152:153], v[138:139], v[138:139], v[166:167] op_sel:[1,1,0] op_sel_hi:[1,0,1] neg_lo:[0,1,0]\n"
"v_pk_fma_f32 v[206:207], v[192:193], v[192:193], v[220:221] op_sel:[1,1,0] op_sel_hi:[1,0,1] neg_lo:[0,1,0]\n"
"v_cvt_pk_bf16_f32 v118, v98, v99\n"
"v_cvt_pk_bf16_f32 v172, v152, v153\n"
"v_cvt_pk_bf16_f32 v226, v206, v207\n"
"ds_write_addtid_b32 v118 offset:19040\n"
"ds_write_addtid_b32 v172 offset:47872\n"
"ds_write_b32 v70, v226 offset:19040\n"
"v_pk_mul_f32 v[114:115], v[84:85], v[86:87] op_sel:[0,0] op_sel_hi:[0,1]\n"
"v_pk_mul_f32 v[168:169], v[138:139], v[140:141] op_sel:[0,0] op_sel_hi:[0,1]\n"
"v_pk_mul_f32 v[222:223], v[192:193], v[194:195] op_sel:[0,0] op_sel_hi:[0,1]\n"
"v_pk_fma_f32 v[108:109], v[84:85], v[86:87], v[114:115] op_sel:[1,1,0] op_sel_hi:[1,0,1] neg_lo:[0,1,0]\n"
"v_pk_fma_f32 v[162:163], v[138:139], v[140:141], v[168:169] op_sel:[1,1,0] op_sel_hi:[1,0,1] neg_lo:[0,1,0]\n"
"v_pk_fma_f32 v[216:217], v[192:193], v[194:195], v[222:223] op_sel:[1,1,0] op_sel_hi:[1,0,1] neg_lo:[0,1,0]\n"
"v_cvt_pk_bf16_f32 v119, v108, v109\n"
"v_cvt_pk_bf16_f32 v173, v162, v163\n"
"v_cvt_pk_bf16_f32 v227, v216, v217\n"
"ds_write_addtid_b32 v119 offset:19584\n"
"ds_write_addtid_b32 v173 offset:48416\n"
"ds_write_b32 v70, v227 offset:19584\n"
"v_pk_mul_f32 v[112:113], v[86:87], v[86:87] op_sel:[0,0] op_sel_hi:[0,1]\n"
"v_pk_mul_f32 v[166:167], v[140:141], v[140:141] op_sel:[0,0] op_sel_hi:[0,1]\n"
"v_pk_mul_f32 v[220:221], v[194:195], v[194:195] op_sel:[0,0] op_sel_hi:[0,1]\n"
"v_pk_fma_f32 v[102:103], v[86:87], v[86:87], v[112:113] op_sel:[1,1,0] op_sel_hi:[1,0,1] neg_lo:[0,1,0]\n"
"global_load_dwordx2 v[44:45], v249, s[26:27] offset:1152\n"
"v_pk_fma_f32 v[156:157], v[140:141], v[140:141], v[166:167] op_sel:[1,1,0] op_sel_hi:[1,0,1] neg_lo:[0,1,0]\n"
"v_pk_fma_f32 v[210:211], v[194:195], v[194:195], v[220:221] op_sel:[1,1,0] op_sel_hi:[1,0,1] neg_lo:[0,1,0]\n"
"v_cvt_pk_bf16_f32 v116, v102, v103\n"
"v_cvt_pk_bf16_f32 v170, v156, v157\n"
"v_cvt_pk_bf16_f32 v224, v210, v211\n"
"ds_write_addtid_b32 v116 offset:20128\n"
"ds_write_addtid_b32 v170 offset:48960\n"
"ds_write_b32 v70, v224 offset:20128\n"
"v_pk_mul_f32 v[114:115], v[86:87], v[88:89] op_sel:[0,0] op_sel_hi:[0,1]\n"
"v_pk_mul_f32 v[168:169], v[140:141], v[142:143] op_sel:[0,0] op_sel_hi:[0,1]\n"
"v_pk_mul_f32 v[222:223], v[194:195], v[196:197] op_sel:[0,0] op_sel_hi:[0,1]\n"
"v_pk_fma_f32 v[104:105], v[86:87], v[88:89], v[114:115] op_sel:[1,1,0] op_sel_hi:[1,0,1] neg_lo:[0,1,0]\n"
"v_pk_fma_f32 v[158:159], v[140:141], v[142:143], v[168:169] op_sel:[1,1,0] op_sel_hi:[1,0,1] neg_lo:[0,1,0]\n"
"v_pk_fma_f32 v[212:213], v[194:195], v[196:197], v[222:223] op_sel:[1,1,0] op_sel_hi:[1,0,1] neg_lo:[0,1,0]\n"
"v_cvt_pk_bf16_f32 v117, v104, v105\n"
"v_cvt_pk_bf16_f32 v171, v158, v159\n"
"v_cvt_pk_bf16_f32 v225, v212, v213\n"
"ds_write_addtid_b32 v117 offset:20672\n"
"ds_write_addtid_b32 v171 offset:49504\n"
"ds_write_b32 v70, v225 offset:20672\n"
"v_pk_mul_f32 v[112:113], v[88:89], v[88:89] op_sel:[0,0] op_sel_hi:[0,1]\n"
"v_pk_mul_f32 v[166:167], v[142:143], v[142:143] op_sel:[0,0] op_sel_hi:[0,1]\n"
"v_pk_mul_f32 v[220:221], v[196:197], v[196:197] op_sel:[0,0] op_sel_hi:[0,1]\n"
"v_pk_fma_f32 v[106:107], v[88:89], v[88:89], v[112:113] op_sel:[1,1,0] op_sel_hi:[1,0,1] neg_lo:[0,1,0]\n"
"v_pk_fma_f32 v[160:161], v[142:143], v[142:143], v[166:167] op_sel:[1,1,0] op_sel_hi:[1,0,1] neg_lo:[0,1,0]\n"
"v_pk_fma_f32 v[214:215], v[196:197], v[196:197], v[220:221] op_sel:[1,1,0] op_sel_hi:[1,0,1] neg_lo:[0,1,0]\n"
"v_cvt_pk_bf16_f32 v118, v106, v107\n"
"v_cvt_pk_bf16_f32 v172, v160, v161\n"
"v_cvt_pk_bf16_f32 v226, v214, v215\n"
"ds_write_addtid_b32 v118 offset:21216\n"
"ds_write_addtid_b32 v172 offset:50048\n"
"ds_write_b32 v70, v226 offset:21216\n"
"v_pk_mul_f32 v[114:115], v[88:89], v[90:91] op_sel:[0,0] op_sel_hi:[0,1]\n"
"v_pk_mul_f32 v[168:169], v[142:143], v[144:145] op_sel:[0,0] op_sel_hi:[0,1]\n"
"v_pk_mul_f32 v[222:223], v[196:197], v[198:199] op_sel:[0,0] op_sel_hi:[0,1]\n"
"v_pk_fma_f32 v[108:109], v[88:89], v[90:91], v[114:115] op_sel:[1,1,0] op_sel_hi:[1,0,1] neg_lo:[0,1,0]\n"
"v_pk_fma_f32 v[162:163], v[142:143], v[144:145], v[168:169] op_sel:[1,1,0] op_sel_hi:[1,0,1] neg_lo:[0,1,0]\n"
"v_pk_fma_f32 v[216:217], v[196:197], v[198:199], v[222:223] op_sel:[1,1,0] op_sel_hi:[1,0,1] neg_lo:[0,1,0]\n"
"v_cvt_pk_bf16_f32 v119, v108, v109\n"
"v_cvt_pk_bf16_f32 v173, v162, v163\n"
"v_cvt_pk_bf16_f32 v227, v216, v217\n"
"ds_write_addtid_b32 v119 offset:21760\n"
"ds_write_addtid_b32 v173 offset:50592\n"
"ds_write_b32 v70, v227 offset:21760\n"
"v_pk_mul_f32 v[112:113], v[90:91], v[90:91] op_sel:[0,0] op_sel_hi:[0,1]\n"
"v_pk_mul_f32 v[166:167], v[144:145], v[144:145] op_sel:[0,0] op_sel_hi:[0,1]\n"
"v_pk_mul_f32 v[220:221], v[198:199], v[198:199] op_sel:[0,0] op_sel_hi:[0,1]\n"
"v_pk_fma_f32 v[102:103], v[90:91], v[90:91], v[112:113] op_sel:[1,1,0] op_sel_hi:[1,0,1] neg_lo:[0,1,0]\n"
"v_pk_fma_f32 v[156:157], v[144:145], v[144:145], v[166:167] op_sel:[1,1,0] op_sel_hi:[1,0,1] neg_lo:[0,1,0]\n"
"v_pk_fma_f32 v[210:211], v[198:199], v[198:199], v[220:221] op_sel:[1,1,0] op_sel_hi:[1,0,1] neg_lo:[0,1,0]\n"
"v_cvt_pk_bf16_f32 v116, v102, v103\n"
"v_cvt_pk_bf16_f32 v170, v156, v157\n"
"v_cvt_pk_bf16_f32 v224, v210, v211\n"
"ds_write_addtid_b32 v116 offset:22304\n"
"ds_write_addtid_b32 v170 offset:51136\n"
"ds_write_b32 v70, v224 offset:22304\n"
"v_pk_mul_f32 v[114:115], v[90:91], v[92:93] op_sel:[0,0] op_sel_hi:[0,1]\n"
"v_pk_mul_f32 v[168:169], v[144:145], v[146:147] op_sel:[0,0] op_sel_hi:[0,1]\n"
"v_pk_mul_f32 v[222:223], v[198:199], v[200:201] op_sel:[0,0] op_sel_hi:[0,1]\n"
"v_pk_fma_f32 v[104:105], v[90:91], v[92:93], v[114:115] op_sel:[1,1,0] op_sel_hi:[1,0,1] neg_lo:[0,1,0]\n"
"v_pk_fma_f32 v[158:159], v[144:145], v[146:147], v[168:169] op_sel:[1,1,0] op_sel_hi:[1,0,1] neg_lo:[0,1,0]\n"
"v_pk_fma_f32 v[212:213], v[198:199], v[200:201], v[222:223] op_sel:[1,1,0] op_sel_hi:[1,0,1] neg_lo:[0,1,0]\n"
"v_cvt_pk_bf16_f32 v117, v104, v105\n"
"v_cvt_pk_bf16_f32 v171, v158, v159\n"
"v_cvt_pk_bf16_f32 v225, v212, v213\n"
"ds_write_addtid_b32 v117 offset:22848\n"
"ds_write_addtid_b32 v171 offset:51680\n"
"ds_write_b32 v70, v225 offset:22848\n"
"v_pk_mul_f32 v[112:113], v[92:93], v[92:93] op_sel:[0,0] op_sel_hi:[0,1]\n"
"v_pk_mul_f32 v[166:167], v[146:147], v[146:147] op_sel:[0,0] op_sel_hi:[0,1]\n"
"v_pk_mul_f32 v[220:221], v[200:201], v[200:201] op_sel:[0,0] op_sel_hi:[0,1]\n"
"v_pk_fma_f32 v[106:107], v[92:93], v[92:93], v[112:113] op_sel:[1,1,0] op_sel_hi:[1,0,1] neg_lo:[0,1,0]\n"
"v_pk_fma_f32 v[160:161], v[146:147], v[146:147], v[166:167] op_sel:[1,1,0] op_sel_hi:[1,0,1] neg_lo:[0,1,0]\n"
"v_pk_fma_f32 v[214:215], v[200:201], v[200:201], v[220:221] op_sel:[1,1,0] op_sel_hi:[1,0,1] neg_lo:[0,1,0]\n"
"v_cvt_pk_bf16_f32 v118, v106, v107\n"
"v_cvt_pk_bf16_f32 v172, v160, v161\n"
"global_load_dwordx2 v[46:47], v249, s[26:27] offset:2304\n"
"v_cvt_pk_bf16_f32 v226, v214, v215\n"
"ds_write_addtid_b32 v118 offset:23392\n"
"ds_write_addtid_b32 v172 offset:52224\n"
"ds_write_b32 v70, v226 offset:23392\n"
"v_pk_mul_f32 v[114:115], v[92:93], v[94:95] op_sel:[0,0] op_sel_hi:[0,1]\n"
"v_pk_mul_f32 v[168:169], v[146:147], v[148:149] op_sel:[0,0] op_sel_hi:[0,1]\n"
"v_pk_mul_f32 v[222:223], v[200:201], v[202:203] op_sel:[0,0] op_sel_hi:[0,1]\n"
"v_pk_fma_f32 v[108:109], v[92:93], v[94:95], v[114:115] op_sel:[1,1,0] op_sel_hi:[1,0,1] neg_lo:[0,1,0]\n"
"v_pk_fma_f32 v[162:163], v[146:147], v[148:149], v[168:169] op_sel:[1,1,0] op_sel_hi:[1,0,1] neg_lo:[0,1,0]\n"
"v_pk_fma_f32 v[216:217], v[200:201], v[202:203], v[222:223] op_sel:[1,1,0] op_sel_hi:[1,0,1] neg_lo:[0,1,0]\n"
"v_cvt_pk_bf16_f32 v119, v108, v109\n"
"v_cvt_pk_bf16_f32 v173, v162, v163\n"
"v_cvt_pk_bf16_f32 v227, v216, v217\n"
"ds_write_addtid_b32 v119 offset:23936\n"
"ds_write_addtid_b32 v173 offset:52768\n"
"ds_write_b32 v70, v227 offset:23936\n"
"v_pk_mul_f32 v[112:113], v[94:95], v[94:95] op_sel:[0,0] op_sel_hi:[0,1]\n"
"v_pk_mul_f32 v[166:167], v[148:149], v[148:149] op_sel:[0,0] op_sel_hi:[0,1]\n"
"v_pk_mul_f32 v[220:221], v[202:203], v[202:203] op_sel:[0,0] op_sel_hi:[0,1]\n"
"v_pk_fma_f32 v[102:103], v[94:95], v[94:95], v[112:113] op_sel:[1,1,0] op_sel_hi:[1,0,1] neg_lo:[0,1,0]\n"
"v_pk_fma_f32 v[156:157], v[148:149], v[148:149], v[166:167] op_sel:[1,1,0] op_sel_hi:[1,0,1] neg_lo:[0,1,0]\n"
"v_pk_fma_f32 v[210:211], v[202:203], v[202:203], v[220:221] op_sel:[1,1,0] op_sel_hi:[1,0,1] neg_lo:[0,1,0]\n"
"v_cvt_pk_bf16_f32 v116, v102, v103\n"
"v_cvt_pk_bf16_f32 v170, v156, v157\n"
"v_cvt_pk_bf16_f32 v224, v210, v211\n"
"ds_write_addtid_b32 v116 offset:24480\n"
"ds_write_addtid_b32 v170 offset:53312\n"
"ds_write_b32 v70, v224 offset:24480\n"
"v_pk_mul_f32 v[114:115], v[94:95], v[96:97] op_sel:[0,0] op_sel_hi:[0,1]\n"
"v_pk_mul_f32 v[168:169], v[148:149], v[150:151] op_sel:[0,0] op_sel_hi:[0,1]\n"
"v_pk_mul_f32 v[222:223], v[202:203], v[204:205] op_sel:[0,0] op_sel_hi:[0,1]\n"
"v_pk_fma_f32 v[104:105], v[94:95], v[96:97], v[114:115] op_sel:[1,1,0] op_sel_hi:[1,0,1] neg_lo:[0,1,0]\n"
"v_pk_fma_f32 v[158:159], v[148:149], v[150:151], v[168:169] op_sel:[1,1,0] op_sel_hi:[1,0,1] neg_lo:[0,1,0]\n"
"v_pk_fma_f32 v[212:213], v[202:203], v[204:205], v[222:223] op_sel:[1,1,0] op_sel_hi:[1,0,1] neg_lo:[0,1,0]\n"
"v_cvt_pk_bf16_f32 v117, v104, v105\n"
"v_cvt_pk_bf16_f32 v171, v158, v159\n"
"v_cvt_pk_bf16_f32 v225, v212, v213\n"
"ds_write_addtid_b32 v117 offset:25024\n"
"ds_write_addtid_b32 v171 offset:53856\n"
"ds_write_b32 v70, v225 offset:25024\n"
"v_pk_mul_f32 v[112:113], v[96:97], v[96:97] op_sel:[0,0] op_sel_hi:[0,1]\n"
"v_pk_mul_f32 v[166:167], v[150:151], v[150:151] op_sel:[0,0] op_sel_hi:[0,1]\n"
"v_pk_mul_f32 v[220:221], v[204:205], v[204:205] op_sel:[0,0] op_sel_hi:[0,1]\n"
"v_pk_fma_f32 v[106:107], v[96:97], v[96:97], v[112:113] op_sel:[1,1,0] op_sel_hi:[1,0,1] neg_lo:[0,1,0]\n"
"v_pk_fma_f32 v[160:161], v[150:151], v[150:151], v[166:167] op_sel:[1,1,0] op_sel_hi:[1,0,1] neg_lo:[0,1,0]\n"
"v_pk_fma_f32 v[214:215], v[204:205], v[204:205], v[220:221] op_sel:[1,1,0] op_sel_hi:[1,0,1] neg_lo:[0,1,0]\n"
"v_cvt_pk_bf16_f32 v118, v106, v107\n"
"v_cvt_pk_bf16_f32 v172, v160, v161\n"
"v_cvt_pk_bf16_f32 v226, v214, v215\n"
"ds_write_addtid_b32 v118 offset:25568\n"
"ds_write_addtid_b32 v172 offset:54400\n"
"ds_write_b32 v70, v226 offset:25568\n"
"v_pk_mul_f32 v[114:115], v[96:97], v[98:99] op_sel:[0,0] op_sel_hi:[0,1]\n"
"v_pk_mul_f32 v[168:169], v[150:151], v[152:153] op_sel:[0,0] op_sel_hi:[0,1]\n"
"v_pk_mul_f32 v[222:223], v[204:205], v[206:207] op_sel:[0,0] op_sel_hi:[0,1]\n"
"v_pk_fma_f32 v[108:109], v[96:97], v[98:99], v[114:115] op_sel:[1,1,0] op_sel_hi:[1,0,1] neg_lo:[0,1,0]\n"
"v_pk_fma_f32 v[162:163], v[150:151], v[152:153], v[168:169] op_sel:[1,1,0] op_sel_hi:[1,0,1] neg_lo:[0,1,0]\n"
"global_load_dwordx2 v[48:49], v249, s[26:27] offset:3456\n"
"v_pk_fma_f32 v[216:217], v[204:205], v[206:207], v[222:223] op_sel:[1,1,0] op_sel_hi:[1,0,1] neg_lo:[0,1,0]\n"
"v_cvt_pk_bf16_f32 v119, v108, v109\n"
"v_cvt_pk_bf16_f32 v173, v162, v163\n"
"v_cvt_pk_bf16_f32 v227, v216, v217\n"
"ds_write_addtid_b32 v119 offset:26112\n"
"ds_write_addtid_b32 v173 offset:54944\n"
"ds_write_b32 v70, v227 offset:26112\n"
"v_pk_mul_f32 v[112:113], v[98:99], v[98:99] op_sel:[0,0] op_sel_hi:[0,1]\n"
"v_pk_mul_f32 v[166:167], v[152:153], v[152:153] op_sel:[0,0] op_sel_hi:[0,1]\n"
"v_pk_mul_f32 v[220:221], v[206:207], v[206:207] op_sel:[0,0] op_sel_hi:[0,1]\n"
"v_pk_fma_f32 v[102:103], v[98:99], v[98:99], v[112:113] op_sel:[1,1,0] op_sel_hi:[1,0,1] neg_lo:[0,1,0]\n"
"v_pk_fma_f32 v[156:157], v[152:153], v[152:153], v[166:167] op_sel:[1,1,0] op_sel_hi:[1,0,1] neg_lo:[0,1,0]\n"
"v_pk_fma_f32 v[210:211], v[206:207], v[206:207], v[220:221] op_sel:[1,1,0] op_sel_hi:[1,0,1] neg_lo:[0,1,0]\n"
"v_cvt_pk_bf16_f32 v116, v102, v103\n"
"v_cvt_pk_bf16_f32 v170, v156, v157\n"
"v_cvt_pk_bf16_f32 v224, v210, v211\n"
"ds_write_addtid_b32 v116 offset:26656\n"
"ds_write_addtid_b32 v170 offset:55488\n"
"ds_write_b32 v70, v224 offset:26656\n"
"s_branch FR2_%=\n"
"FR1_%=:\n"
"global_load_dwordx2 v[38:39], v248, s[26:27] offset:2304\n"
"v_mov_b32 v108, 0x3fb8aa3b\n"
"v_mov_b32 v162, 0x3fb8aa3b\n"
"v_mov_b32 v216, 0x3fb8aa3b\n"
"v_mul_f32 v108, s28, v108\n"
"v_mul_f32 v162, s29, v162\n"
"v_mul_f32 v216, s30, v216\n"
"v_exp_f32 v108, v108\n"
"v_exp_f32 v162, v162\n"
"v_exp_f32 v216, v216\n"
"v_cvt_pk_bf16_f32 v108, v15, v108\n"
"v_cvt_pk_bf16_f32 v162, v15, v162\n"
"v_cvt_pk_bf16_f32 v216, v15, v216\n"
"v_mul_f32 v110, 0x3fb8aa3b, v108\n"
"v_mul_f32 v164, 0x3fb8aa3b, v162\n"
"v_mul_f32 v218, 0x3fb8aa3b, v216\n"
"v_mul_f32 v111, 0xbe22f983, v108\n"
"v_mul_f32 v165, 0xbe22f983, v162\n"
"v_mul_f32 v219, 0xbe22f983, v216\n"
"s_waitcnt vmcnt(23)\n"
"v_pk_mul_f32 v[110:111], v[110:111], v[16:17]\n"
"s_waitcnt vmcnt(17)\n"
"v_pk_mul_f32 v[164:165], v[164:165], v[22:23]\n"
"s_waitcnt vmcnt(11)\n"
"v_pk_mul_f32 v[218:219], v[218:219], v[28:29]\n"
"v_pk_mul_f32 v[90:91], v[18:19], v[108:109] op_sel_hi:[1,0]\n"
"v_pk_mul_f32 v[144:145], v[24:25], v[162:163] op_sel_hi:[1,0]\n"
"s_waitcnt vmcnt(9)\n"
"v_pk_mul_f32 v[198:199], v[30:31], v[216:217] op_sel_hi:[1,0]\n"
"v_pk_mul_f32 v[98:99], v[20:21], v[90:91] op_sel:[0,0] op_sel_hi:[0,1] neg_hi:[0,1]\n"
"v_pk_mul_f32 v[152:153], v[26:27], v[144:145] op_sel:[0,0] op_sel_hi:[0,1] neg_hi:[0,1]\n"
"s_waitcnt vmcnt(7)\n"
"v_pk_mul_f32 v[206:207], v[32:33], v[198:199] op_sel:[0,0] op_sel_hi:[0,1] neg_hi:[0,1]\n"
"v_pk_fma_f32 v[72:73], v[20:21], v[90:91], v[98:99] op_sel:[1,1,0] op_sel_hi:[1,0,1] neg_lo:[0,1,0] neg_hi:[0,1,0]\n"
"v_pk_fma_f32 v[126:127], v[26:27], v[144:145], v[152:153] op_sel:[1,1,0] op_sel_hi:[1,0,1] neg_lo:[0,1,0] neg_hi:[0,1,0]\n"
"v_pk_fma_f32 v[180:181], v[32:33], v[198:199], v[206:207] op_sel:[1,1,0] op_sel_hi:[1,0,1] neg_lo:[0,1,0] neg_hi:[0,1,0]\n"
"v_exp_f32 v100, v110\n"
"v_exp_f32 v154, v164\n"
"v_exp_f32 v208, v218\n"
"v_fract_f32 v101, v111\n"
"v_fract_f32 v155, v165\n"
"v_fract_f32 v209, v219\n"
"v_cos_f32 v88, v101\n"
"v_cos_f32 v142, v155\n"
"v_cos_f32 v196, v209\n"
"v_sin_f32 v89, v101\n"
"v_sin_f32 v143, v155\n"
"v_sin_f32 v197, v209\n"
"v_pk_mul_f32 v[88:89], v[88:89], v[100:101] op_sel_hi:[1,0]\n"
"v_pk_mul_f32 v[142:143], v[142:143], v[154:155] op_sel_hi:[1,0]\n"
"v_pk_mul_f32 v[196:197], v[196:197], v[208:209] op_sel_hi:[1,0]\n"
"v_cvt_pk_bf16_f32 v105, v72, v73\n"
"v_cvt_pk_bf16_f32 v159, v126, v127\n"
"v_cvt_pk_bf16_f32 v213, v180, v181\n"
"ds_write_addtid_b32 v105\n"
"ds_write_addtid_b32 v159 offset:28832\n"
"ds_write_b32 v70, v213\n"
"v_mul_f32 v98, 0x43800000, v110\n"
"v_mul_f32 v152, 0x43800000, v164\n"
"v_mul_f32 v206, 0x43800000, v218\n"
"v_mul_f32 v99, 0x43800000, v111\n"
"v_mul_f32 v153, 0x43800000, v165\n"
"v_mul_f32 v207, 0x43800000, v219\n"
"v_exp_f32 v98, v98\n"
"v_exp_f32 v152, v152\n"
"v_exp_f32 v206, v206\n"
"v_fract_f32 v99, v99\n"
"v_fract_f32 v153, v153\n"
"v_fract_f32 v207, v207\n"
"v_cos_f32 v94, v99\n"
"v_cos_f32 v148, v153\n"
"v_cos_f32 v202, v207\n"
"v_sin_f32 v95, v99\n"
"v_sin_f32 v149, v153\n"
"v_sin_f32 v203, v207\n"
"v_pk_mul_f32 v[94:95], v[94:95], v[98:99] op_sel_hi:[1,0]\n"
"v_pk_mul_f32 v[148:149], v[148:149], v[152:153] op_sel_hi:[1,0]\n"
"v_pk_mul_f32 v[202:203], v[202:203], v[206:207] op_sel_hi:[1,0]\n"
"v_pk_mul_f32 v[100:101], v[88:89], v[88:89] op_sel:[0,0] op_sel_hi:[0,1]\n"
"v_pk_mul_f32 v[154:155], v[142:143], v[142:143] op_sel:[0,0] op_sel_hi:[0,1]\n"
"v_pk_mul_f32 v[208:209], v[196:197], v[196:197] op_sel:[0,0] op_sel_hi:[0,1]\n"
"v_pk_fma_f32 v[90:91], v[88:89], v[88:89], v[100:101] op_sel:[1,1,0] op_sel_hi:[1,0,1] neg_lo:[0,1,0]\n"
"global_load_dwordx2 v[40:41], v248, s[26:27] offset:3456\n"
"v_pk_fma_f32 v[144:145], v[142:143], v[142:143], v[154:155] op_sel:[1,1,0] op_sel_hi:[1,0,1] neg_lo:[0,1,0]\n"
"v_pk_fma_f32 v[198:199], v[196:197], v[196:197], v[208:209] op_sel:[1,1,0] op_sel_hi:[1,0,1] neg_lo:[0,1,0]\n"
"v_pk_mul_f32 v[98:99], v[72:73], v[88:89] op_sel:[0,0] op_sel_hi:[0,1]\n"
"v_pk_mul_f32 v[152:153], v[126:127], v[142:143] op_sel:[0,0] op_sel_hi:[0,1]\n"
"v_pk_mul_f32 v[206:207], v[180:181], v[196:197] op_sel:[0,0] op_sel_hi:[0,1]\n"
"v_pk_fma_f32 v[74:75], v[72:73], v[88:89], v[98:99] op_sel:[1,1,0] op_sel_hi:[1,0,1] neg_lo:[0,1,0]\n"
"v_pk_fma_f32 v[128:129], v[126:127], v[142:143], v[152:153] op_sel:[1,1,0] op_sel_hi:[1,0,1] neg_lo:[0,1,0]\n"
"v_pk_fma_f32 v[182:183], v[180:181], v[196:197], v[206:207] op_sel:[1,1,0] op_sel_hi:[1,0,1] neg_lo:[0,1,0]\n"
"v_cvt_pk_bf16_f32 v106, v74, v75\n"
"v_cvt_pk_bf16_f32 v160, v128, v129\n"
"v_cvt_pk_bf16_f32 v214, v182, v183\n"
"ds_write_addtid_b32 v106 offset:544\n"
"ds_write_addtid_b32 v160 offset:29376\n"
"ds_write_b32 v70, v214 offset:544\n"
"v_mul_f32 v100, 0x43680000, v110\n"
"v_mul_f32 v154, 0x43680000, v164\n"
"v_mul_f32 v208, 0x43680000, v218\n"
"v_mul_f32 v101, 0x43680000, v111\n"
"v_mul_f32 v155, 0x43680000, v165\n"
"v_mul_f32 v209, 0x43680000, v219\n"
"v_exp_f32 v100, v100\n"
"v_exp_f32 v154, v154\n"
"v_exp_f32 v208, v208\n"
"v_fract_f32 v101, v101\n"
"v_fract_f32 v155, v155\n"
"v_fract_f32 v209, v209\n"
"v_cos_f32 v96, v101\n"
"v_cos_f32 v150, v155\n"
"v_cos_f32 v204, v209\n"
"v_sin_f32 v97, v101\n"
"v_sin_f32 v151, v155\n"
"v_sin_f32 v205, v209\n"
"v_pk_mul_f32 v[96:97], v[96:97], v[100:101] op_sel_hi:[1,0] neg_hi:[1,0]\n"
"v_pk_mul_f32 v[150:151], v[150:151], v[154:155] op_sel_hi:[1,0] neg_hi:[1,0]\n"
"v_pk_mul_f32 v[204:205], v[204:205], v[208:209] op_sel_hi:[1,0] neg_hi:[1,0]\n"
"v_cvt_pk_bf16_f32 v107, v96, v97\n"
"v_cvt_pk_bf16_f32 v161, v150, v151\n"
"v_cvt_pk_bf16_f32 v215, v204, v205\n"
"ds_write_addtid_b32 v107 offset:27200\n"
"ds_write_addtid_b32 v161 offset:56032\n"
"ds_write_b32 v70, v215 offset:27200\n"
"v_pk_mul_f32 v[100:101], v[72:73], v[90:91] op_sel:[0,0] op_sel_hi:[0,1]\n"
"v_pk_mul_f32 v[154:155], v[126:127], v[144:145] op_sel:[0,0] op_sel_hi:[0,1]\n"
"v_pk_mul_f32 v[208:209], v[180:181], v[198:199] op_sel:[0,0] op_sel_hi:[0,1]\n"
"v_pk_fma_f32 v[76:77], v[72:73], v[90:91], v[100:101] op_sel:[1,1,0] op_sel_hi:[1,0,1] neg_lo:[0,1,0]\n"
"v_pk_fma_f32 v[130:131], v[126:127], v[144:145], v[154:155] op_sel:[1,1,0] op_sel_hi:[1,0,1] neg_lo:[0,1,0]\n"
"v_pk_fma_f32 v[184:185], v[180:181], v[198:199], v[208:209] op_sel:[1,1,0] op_sel_hi:[1,0,1] neg_lo:[0,1,0]\n"
"v_cvt_pk_bf16_f32 v104, v76, v77\n"
"v_cvt_pk_bf16_f32 v158, v130, v131\n"
"v_cvt_pk_bf16_f32 v212, v184, v185\n"
"ds_write_addtid_b32 v104 offset:1088\n"
"ds_write_addtid_b32 v158 offset:29920\n"
"ds_write_b32 v70, v212 offset:1088\n"
"v_pk_mul_f32 v[98:99], v[74:75], v[90:91] op_sel:[0,0] op_sel_hi:[0,1]\n"
"v_pk_mul_f32 v[152:153], v[128:129], v[144:145] op_sel:[0,0] op_sel_hi:[0,1]\n"
"v_pk_mul_f32 v[206:207], v[182:183], v[198:199] op_sel:[0,0] op_sel_hi:[0,1]\n"
"v_pk_fma_f32 v[78:79], v[74:75], v[90:91], v[98:99] op_sel:[1,1,0] op_sel_hi:[1,0,1] neg_lo:[0,1,0]\n"
"v_pk_fma_f32 v[132:133], v[128:129], v[144:145], v[152:153] op_sel:[1,1,0] op_sel_hi:[1,0,1] neg_lo:[0,1,0]\n"
"v_pk_fma_f32 v[186:187], v[182:183], v[198:199], v[206:207] op_sel:[1,1,0] op_sel_hi:[1,0,1] neg_lo:[0,1,0]\n"
"v_cvt_pk_bf16_f32 v105, v78, v79\n"
"v_cvt_pk_bf16_f32 v159, v132, v133\n"
"v_cvt_pk_bf16_f32 v213, v186, v187\n"
"ds_write_addtid_b32 v105 offset:1632\n"
"ds_write_addtid_b32 v159 offset:30464\n"
"ds_write_b32 v70, v213 offset:1632\n"
"v_pk_mul_f32 v[100:101], v[76:77], v[90:91] op_sel:[0,0] op_sel_hi:[0,1]\n"
"v_pk_mul_f32 v[154:155], v[130:131], v[144:145] op_sel:[0,0] op_sel_hi:[0,1]\n"
"v_pk_mul_f32 v[208:209], v[184:185], v[198:199] op_sel:[0,0] op_sel_hi:[0,1]\n"
"v_pk_fma_f32 v[80:81], v[76:77], v[90:91], v[100:101] op_sel:[1,1,0] op_sel_hi:[1,0,1] neg_lo:[0,1,0]\n"
"v_pk_fma_f32 v[134:135], v[130:131], v[144:145], v[154:155] op_sel:[1,1,0] op_sel_hi:[1,0,1] neg_lo:[0,1,0]\n"
"v_pk_fma_f32 v[188:189], v[184:185], v[198:199], v[208:209] op_sel:[1,1,0] op_sel_hi:[1,0,1] neg_lo:[0,1,0]\n"
"v_cvt_pk_bf16_f32 v106, v80, v81\n"
"v_cvt_pk_bf16_f32 v160, v134, v135\n"
"v_cvt_pk_bf16_f32 v214, v188, v189\n"
"ds_write_addtid_b32 v106 offset:2176\n"
"ds_write_addtid_b32 v160 offset:31008\n"
"global_load_dwordx2 v[42:43], v249, s[26:27]\n"
"ds_write_b32 v70, v214 offset:2176\n"
"v_pk_mul_f32 v[98:99], v[78:79], v[90:91] op_sel:[0,0] op_sel_hi:[0,1]\n"
"v_pk_mul_f32 v[152:153], v[132:133], v[144:145] op_sel:[0,0] op_sel_hi:[0,1]\n"
"v_pk_mul_f32 v[206:207], v[186:187], v[198:199] op_sel:[0,0] op_sel_hi:[0,1]\n"
"v_pk_fma_f32 v[82:83], v[78:79], v[90:91], v[98:99] op_sel:[1,1,0] op_sel_hi:[1,0,1] neg_lo:[0,1,0]\n"
"v_pk_fma_f32 v[136:137], v[132:133], v[144:145], v[152:153] op_sel:[1,1,0] op_sel_hi:[1,0,1] neg_lo:[0,1,0]\n"
"v_pk_fma_f32 v[190:191], v[186:187], v[198:199], v[206:207] op_sel:[1,1,0] op_sel_hi:[1,0,1] neg_lo:[0,1,0]\n"
"v_cvt_pk_bf16_f32 v107, v82, v83\n"
"v_cvt_pk_bf16_f32 v161, v136, v137\n"
"v_cvt_pk_bf16_f32 v215, v190, v191\n"
"ds_write_addtid_b32 v107 offset:2720\n"
"ds_write_addtid_b32 v161 offset:31552\n"
"ds_write_b32 v70, v215 offset:2720\n"
"v_pk_mul_f32 v[100:101], v[80:81], v[90:91] op_sel:[0,0] op_sel_hi:[0,1]\n"
"v_pk_mul_f32 v[154:155], v[134:135], v[144:145] op_sel:[0,0] op_sel_hi:[0,1]\n"
"v_pk_mul_f32 v[208:209], v[188:189], v[198:199] op_sel:[0,0] op_sel_hi:[0,1]\n"
"v_pk_fma_f32 v[84:85], v[80:81], v[90:91], v[100:101] op_sel:[1,1,0] op_sel_hi:[1,0,1] neg_lo:[0,1,0]\n"
"v_pk_fma_f32 v[138:139], v[134:135], v[144:145], v[154:155] op_sel:[1,1,0] op_sel_hi:[1,0,1] neg_lo:[0,1,0]\n"
"v_pk_fma_f32 v[192:193], v[188:189], v[198:199], v[208:209] op_sel:[1,1,0] op_sel_hi:[1,0,1] neg_lo:[0,1,0]\n"
"v_cvt_pk_bf16_f32 v104, v84, v85\n"
"v_cvt_pk_bf16_f32 v158, v138, v139\n"
"v_cvt_pk_bf16_f32 v212, v192, v193\n"
"ds_write_addtid_b32 v104 offset:3264\n"
"ds_write_addtid_b32 v158 offset:32096\n"
"ds_write_b32 v70, v212 offset:3264\n"
"v_pk_mul_f32 v[98:99], v[82:83], v[90:91] op_sel:[0,0] op_sel_hi:[0,1]\n"
"v_pk_mul_f32 v[152:153], v[136:137], v[144:145] op_sel:[0,0] op_sel_hi:[0,1]\n"
"v_pk_mul_f32 v[206:207], v[190:191], v[198:199] op_sel:[0,0] op_sel_hi:[0,1]\n"
"v_pk_fma_f32 v[86:87], v[82:83], v[90:91], v[98:99] op_sel:[1,1,0] op_sel_hi:[1,0,1] neg_lo:[0,1,0]\n"
"v_pk_fma_f32 v[140:141], v[136:137], v[144:145], v[152:153] op_sel:[1,1,0] op_sel_hi:[1,0,1] neg_lo:[0,1,0]\n"
"v_pk_fma_f32 v[194:195], v[190:191], v[198:199], v[206:207] op_sel:[1,1,0] op_sel_hi:[1,0,1] neg_lo:[0,1,0]\n"
"v_cvt_pk_bf16_f32 v105, v86, v87\n"
"v_cvt_pk_bf16_f32 v159, v140, v141\n"
"v_cvt_pk_bf16_f32 v213, v194, v195\n"
"ds_write_addtid_b32 v105 offset:3808\n"
"ds_write_addtid_b32 v159 offset:32640\n"
"ds_write_b32 v70, v213 offset:3808\n"
"v_pk_mul_f32 v[100:101], v[90:91], v[90:91] op_sel:[0,0] op_sel_hi:[0,1]\n"
"v_pk_mul_f32 v[154:155], v[144:145], v[144:145] op_sel:[0,0] op_sel_hi:[0,1]\n"
"v_pk_mul_f32 v[208:209], v[198:199], v[198:199] op_sel:[0,0] op_sel_hi:[0,1]\n"
"v_pk_fma_f32 v[92:93], v[90:91], v[90:91], v[100:101] op_sel:[1,1,0] op_sel_hi:[1,0,1] neg_lo:[0,1,0]\n"
"v_pk_fma_f32 v[146:147], v[144:145], v[144:145], v[154:155] op_sel:[1,1,0] op_sel_hi:[1,0,1] neg_lo:[0,1,0]\n"
"v_pk_fma_f32 v[200:201], v[198:199], v[198:199], v[208:209] op_sel:[1,1,0] op_sel_hi:[1,0,1] neg_lo:[0,1,0]\n"
"v_pk_mul_f32 v[100:101], v[92:93], v[92:93] op_sel:[0,0] op_sel_hi:[0,1]\n"
"v_pk_mul_f32 v[154:155], v[146:147], v[146:147] op_sel:[0,0] op_sel_hi:[0,1]\n"
"v_pk_mul_f32 v[208:209], v[200:201], v[200:201] op_sel:[0,0] op_sel_hi:[0,1]\n"
"v_pk_fma_f32 v[92:93], v[92:93], v[92:93], v[100:101] op_sel:[1,1,0] op_sel_hi:[1,0,1] neg_lo:[0,1,0]\n"
"v_pk_fma_f32 v[146:147], v[146:147], v[146:147], v[154:155] op_sel:[1,1,0] op_sel_hi:[1,0,1] neg_lo:[0,1,0]\n"
"v_pk_fma_f32 v[200:201], v[200:201], v[200:201], v[208:209] op_sel:[1,1,0] op_sel_hi:[1,0,1] neg_lo:[0,1,0]\n"
"v_pk_mul_f32 v[100:101], v[72:73], v[94:95] op_sel:[0,0] op_sel_hi:[0,1]\n"
"v_pk_mul_f32 v[154:155], v[126:127], v[148:149] op_sel:[0,0] op_sel_hi:[0,1]\n"
"v_pk_mul_f32 v[208:209], v[180:181], v[202:203] op_sel:[0,0] op_sel_hi:[0,1]\n"
"v_pk_fma_f32 v[72:73], v[72:73], v[94:95], v[100:101] op_sel:[1,1,0] op_sel_hi:[1,0,1] neg_lo:[0,1,0]\n"
"v_pk_fma_f32 v[126:127], v[126:127], v[148:149], v[154:155] op_sel:[1,1,0] op_sel_hi:[1,0,1] neg_lo:[0,1,0]\n"
"v_pk_fma_f32 v[180:181], v[180:181], v[202:203], v[208:209] op_sel:[1,1,0] op_sel_hi:[1,0,1] neg_lo:[0,1,0]\n"
"v_cvt_pk_bf16_f32 v106, v72, v73\n"
"v_cvt_pk_bf16_f32 v160, v126, v127\n"
"v_cvt_pk_bf16_f32 v214, v180, v181\n"
"ds_write_addtid_b32 v106 offset:4352\n"
"ds_write_addtid_b32 v160 offset:33184\n"
"ds_write_b32 v70, v214 offset:4352\n"
"v_pk_mul_f32 v[98:99], v[76:77], v[94:95] op_sel:[0,0] op_sel_hi:[0,1]\n"
"v_pk_mul_f32 v[152:153], v[130:131], v[148:149] op_sel:[0,0] op_sel_hi:[0,1]\n"
"v_pk_mul_f32 v[206:207], v[184:185], v[202:203] op_sel:[0,0] op_sel_hi:[0,1]\n"
"v_pk_fma_f32 v[76:77], v[76:77], v[94:95], v[98:99] op_sel:[1,1,0] op_sel_hi:[1,0,1] neg_lo:[0,1,0]\n"
"v_pk_fma_f32 v[130:131], v[130:131], v[148:149], v[152:153] op_sel:[1,1,0] op_sel_hi:[1,0,1] neg_lo:[0,1,0]\n"
"v_pk_fma_f32 v[184:185], v[184:185], v[202:203], v[206:207] op_sel:[1,1,0] op_sel_hi:[1,0,1] neg_lo:[0,1,0]\n"
"v_cvt_pk_bf16_f32 v107, v76, v77\n"
"v_cvt_pk_bf16_f32 v161, v130, v131\n"
"v_cvt_pk_bf16_f32 v215, v184, v185\n"
"ds_write_addtid_b32 v107 offset:4896\n"
"ds_write_addtid_b32 v161 offset:33728\n"
"ds_write_b32 v70, v215 offset:4896\n"
"v_pk_mul_f32 v[100:101], v[80:81], v[94:95] op_sel:[0,0] op_sel_hi:[0,1]\n"
"v_pk_mul_f32 v[154:155], v[134:135], v[148:149] op_sel:[0,0] op_sel_hi:[0,1]\n"
"v_pk_mul_f32 v[208:209], v[188:189], v[202:203] op_sel:[0,0] op_sel_hi:[0,1]\n"
"global_load_dwordx2 v[44:45], v249, s[26:27] offset:1152\n"
"v_pk_fma_f32 v[80:81], v[80:81], v[94:95], v[100:101] op_sel:[1,1,0] op_sel_hi:[1,0,1] neg_lo:[0,1,0]\n"
"v_pk_fma_f32 v[134:135], v[134:135], v[148:149], v[154:155] op_sel:[1,1,0] op_sel_hi:[1,0,1] neg_lo:[0,1,0]\n"
"v_pk_fma_f32 v[188:189], v[188:189], v[202:203], v[208:209] op_sel:[1,1,0] op_sel_hi:[1,0,1] neg_lo:[0,1,0]\n"
"v_cvt_pk_bf16_f32 v104, v80, v81\n"
"v_cvt_pk_bf16_f32 v158, v134, v135\n"
"v_cvt_pk_bf16_f32 v212, v188, v189\n"
"ds_write_addtid_b32 v104 offset:5440\n"
"ds_write_addtid_b32 v158 offset:34272\n"
"ds_write_b32 v70, v212 offset:5440\n"
"v_pk_mul_f32 v[98:99], v[84:85], v[94:95] op_sel:[0,0] op_sel_hi:[0,1]\n"
"v_pk_mul_f32 v[152:153], v[138:139], v[148:149] op_sel:[0,0] op_sel_hi:[0,1]\n"
"v_pk_mul_f32 v[206:207], v[192:193], v[202:203] op_sel:[0,0] op_sel_hi:[0,1]\n"
"v_pk_fma_f32 v[84:85], v[84:85], v[94:95], v[98:99] op_sel:[1,1,0] op_sel_hi:[1,0,1] neg_lo:[0,1,0]\n"
"v_pk_fma_f32 v[138:139], v[138:139], v[148:149], v[152:153] op_sel:[1,1,0] op_sel_hi:[1,0,1] neg_lo:[0,1,0]\n"
"v_pk_fma_f32 v[192:193], v[192:193], v[202:203], v[206:207] op_sel:[1,1,0] op_sel_hi:[1,0,1] neg_lo:[0,1,0]\n"
"v_cvt_pk_bf16_f32 v105, v84, v85\n"
"v_cvt_pk_bf16_f32 v159, v138, v139\n"
"v_cvt_pk_bf16_f32 v213, v192, v193\n"
"ds_write_addtid_b32 v105 offset:5984\n"
"ds_write_addtid_b32 v159 offset:34816\n"
"ds_write_b32 v70, v213 offset:5984\n"
"v_pk_mul_f32 v[100:101], v[72:73], v[94:95] op_sel:[0,0] op_sel_hi:[0,1]\n"
"v_pk_mul_f32 v[154:155], v[126:127], v[148:149] op_sel:[0,0] op_sel_hi:[0,1]\n"
"v_pk_mul_f32 v[208:209], v[180:181], v[202:203] op_sel:[0,0] op_sel_hi:[0,1]\n"
"v_pk_fma_f32 v[72:73], v[72:73], v[94:95], v[100:101] op_sel:[1,1,0] op_sel_hi:[1,0,1] neg_lo:[0,1,0]\n"
"v_pk_fma_f32 v[126:127], v[126:127], v[148:149], v[154:155] op_sel:[1,1,0] op_sel_hi:[1,0,1] neg_lo:[0,1,0]\n"
"v_pk_fma_f32 v[180:181], v[180:181], v[202:203], v[208:209] op_sel:[1,1,0] op_sel_hi:[1,0,1] neg_lo:[0,1,0]\n"
"v_cvt_pk_bf16_f32 v106, v72, v73\n"
"v_cvt_pk_bf16_f32 v160, v126, v127\n"
"v_cvt_pk_bf16_f32 v214, v180, v181\n"
"ds_write_addtid_b32 v106 offset:6528\n"
"ds_write_addtid_b32 v160 offset:35360\n"
"ds_write_b32 v70, v214 offset:6528\n"
"v_pk_mul_f32 v[98:99], v[80:81], v[94:95] op_sel:[0,0] op_sel_hi:[0,1]\n"
"v_pk_mul_f32 v[152:153], v[134:135], v[148:149] op_sel:[0,0] op_sel_hi:[0,1]\n"
"v_pk_mul_f32 v[206:207], v[188:189], v[202:203] op_sel:[0,0] op_sel_hi:[0,1]\n"
"v_pk_fma_f32 v[80:81], v[80:81], v[94:95], v[98:99] op_sel:[1,1,0] op_sel_hi:[1,0,1] neg_lo:[0,1,0]\n"
"v_pk_fma_f32 v[134:135], v[134:135], v[148:149], v[152:153] op_sel:[1,1,0] op_sel_hi:[1,0,1] neg_lo:[0,1,0]\n"
"v_pk_fma_f32 v[188:189], v[188:189], v[202:203], v[206:207] op_sel:[1,1,0] op_sel_hi:[1,0,1] neg_lo:[0,1,0]\n"
"v_cvt_pk_bf16_f32 v107, v80, v81\n"
"v_cvt_pk_bf16_f32 v161, v134, v135\n"
"v_cvt_pk_bf16_f32 v215, v188, v189\n"
"ds_write_addtid_b32 v107 offset:7072\n"
"ds_write_addtid_b32 v161 offset:35904\n"
"ds_write_b32 v70, v215 offset:7072\n"
"v_pk_mul_f32 v[100:101], v[72:73], v[94:95] op_sel:[0,0] op_sel_hi:[0,1]\n"
"v_pk_mul_f32 v[154:155], v[126:127], v[148:149] op_sel:[0,0] op_sel_hi:[0,1]\n"
"v_pk_mul_f32 v[208:209], v[180:181], v[202:203] op_sel:[0,0] op_sel_hi:[0,1]\n"
"v_pk_fma_f32 v[72:73], v[72:73], v[94:95], v[100:101] op_sel:[1,1,0] op_sel_hi:[1,0,1] neg_lo:[0,1,0]\n"
"v_pk_fma_f32 v[126:127], v[126:127], v[148:149], v[154:155] op_sel:[1,1,0] op_sel_hi:[1,0,1] neg_lo:[0,1,0]\n"
"v_pk_fma_f32 v[180:181], v[180:181], v[202:203], v[208:209] op_sel:[1,1,0] op_sel_hi:[1,0,1] neg_lo:[0,1,0]\n"
"v_cvt_pk_bf16_f32 v104, v72, v73\n"
"v_cvt_pk_bf16_f32 v158, v126, v127\n"
"v_cvt_pk_bf16_f32 v212, v180, v181\n"
"ds_write_addtid_b32 v104 offset:7616\n"
"ds_write_addtid_b32 v158 offset:36448\n"
"ds_write_b32 v70, v212 offset:7616\n"
"v_pk_mul_f32 v[98:99], v[80:81], v[94:95] op_sel:[0,0] op_sel_hi:[0,1]\n"
"v_pk_mul_f32 v[152:153], v[134:135], v[148:149] op_sel:[0,0] op_sel_hi:[0,1]\n"
"v_pk_mul_f32 v[206:207], v[188:189], v[202:203] op_sel:[0,0] op_sel_hi:[0,1]\n"
"v_pk_fma_f32 v[80:81], v[80:81], v[94:95], v[98:99] op_sel:[1,1,0] op_sel_hi:[1,0,1] neg_lo:[0,1,0]\n"
"v_pk_fma_f32 v[134:135], v[134:135], v[148:149], v[152:153] op_sel:[1,1,0] op_sel_hi:[1,0,1] neg_lo:[0,1,0]\n"
"v_pk_fma_f32 v[188:189], v[188:189], v[202:203], v[206:207] op_sel:[1,1,0] op_sel_hi:[1,0,1] neg_lo:[0,1,0]\n"
"v_cvt_pk_bf16_f32 v105, v80, v81\n"
"v_cvt_pk_bf16_f32 v159, v134, v135\n"
"v_cvt_pk_bf16_f32 v213, v188, v189\n"
"ds_write_addtid_b32 v105 offset:8160\n"
"ds_write_addtid_b32 v159 offset:36992\n"
"ds_write_b32 v70, v213 offset:8160\n"
"v_pk_mul_f32 v[100:101], v[72:73], v[94:95] op_sel:[0,0] op_sel_hi:[0,1]\n"
"v_pk_mul_f32 v[154:155], v[126:127], v[148:149] op_sel:[0,0] op_sel_hi:[0,1]\n"
"v_pk_mul_f32 v[208:209], v[180:181], v[202:203] op_sel:[0,0] op_sel_hi:[0,1]\n"
"v_pk_fma_f32 v[72:73], v[72:73], v[94:95], v[100:101] op_sel:[1,1,0] op_sel_hi:[1,0,1] neg_lo:[0,1,0]\n"
"v_pk_fma_f32 v[126:127], v[126:127], v[148:149], v[154:155] op_sel:[1,1,0] op_sel_hi:[1,0,1] neg_lo:[0,1,0]\n"
"v_pk_fma_f32 v[180:181], v[180:181], v[202:203], v[208:209] op_sel:[1,1,0] op_sel_hi:[1,0,1] neg_lo:[0,1,0]\n"
"v_cvt_pk_bf16_f32 v106, v72, v73\n"
"global_load_dwordx2 v[46:47], v249, s[26:27] offset:2304\n"
"v_cvt_pk_bf16_f32 v160, v126, v127\n"
"v_cvt_pk_bf16_f32 v214, v180, v181\n"
"ds_write_addtid_b32 v106 offset:8704\n"
"ds_write_addtid_b32 v160 offset:37536\n"
"ds_write_b32 v70, v214 offset:8704\n"
"v_pk_mul_f32 v[98:99], v[96:97], v[92:93] op_sel:[0,0] op_sel_hi:[0,1] neg_hi:[0,1]\n"
"v_pk_mul_f32 v[152:153], v[150:151], v[146:147] op_sel:[0,0] op_sel_hi:[0,1] neg_hi:[0,1]\n"
"v_pk_mul_f32 v[206:207], v[204:205], v[200:201] op_sel:[0,0] op_sel_hi:[0,1] neg_hi:[0,1]\n"
"v_pk_fma_f32 v[96:97], v[96:97], v[92:93], v[98:99] op_sel:[1,1,0] op_sel_hi:[1,0,1]\n"
"v_pk_fma_f32 v[150:151], v[150:151], v[146:147], v[152:153] op_sel:[1,1,0] op_sel_hi:[1,0,1]\n"
"v_pk_fma_f32 v[204:205], v[204:205], v[200:201], v[206:207] op_sel:[1,1,0] op_sel_hi:[1,0,1]\n"
"v_cvt_pk_bf16_f32 v107, v96, v97\n"
"v_cvt_pk_bf16_f32 v161, v150, v151\n"
"v_cvt_pk_bf16_f32 v215, v204, v205\n"
"ds_write_addtid_b32 v107 offset:27744\n"
"ds_write_addtid_b32 v161 offset:56576\n"
"ds_write_b32 v70, v215 offset:27744\n"
"v_pk_mul_f32 v[100:101], v[72:73], v[94:95] op_sel:[0,0] op_sel_hi:[0,1]\n"
"v_pk_mul_f32 v[154:155], v[126:127], v[148:149] op_sel:[0,0] op_sel_hi:[0,1]\n"
"v_pk_mul_f32 v[208:209], v[180:181], v[202:203] op_sel:[0,0] op_sel_hi:[0,1]\n"
"v_pk_fma_f32 v[72:73], v[72:73], v[94:95], v[100:101] op_sel:[1,1,0] op_sel_hi:[1,0,1] neg_lo:[0,1,0]\n"
"v_pk_fma_f32 v[126:127], v[126:127], v[148:149], v[154:155] op_sel:[1,1,0] op_sel_hi:[1,0,1] neg_lo:[0,1,0]\n"
"v_pk_fma_f32 v[180:181], v[180:181], v[202:203], v[208:209] op_sel:[1,1,0] op_sel_hi:[1,0,1] neg_lo:[0,1,0]\n"
"v_cvt_pk_bf16_f32 v104, v72, v73\n"
"v_cvt_pk_bf16_f32 v158, v126, v127\n"
"v_cvt_pk_bf16_f32 v212, v180, v181\n"
"ds_write_addtid_b32 v104 offset:9248\n"
"ds_write_addtid_b32 v158 offset:38080\n"
"ds_write_b32 v70, v212 offset:9248\n"
"v_pk_mul_f32 v[98:99], v[96:97], v[92:93] op_sel:[0,0] op_sel_hi:[0,1] neg_hi:[0,1]\n"
"v_pk_mul_f32 v[152:153], v[150:151], v[146:147] op_sel:[0,0] op_sel_hi:[0,1] neg_hi:[0,1]\n"
"v_pk_mul_f32 v[206:207], v[204:205], v[200:201] op_sel:[0,0] op_sel_hi:[0,1] neg_hi:[0,1]\n"
"v_pk_fma_f32 v[96:97], v[96:97], v[92:93], v[98:99] op_sel:[1,1,0] op_sel_hi:[1,0,1]\n"
"v_pk_fma_f32 v[150:151], v[150:151], v[146:147], v[152:153] op_sel:[1,1,0] op_sel_hi:[1,0,1]\n"
"v_pk_fma_f32 v[204:205], v[204:205], v[200:201], v[206:207] op_sel:[1,1,0] op_sel_hi:[1,0,1]\n"
"v_cvt_pk_bf16_f32 v105, v96, v97\n"
"v_cvt_pk_bf16_f32 v159, v150, v151\n"
"v_cvt_pk_bf16_f32 v213, v204, v205\n"
"ds_write_addtid_b32 v105 offset:28288\n"
"ds_write_addtid_b32 v159 offset:57120\n"
"ds_write_b32 v70, v213 offset:28288\n"
"v_pk_mul_f32 v[100:101], v[72:73], v[94:95] op_sel:[0,0] op_sel_hi:[0,1]\n"
"v_pk_mul_f32 v[154:155], v[126:127], v[148:149] op_sel:[0,0] op_sel_hi:[0,1]\n"
"v_pk_mul_f32 v[208:209], v[180:181], v[202:203] op_sel:[0,0] op_sel_hi:[0,1]\n"
"v_pk_fma_f32 v[72:73], v[72:73], v[94:95], v[100:101] op_sel:[1,1,0] op_sel_hi:[1,0,1] neg_lo:[0,1,0]\n"
"v_pk_fma_f32 v[126:127], v[126:127], v[148:149], v[154:155] op_sel:[1,1,0] op_sel_hi:[1,0,1] neg_lo:[0,1,0]\n"
"v_pk_fma_f32 v[180:181], v[180:181], v[202:203], v[208:209] op_sel:[1,1,0] op_sel_hi:[1,0,1] neg_lo:[0,1,0]\n"
"v_cvt_pk_bf16_f32 v106, v72, v73\n"
"v_cvt_pk_bf16_f32 v160, v126, v127\n"
"v_cvt_pk_bf16_f32 v214, v180, v181\n"
"ds_write_addtid_b32 v106 offset:9792\n"
"ds_write_addtid_b32 v160 offset:38624\n"
"ds_write_b32 v70, v214 offset:9792\n"
"v_pk_mul_f32 v[98:99], v[72:73], v[94:95] op_sel:[0,0] op_sel_hi:[0,1]\n"
"v_pk_mul_f32 v[152:153], v[126:127], v[148:149] op_sel:[0,0] op_sel_hi:[0,1]\n"
"v_pk_mul_f32 v[206:207], v[180:181], v[202:203] op_sel:[0,0] op_sel_hi:[0,1]\n"
"v_pk_fma_f32 v[72:73], v[72:73], v[94:95], v[98:99] op_sel:[1,1,0] op_sel_hi:[1,0,1] neg_lo:[0,1,0]\n"
"global_load_dwordx2 v[48:49], v249, s[26:27] offset:3456\n"
"v_pk_fma_f32 v[126:127], v[126:127], v[148:149], v[152:153] op_sel:[1,1,0] op_sel_hi:[1,0,1] neg_lo:[0,1,0]\n"
"v_pk_fma_f32 v[180:181], v[180:181], v[202:203], v[206:207] op_sel:[1,1,0] op_sel_hi:[1,0,1] neg_lo:[0,1,0]\n"
"v_cvt_pk_bf16_f32 v107, v72, v73\n"
"v_cvt_pk_bf16_f32 v161, v126, v127\n"
"v_cvt_pk_bf16_f32 v215, v180, v181\n"
"ds_write_addtid_b32 v107 offset:10336\n"
"ds_write_addtid_b32 v161 offset:39168\n"
"ds_write_b32 v70, v215 offset:10336\n"
"v_pk_mul_f32 v[100:101], v[72:73], v[94:95] op_sel:[0,0] op_sel_hi:[0,1]\n"
"v_pk_mul_f32 v[154:155], v[126:127], v[148:149] op_sel:[0,0] op_sel_hi:[0,1]\n"
"v_pk_mul_f32 v[208:209], v[180:181], v[202:203] op_sel:[0,0] op_sel_hi:[0,1]\n"
"v_pk_fma_f32 v[72:73], v[72:73], v[94:95], v[100:101] op_sel:[1,1,0] op_sel_hi:[1,0,1] neg_lo:[0,1,0]\n"
"v_pk_fma_f32 v[126:127], v[126:127], v[148:149], v[154:155] op_sel:[1,1,0] op_sel_hi:[1,0,1] neg_lo:[0,1,0]\n"
"v_pk_fma_f32 v[180:181], v[180:181], v[202:203], v[208:209] op_sel:[1,1,0] op_sel_hi:[1,0,1] neg_lo:[0,1,0]\n"
"v_cvt_pk_bf16_f32 v104, v72, v73\n"
"v_cvt_pk_bf16_f32 v158, v126, v127\n"
"v_cvt_pk_bf16_f32 v212, v180, v181\n"
"ds_write_addtid_b32 v104 offset:10880\n"
"ds_write_addtid_b32 v158 offset:39712\n"
"ds_write_b32 v70, v212 offset:10880\n"
"FR2_%=:\n"
"s_waitcnt lgkmcnt(0)\n"
"s_barrier\n"
"s_waitcnt vmcnt(7)\n"
"s_nop 0\n"
"s_cmp_eq_u32 s33, 3\n"
"s_cbranch_scc0 FK0_%=\n"
"s_and_b32 s41, s42, 127\n"
"s_cmp_eq_u32 s41, 124\n"
"s_cbranch_scc1 FT3_%=\n"
"global_load_dword v14, v250, s[62:63]\n"
"s_sub_u32 s62, s62, 1327104\n"
"s_subb_u32 s63, s63, 0\n"
"global_load_dword v14, v250, s[62:63]\n"
"s_sub_u32 s62, s62, 1327104\n"
"s_subb_u32 s63, s63, 0\n"
"global_load_dword v14, v250, s[62:63]\n"
"s_sub_u32 s62, s62, 1327104\n"
"s_subb_u32 s63, s63, 0\n"
"global_load_dword v14, v250, s[62:63]\n"
"FT3_%=:\n"
"v_accvgpr_read_b32 v84, a204\n"
"v_accvgpr_read_b32 v85, a205\n"
"v_accvgpr_read_b32 v86, a206\n"
"v_accvgpr_read_b32 v87, a207\n"
"v_and_b32 v72, 0xffff, v84\n"
"v_lshrrev_b32 v73, 16, v84\n"
"v_and_b32 v74, 0xffff, v85\n"
"v_lshrrev_b32 v75, 16, v85\n"
"v_and_b32 v76, 0xffff, v86\n"
"v_lshrrev_b32 v77, 16, v86\n"
"v_and_b32 v78, 0xffff, v87\n"
"v_lshrrev_b32 v79, 16, v87\n"
"v_mov_b32 v100, 0\n"
"v_mov_b32 v101, 0\n"
"v_mov_b32 v102, 0\n"
"v_mov_b32 v103, 0\n"
"v_min_u32 v104, 23, v1\n"
"v_lshrrev_b32 v105, 1, v104\n"
"v_mul_u32_u24 v105, 1024, v105\n"
"v_lshl_add_u32 v104, v104, 4, v105\n"
"v_add_u32 v104, 119264, v104\n"
"v_min_u32 v105, 34, v1\n"
"v_lshlrev_b32 v105, 4, v105\n"
"v_add_u32 v105, 131936, v105\n"
"v_subrev_u32 v106, 2496, v236\n"
"ds_write_b128 v104, v[100:103]\n"
"ds_write_b128 v105, v[100:103] offset:0\n"
"ds_write_b128 v105, v[100:103] offset:4672\n"
"ds_write_b128 v105, v[100:103] offset:9344\n"
"ds_write_b128 v106, v[100:103] offset:0\n"
"ds_write_b128 v106, v[100:103] offset:1024\n"
"ds_write_b128 v106, v[100:103] offset:2048\n"
"v_and_b32 v88, 15, v1\n"
"v_lshrrev_b32 v89, 1, v1\n"
"v_and_b32 v89, 24, v89\n"
"s_mov_b32 s41, 57344\n"
"v_lshlrev_b32_e64 v90, v88, s41\n"
"v_lshrrev_b32 v90, v89, v90\n"
"v_bfe_u32 v92, v90, 0, 2\n"
"v_bfe_u32 v93, v90, 2, 2\n"
"v_bfe_u32 v94, v90, 4, 2\n"
"v_bfe_u32 v95, v90, 6, 2\n"
"v_mul_u32_u24 v92, 0x8001, v92\n"
"v_mul_u32_u24 v93, 0x8001, v93\n"
"v_mul_u32_u24 v94, 0x8001, v94\n"
"v_mul_u32_u24 v95, 0x8001, v95\n"
"v_and_b32 v92, 0x10001, v92\n"
"v_and_b32 v93, 0x10001, v93\n"
"v_and_b32 v94, 0x10001, v94\n"
"v_and_b32 v95, 0x10001, v95\n"
"v_mul_u32_u24 v92, 0x3f80, v92\n"
"v_mul_u32_u24 v93, 0x3f80, v93\n"
"v_mul_u32_u24 v94, 0x3f80, v94\n"
"v_mul_u32_u24 v95, 0x3f80, v95\n"
"ds_write_b128 v236, v[92:95] offset:6144\n"
"s_mov_b32 s41, 7936\n"
"v_lshlrev_b32_e64 v90, v88, s41\n"
"v_lshrrev_b32 v90, v89, v90\n"
"v_bfe_u32 v96, v90, 0, 2\n"
"v_bfe_u32 v97, v90, 2, 2\n"
"v_bfe_u32 v98, v90, 4, 2\n"
"v_bfe_u32 v99, v90, 6, 2\n"
"v_mul_u32_u24 v96, 0x8001, v96\n"
"v_mul_u32_u24 v97, 0x8001, v97\n"
"v_mul_u32_u24 v98, 0x8001, v98\n"
"v_mul_u32_u24 v99, 0x8001, v99\n"
"v_and_b32 v96, 0x10001, v96\n"
"v_and_b32 v97, 0x10001, v97\n"
"v_and_b32 v98, 0x10001, v98\n"
"v_and_b32 v99, 0x10001, v99\n"
"v_mul_u32_u24 v96, 0x3f80, v96\n"
"v_mul_u32_u24 v97, 0x3f80, v97\n"
"v_mul_u32_u24 v98, 0x3f80, v98\n"
"v_mul_u32_u24 v99, 0x3f80, v99\n"
"ds_write_b128 v236, v[96:99] offset:7168\n"
"s_mov_b32 s41, 224\n"
"v_lshlrev_b32_e64 v90, v88, s41\n"
"v_lshrrev_b32 v90, v89, v90\n"
"v_bfe_u32 v92, v90, 0, 2\n"
"v_bfe_u32 v93, v90, 2, 2\n"
"v_bfe_u32 v94, v90, 4, 2\n"
"v_bfe_u32 v95, v90, 6, 2\n"
"v_mul_u32_u24 v92, 0x8001, v92\n"
"v_mul_u32_u24 v93, 0x8001, v93\n"
"v_mul_u32_u24 v94, 0x8001, v94\n"
"v_mul_u32_u24 v95, 0x8001, v95\n"
"v_and_b32 v92, 0x10001, v92\n"
"v_and_b32 v93, 0x10001, v93\n"
"v_and_b32 v94, 0x10001, v94\n"
"v_and_b32 v95, 0x10001, v95\n"
"v_mul_u32_u24 v92, 0x3f80, v92\n"
"v_mul_u32_u24 v93, 0x3f80, v93\n"
"v_mul_u32_u24 v94, 0x3f80, v94\n"
"v_mul_u32_u24 v95, 0x3f80, v95\n"
"ds_write_b128 v236, v[92:95] offset:8192\n"
"s_mov_b32 s41, 31\n"
"v_lshlrev_b32_e64 v90, v88, s41\n"
"v_lshrrev_b32 v90, v89, v90\n"
"v_bfe_u32 v96, v90, 0, 2\n"
"v_bfe_u32 v97, v90, 2, 2\n"
"v_bfe_u32 v98, v90, 4, 2\n"
"v_bfe_u32 v99, v90, 6, 2\n"
"v_mul_u32_u24 v96, 0x8001, v96\n"
"v_mul_u32_u24 v97, 0x8001, v97\n"
"v_mul_u32_u24 v98, 0x8001, v98\n"
"v_mul_u32_u24 v99, 0x8001, v99\n"
"v_and_b32 v96, 0x10001, v96\n"
"v_and_b32 v97, 0x10001, v97\n"
"v_and_b32 v98, 0x10001, v98\n"
"v_and_b32 v99, 0x10001, v99\n"
"v_mul_u32_u24 v96, 0x3f80, v96\n"
"v_mul_u32_u24 v97, 0x3f80, v97\n"
"v_mul_u32_u24 v98, 0x3f80, v98\n"
"v_mul_u32_u24 v99, 0x3f80, v99\n"
"ds_write_b128 v236, v[96:99] offset:9216\n"
"s_mov_b32 s41, 65024\n"
"v_lshlrev_b32_e64 v90, v88, s41\n"
"v_lshrrev_b32 v90, v89, v90\n"
"v_bfe_u32 v92, v90, 0, 2\n"
"v_bfe_u32 v93, v90, 2, 2\n"
"v_bfe_u32 v94, v90, 4, 2\n"
"v_bfe_u32 v95, v90, 6, 2\n"
"v_mul_u32_u24 v92, 0x8001, v92\n"
"v_mul_u32_u24 v93, 0x8001, v93\n"
"v_mul_u32_u24 v94, 0x8001, v94\n"
"v_mul_u32_u24 v95, 0x8001, v95\n"
"v_and_b32 v92, 0x10001, v92\n"
"v_and_b32 v93, 0x10001, v93\n"
"v_and_b32 v94, 0x10001, v94\n"
"v_and_b32 v95, 0x10001, v95\n"
"v_mul_u32_u24 v92, 0x3f80, v92\n"
"v_mul_u32_u24 v93, 0x3f80, v93\n"
"v_mul_u32_u24 v94, 0x3f80, v94\n"
"v_mul_u32_u24 v95, 0x3f80, v95\n"
"ds_write_b128 v236, v[92:95] offset:10240\n"
"s_mov_b32 s41, 511\n"
"v_lshlrev_b32_e64 v90, v88, s41\n"
"v_lshrrev_b32 v90, v89, v90\n"
"v_bfe_u32 v96, v90, 0, 2\n"
"v_bfe_u32 v97, v90, 2, 2\n"
"v_bfe_u32 v98, v90, 4, 2\n"
"v_bfe_u32 v99, v90, 6, 2\n"
"v_mul_u32_u24 v96, 0x8001, v96\n"
"v_mul_u32_u24 v97, 0x8001, v97\n"
"v_mul_u32_u24 v98, 0x8001, v98\n"
"v_mul_u32_u24 v99, 0x8001, v99\n"
"v_and_b32 v96, 0x10001, v96\n"
"v_and_b32 v97, 0x10001, v97\n"
"v_and_b32 v98, 0x10001, v98\n"
"v_and_b32 v99, 0x10001, v99\n"
"v_mul_u32_u24 v96, 0x3f80, v96\n"
"v_mul_u32_u24 v97, 0x3f80, v97\n"
"v_mul_u32_u24 v98, 0x3f80, v98\n"
"v_mul_u32_u24 v99, 0x3f80, v99\n"
"ds_write_b128 v236, v[96:99] offset:11264\n"
"s_mov_b32 s41, 49152\n"
"v_lshlrev_b32_e64 v90, v88, s41\n"
"v_lshrrev_b32 v90, v89, v90\n"
"v_bfe_u32 v80, v90, 0, 2\n"
"v_bfe_u32 v81, v90, 2, 2\n"
"v_bfe_u32 v82, v90, 4, 2\n"
"v_bfe_u32 v83, v90, 6, 2\n"
"v_mul_u32_u24 v80, 0x8001, v80\n"
"v_mul_u32_u24 v81, 0x8001, v81\n"
"v_mul_u32_u24 v82, 0x8001, v82\n"
"v_mul_u32_u24 v83, 0x8001, v83\n"
"v_and_b32 v80, 0x10001, v80\n"
"v_and_b32 v81, 0x10001, v81\n"
"v_and_b32 v82, 0x10001, v82\n"
"v_and_b32 v83, 0x10001, v83\n"
"v_mul_u32_u24 v80, 0xffff, v80\n"
"v_mul_u32_u24 v81, 0xffff, v81\n"
"v_mul_u32_u24 v82, 0xffff, v82\n"
"v_mul_u32_u24 v83, 0xffff, v83\n"
"ds_write_b128 v236, v[80:83] offset:3072\n"
"s_branch FK3_%=\n"
"FK0_%=:\n"
"ds_read_b128 a[0:3], v68\n"
"ds_read_b128 a[4:7], v68 offset:11424\n"
"ds_read_b128 a[8:11], v68 offset:20128\n"
"ds_read_b128 a[12:15], v68 offset:8704\n"
"ds_read_b128 a[16:19], v68 offset:64\n"
"ds_read_b128 a[20:23], v68 offset:11488\n"
"ds_read_b128 a[24:27], v68 offset:20192\n"
"ds_read_b128 a[28:31], v68 offset:8768\n"
"ds_read_b128 a[32:35], v68 offset:128\n"
"ds_read_b128 a[36:39], v68 offset:11552\n"
"ds_read_b128 a[40:43], v68 offset:20256\n"
"ds_read_b128 a[44:47], v68 offset:8832\n"
"s_mul_i32 s41, s33, 135268368\n"
"s_waitcnt lgkmcnt(10)\n"
"v_mfma_f32_16x16x32_bf16 v[72:75], a[0:3], a[4:7], 0\n"
"v_accvgpr_read_b32 v8, a204\n"
"s_waitcnt lgkmcnt(9)\n"
"v_mfma_f32_16x16x32_bf16 v[76:79], a[0:3], a[8:11], 0\n"
"v_accvgpr_read_b32 v9, a205\n"
"s_waitcnt lgkmcnt(8)\n"
"v_mfma_f32_16x16x32_bf16 v[80:83], a[12:15], a[4:7], 0\n"
"v_add_u32 v62, s41, v62\n"
"v_mfma_f32_16x16x32_bf16 v[84:87], a[12:15], a[8:11], 0\n"
"ds_read_b128 a[48:51], v68 offset:192\n"
"ds_read_b128 a[52:55], v68 offset:11616\n"
"ds_read_b128 a[56:59], v68 offset:20320\n"
"ds_read_b128 a[60:63], v68 offset:8896\n"
"v_add_u32 v63, s41, v63\n"
"s_waitcnt lgkmcnt(10)\n"
"v_mfma_f32_16x16x32_bf16 v[72:75], a[16:19], a[20:23], v[72:75]\n"
"v_add_u32 v64, s41, v64\n"
"s_waitcnt lgkmcnt(9)\n"
"v_mfma_f32_16x16x32_bf16 v[76:79], a[16:19], a[24:27], v[76:79]\n"
"v_add_u32 v65, s41, v65\n"
"s_waitcnt lgkmcnt(8)\n"
"v_mfma_f32_16x16x32_bf16 v[80:83], a[28:31], a[20:23], v[80:83]\n"
"v_add_u32 v66, s41, v66\n"
"v_mfma_f32_16x16x32_bf16 v[84:87], a[28:31], a[24:27], v[84:87]\n"
"ds_read_b128 a[64:67], v68 offset:256\n"
"ds_read_b128 a[68:71], v68 offset:11680\n"
"ds_read_b128 a[72:75], v68 offset:20384\n"
"ds_read_b128 a[76:79], v68 offset:8960\n"
"v_add_u32 v67, s41, v67\n"
"s_waitcnt lgkmcnt(10)\n"
"v_mfma_f32_16x16x32_bf16 v[72:75], a[32:35], a[36:39], v[72:75]\n"
"v_add_u32 v8, s41, v8\n"
"s_waitcnt lgkmcnt(9)\n"
"v_mfma_f32_16x16x32_bf16 v[76:79], a[32:35], a[40:43], v[76:79]\n"
"v_add_u32 v9, s41, v9\n"
"s_waitcnt lgkmcnt(8)\n"
"v_mfma_f32_16x16x32_bf16 v[80:83], a[44:47], a[36:39], v[80:83]\n"
"v_and_b32 v4, 0xffff, v8\n"
"v_mfma_f32_16x16x32_bf16 v[84:87], a[44:47], a[40:43], v[84:87]\n"
"ds_read_b128 a[80:83], v68 offset:320\n"
"ds_read_b128 a[84:87], v68 offset:11744\n"
"ds_read_b128 a[88:91], v68 offset:20448\n"
"ds_read_b128 a[92:95], v68 offset:9024\n"
"v_lshrrev_b32 v5, 16, v8\n"
"s_waitcnt lgkmcnt(10)\n"
"v_mfma_f32_16x16x32_bf16 v[72:75], a[48:51], a[52:55], v[72:75]\n"
"v_and_b32 v6, 0xffff, v9\n"
"s_waitcnt lgkmcnt(9)\n"
"v_mfma_f32_16x16x32_bf16 v[76:79], a[48:51], a[56:59], v[76:79]\n"
"v_lshrrev_b32 v7, 16, v9\n"
"s_waitcnt lgkmcnt(8)\n"
"v_mfma_f32_16x16x32_bf16 v[80:83], a[60:63], a[52:55], v[80:83]\n"
"v_lshrrev_b32 v50, 16, v62\n"
"v_mfma_f32_16x16x32_bf16 v[84:87], a[60:63], a[56:59], v[84:87]\n"
"ds_read_b128 a[96:99], v68 offset:384\n"
"ds_read_b128 a[100:103], v68 offset:11808\n"
"ds_read_b128 a[104:107], v68 offset:20512\n"
"ds_read_b128 a[108:111], v68 offset:9088\n"
"v_and_b32 v51, 0xffff, v63\n"
"s_waitcnt lgkmcnt(10)\n"
"v_mfma_f32_16x16x32_bf16 v[72:75], a[64:67], a[68:71], v[72:75]\n"
"v_lshrrev_b32 v52, 16, v63\n"
"s_waitcnt lgkmcnt(9)\n"
"v_mfma_f32_16x16x32_bf16 v[76:79], a[64:67], a[72:75], v[76:79]\n"
"v_and_b32 v53, 0xffff, v64\n"
"s_waitcnt lgkmcnt(8)\n"
"v_mfma_f32_16x16x32_bf16 v[80:83], a[76:79], a[68:71], v[80:83]\n"
"v_lshrrev_b32 v54, 16, v64\n"
"v_mfma_f32_16x16x32_bf16 v[84:87], a[76:79], a[72:75], v[84:87]\n"
"ds_read_b128 a[112:115], v68 offset:448\n"
"ds_read_b128 a[116:119], v68 offset:11872\n"
"ds_read_b128 a[120:123], v68 offset:20576\n"
"ds_read_b128 a[124:127], v68 offset:9152\n"
"v_and_b32 v55, 0xffff, v65\n"
"s_waitcnt lgkmcnt(10)\n"
"v_mfma_f32_16x16x32_bf16 v[72:75], a[80:83], a[84:87], v[72:75]\n"
"v_lshrrev_b32 v56, 16, v65\n"
"s_waitcnt lgkmcnt(9)\n"
"v_mfma_f32_16x16x32_bf16 v[76:79], a[80:83], a[88:91], v[76:79]\n"
"v_and_b32 v57, 0xffff, v66\n"
"s_waitcnt lgkmcnt(8)\n"
"v_mfma_f32_16x16x32_bf16 v[80:83], a[92:95], a[84:87], v[80:83]\n"
"v_lshrrev_b32 v58, 16, v66\n"
"v_mfma_f32_16x16x32_bf16 v[84:87], a[92:95], a[88:91], v[84:87]\n"
"v_and_b32 v59, 0xffff, v67\n"
"s_waitcnt lgkmcnt(6)\n"
"v_mfma_f32_16x16x32_bf16 v[72:75], a[96:99], a[100:103], v[72:75]\n"
"v_lshrrev_b32 v60, 16, v67\n"
"s_waitcnt lgkmcnt(5)\n"
"v_mfma_f32_16x16x32_bf16 v[76:79], a[96:99], a[104:107], v[76:79]\n"
"s_mul_i32 s41, s33, 832\n"
"s_waitcnt lgkmcnt(4)\n"
"v_mfma_f32_16x16x32_bf16 v[80:83], a[108:111], a[100:103], v[80:83]\n"
"v_lshrrev_b32 v8, 5, v1\n"
"v_mfma_f32_16x16x32_bf16 v[84:87], a[108:111], a[104:107], v[84:87]\n"
"s_add_u32 s41, s41, 146072\n"
"s_waitcnt lgkmcnt(2)\n"
"v_mfma_f32_16x16x32_bf16 v[72:75], a[112:115], a[116:119], v[72:75]\n"
"v_mul_u32_u24 v8, 200, v8\n"
"s_waitcnt lgkmcnt(1)\n"
"v_mfma_f32_16x16x32_bf16 v[76:79], a[112:115], a[120:123], v[76:79]\n"
"v_lshl_add_u32 v9, v1, 3, s41\n"
"s_waitcnt lgkmcnt(0)\n"
"v_mfma_f32_16x16x32_bf16 v[80:83], a[124:127], a[116:119], v[80:83]\n"
"v_add_u32 v255, v8, v9\n"
"v_mfma_f32_16x16x32_bf16 v[84:87], a[124:127], a[120:123], v[84:87]\n"
"s_mul_i32 m0, s33, 2064\n"
"v_add_f32 v72, v72, v61\n"
"v_cvt_pk_bf16_f32 v88, v72, v73\n"
"ds_write_addtid_b32 v88\n"
"v_cvt_pk_bf16_f32 v89, v74, v75\n"
"ds_write_addtid_b32 v89 offset:256\n"
"v_cvt_pk_bf16_f32 v90, v76, v77\n"
"ds_write_addtid_b32 v90 offset:512\n"
"v_cvt_pk_bf16_f32 v91, v78, v79\n"
"ds_write_addtid_b32 v91 offset:768\n"
"v_cvt_pk_bf16_f32 v92, v80, v81\n"
"ds_write_addtid_b32 v92 offset:1024\n"
"v_cvt_pk_bf16_f32 v93, v82, v83\n"
"ds_write_addtid_b32 v93 offset:1280\n"
"v_cvt_pk_bf16_f32 v94, v84, v85\n"
"ds_write_addtid_b32 v94 offset:1536\n"
"v_cvt_pk_bf16_f32 v95, v86, v87\n"
"ds_write_addtid_b32 v95 offset:1792\n"
"FK3_%=:\n"
"s_waitcnt lgkmcnt(0)\n"
"s_barrier\n"
"s_cmp_eq_u32 s33, 3\n"
"s_cbranch_scc1 FL3_%=\n"
"ds_read_u16 v72, v50\n"
"ds_read_u16 v73, v51\n"
"ds_read_u16 v74, v52\n"
"ds_read_u16 v75, v53\n"
"ds_read_u16 v76, v54\n"
"ds_read_u16 v77, v55\n"
"ds_read_u16 v78, v56\n"
"ds_read_u16 v79, v57\n"
"ds_read_u16 v80, v58\n"
"ds_read_u16 v81, v59\n"
"ds_read_u16 v82, v60\n"
"ds_read_u16 v84, v4\n"
"ds_read_u16 v85, v5\n"
"ds_read_u16 v86, v6\n"
"ds_read_u16 v87, v7\n"
"s_branch FL0_%=\n"
"FL3_%=:\n"
"ds_read_u16 v84, v72\n"
"ds_read_u16 v85, v73\n"
"ds_read_u16 v86, v74\n"
"ds_read_u16 v87, v75\n"
"ds_read_u16 v88, v76\n"
"ds_read_u16 v89, v77\n"
"ds_read_u16 v90, v78\n"
"ds_read_u16 v91, v79\n"
"ds_read_u16 v92, v72 offset:2064\n"
"ds_read_u16 v93, v73 offset:2064\n"
"ds_read_u16 v94, v74 offset:2064\n"
"ds_read_u16 v95, v75 offset:2064\n"
"ds_read_u16 v96, v76 offset:2064\n"
"ds_read_u16 v97, v77 offset:2064\n"
"ds_read_u16 v98, v78 offset:2064\n"
"ds_read_u16 v99, v79 offset:2064\n"
"ds_read_u16 v100, v72 offset:4128\n"
"ds_read_u16 v101, v73 offset:4128\n"
"ds_read_u16 v102, v74 offset:4128\n"
"ds_read_u16 v103, v75 offset:4128\n"
"ds_read_u16 v104, v76 offset:4128\n"
"ds_read_u16 v105, v77 offset:4128\n"
"ds_read_u16 v106, v78 offset:4128\n"
"ds_read_u16 v107, v79 offset:4128\n"
"FL0_%=:\n"
"s_and_b32 s41, s42, 127\n"
"s_cmp_eq_u32 s41, 124\n"
"s_cbranch_scc0 FMA_%=\n"
"s_lshr_b32 s41, s36, 2\n"
"s_sub_u32 s41, s42, s41\n"
"s_add_u32 s41, s41, 4\n"
"s_bfe_u32 s41, s41, 0x60004\n"
"s_lshl_b32 s41, s41, 2\n"
"s_add_u32 s41, s41, s33\n"
"s_lshl_b32 s41, s41, 12\n"
"s_add_u32 s41, s41, 32047104\n"
"v_lshl_add_u32 v243, v1, 4, s41\n"
"s_nop 0\n"
"FMA_%=:\n"
"global_load_dwordx4 v[164:167], v243, s[96:97] sc1\n"
"global_load_dwordx4 v[168:171], v243, s[96:97] offset:1024 sc1\n"
"global_load_dwordx4 v[172:175], v243, s[96:97] offset:2048 sc1\n"
"global_load_dwordx4 v[176:179], v243, s[96:97] offset:3072 sc1\n"
"global_load_dwordx2 v[180:181], v242, s[96:97] sc1\n"
"s_cmp_eq_u32 s33, 3\n"
"s_cbranch_scc1 FP3_%=\n"
"s_waitcnt lgkmcnt(4)\n"
"v_lshl_or_b32 v108, v76, 16, v75\n"
"v_lshl_or_b32 v109, v78, 16, v77\n"
"v_lshl_or_b32 v110, v80, 16, v79\n"
"v_lshl_or_b32 v111, v82, 16, v81\n"
"ds_write_b128 v236, v[108:111]\n"
"v_lshl_or_b32 v112, v75, 16, v74\n"
"v_lshl_or_b32 v113, v77, 16, v76\n"
"v_lshl_or_b32 v114, v79, 16, v78\n"
"v_lshl_or_b32 v115, v81, 16, v80\n"
"ds_write_b128 v236, v[112:115] offset:1056\n"
"v_lshl_or_b32 v116, v74, 16, v73\n"
"v_lshl_or_b32 v117, v76, 16, v75\n"
"v_lshl_or_b32 v118, v78, 16, v77\n"
"v_lshl_or_b32 v119, v80, 16, v79\n"
"ds_write_b128 v236, v[116:119] offset:2112\n"
"v_lshl_or_b32 v120, v73, 16, v72\n"
"v_lshl_or_b32 v121, v75, 16, v74\n"
"v_lshl_or_b32 v122, v77, 16, v76\n"
"v_lshl_or_b32 v123, v79, 16, v78\n"
"ds_write_b128 v236, v[120:123] offset:3168\n"
"s_waitcnt lgkmcnt(4)\n"
"v_lshl_or_b32 v124, v85, 16, v84\n"
"v_lshl_or_b32 v125, v87, 16, v86\n"
"ds_write_b64 v255, v[124:125]\n"
"s_branch FP0_%=\n"
"FP3_%=:\n"
"s_waitcnt lgkmcnt(15)\n"
"v_lshl_or_b32 v112, v85, 16, v84\n"
"v_lshl_or_b32 v113, v87, 16, v86\n"
"v_lshl_or_b32 v114, v89, 16, v88\n"
"v_lshl_or_b32 v115, v91, 16, v90\n"
"ds_write_b128 v236, v[112:115]\n"
"s_waitcnt lgkmcnt(9)\n"
"v_lshl_or_b32 v116, v93, 16, v92\n"
"v_lshl_or_b32 v117, v95, 16, v94\n"
"v_lshl_or_b32 v118, v97, 16, v96\n"
"v_lshl_or_b32 v119, v99, 16, v98\n"
"ds_write_b128 v236, v[116:119] offset:1024\n"
"s_waitcnt lgkmcnt(2)\n"
"v_lshl_or_b32 v120, v101, 16, v100\n"
"v_lshl_or_b32 v121, v103, 16, v102\n"
"v_lshl_or_b32 v122, v105, 16, v104\n"
"v_lshl_or_b32 v123, v107, 16, v106\n"
"ds_write_b128 v236, v[120:123] offset:2048\n"
"FP0_%=:\n"
"v_mov_b32 v140, 0\n"
"v_mov_b32 v141, 0\n"
"v_mov_b32 v142, 0\n"
"v_mov_b32 v143, 0\n"
"v_min_u32 v144, 107, %[tid]\n"
"v_mul_u32_u24 v145, 57, v144\n"
"v_lshrrev_b32 v145, 9, v145\n"
"v_mul_u32_u24 v145, 1040, v145\n"
"v_lshl_add_u32 v144, v144, 4, v145\n"
"ds_write_b128 v144, v[140:143] offset:6368\n"
"v_lshrrev_b32 v146, 4, v1\n"
"v_and_b32 v149, 15, v1\n"
"s_cmp_eq_u32 s33, 3\n"
"s_cselect_b32 s45, 0, s33\n"
"s_mul_i32 s41, s45, 4672\n"
"s_add_u32 s41, s41, 132448\n"
"v_lshl_add_u32 v147, v146, 4, s41\n"
"v_lshrrev_b32 v150, 2, v149\n"
"v_and_b32 v151, 1, v149\n"
"v_lshl_add_u32 v150, v150, 1, v151\n"
"v_bfe_u32 v151, v149, 1, 1\n"
"v_lshl_add_u32 v150, v151, 3, v150\n"
"v_sub_u32 v150, 15, v150\n"
"v_lshl_add_u32 v3, v150, 5, v147\n"
"v_lshl_add_u32 v150, v149, 5, v147\n"
"v_subrev_u32 v10, 992, v150\n"
"v_sub_u32 v150, 15, v149\n"
"v_lshl_add_u32 v150, v150, 5, v147\n"
"v_subrev_u32 v235, 4, v150\n"
"v_add_u32 v11, 1020, v150\n"
"s_mul_i32 s41, s45, 832\n"
"s_add_u32 s41, s41, 145952\n"
"v_lshl_add_u32 v150, v146, 1, v149\n"
"v_lshl_add_u32 v241, v150, 3, s41\n"
"v_lshl_add_u32 v150, v146, 2, v149\n"
"s_add_u32 s44, s41, 516\n"
"v_lshl_add_u32 v245, v150, 2, s44\n"
"v_lshlrev_b32 v150, 4, %[tid]\n"
"v_add_u32 v2, 132484, v150\n"
"s_mul_i32 s41, s34, 24\n"
"s_lshl_b32 s44, s45, 3\n"
"s_add_u32 s41, s41, s44\n"
"v_lshrrev_b32 v150, 3, v1\n"
"v_add_u32 v150, s41, v150\n"
"v_and_b32 v151, 7, v1\n"
"v_mul_u32_u24 v7, 1152, v150\n"
"v_lshl_add_u32 v7, v151, 4, v7\n"
"v_lshrrev_b32 v152, 1, v151\n"
"v_mul_u32_u24 v152, 147456, v152\n"
"v_lshl_add_u32 v150, v150, 5, v152\n"
"v_and_b32 v151, 1, v151\n"
"v_lshl_add_u32 v150, v151, 4, v150\n"
"v_add_u32 v8, 33095680, v150\n"
"s_mul_i32 s41, s45, 2064\n"
"s_add_u32 s41, s41, 1024\n"
"v_mov_b32 v129, s41\n"
"v_lshlrev_b32 v9, 4, v1\n"
"v_add_u32 v9, 148448, v9\n"
"s_lshl_b32 s41, s45, 10\n"
"v_add_u32 v12, s41, v9\n"
"s_and_b32 s41, s42, 127\n"
"s_cmp_eq_u32 s41, 124\n"
"s_cbranch_scc0 FMB_%=\n"
"s_waitcnt vmcnt(5)\n"
"s_cmp_eq_u32 s36, 0\n"
"s_cbranch_scc1 FME_%=\n"
"v_mov_b32 v35, v34\n"
"v_mov_b32 v37, v36\n"
"v_mov_b32 v39, v38\n"
"v_mov_b32 v41, v40\n"
"v_mov_b32 v43, v42\n"
"v_mov_b32 v45, v44\n"
"v_mov_b32 v47, v46\n"
"v_mov_b32 v49, v48\n"
"s_branch FMB_%=\n"
"FME_%=:\n"
"v_mov_b32 v34, v35\n"
"v_mov_b32 v36, v37\n"
"v_mov_b32 v38, v39\n"
"v_mov_b32 v40, v41\n"
"v_mov_b32 v42, v43\n"
"v_mov_b32 v44, v45\n"
"v_mov_b32 v46, v47\n"
"v_mov_b32 v48, v49\n"
"FMB_%=:\n"
"s_cmp_eq_u32 s33, 3\n"
"s_cbranch_scc1 FV3_%=\n"
"s_waitcnt vmcnt(5)\n"
"FV3_%=:\n"
"s_waitcnt vmcnt(9)\n"
"v_alignbit_b32 v146, v35, v34, s36\n"
"v_lshrrev_b32 v147, s36, v35\n"
"v_lshlrev_b32 v182, 16, v146\n"
"v_and_b32 v190, 0xffff0000, v146\n"
"v_lshlrev_b32 v198, 16, v147\n"
"v_alignbit_b32 v146, v37, v36, s36\n"
"v_lshrrev_b32 v147, s36, v37\n"
"v_lshlrev_b32 v184, 16, v146\n"
"v_and_b32 v192, 0xffff0000, v146\n"
"v_lshlrev_b32 v200, 16, v147\n"
"v_alignbit_b32 v146, v39, v38, s36\n"
"v_lshrrev_b32 v147, s36, v39\n"
"v_lshlrev_b32 v186, 16, v146\n"
"v_and_b32 v194, 0xffff0000, v146\n"
"v_lshlrev_b32 v202, 16, v147\n"
"v_alignbit_b32 v146, v41, v40, s36\n"
"v_lshrrev_b32 v147, s36, v41\n"
"v_lshlrev_b32 v188, 16, v146\n"
"v_and_b32 v196, 0xffff0000, v146\n"
"v_lshlrev_b32 v204, 16, v147\n"
"v_alignbit_b32 v146, v43, v42, s36\n"
"v_lshrrev_b32 v147, s36, v43\n"
"v_lshlrev_b32 v183, 16, v146\n"
"v_and_b32 v191, 0xffff0000, v146\n"
"v_lshlrev_b32 v199, 16, v147\n"
"v_alignbit_b32 v146, v45, v44, s36\n"
"v_lshrrev_b32 v147, s36, v45\n"
"v_lshlrev_b32 v185, 16, v146\n"
"v_and_b32 v193, 0xffff0000, v146\n"
"v_lshlrev_b32 v201, 16, v147\n"
"v_alignbit_b32 v146, v47, v46, s36\n"
"v_lshrrev_b32 v147, s36, v47\n"
"v_lshlrev_b32 v187, 16, v146\n"
"v_and_b32 v195, 0xffff0000, v146\n"
"v_lshlrev_b32 v203, 16, v147\n"
"v_alignbit_b32 v146, v49, v48, s36\n"
"v_lshrrev_b32 v147, s36, v49\n"
"v_lshlrev_b32 v189, 16, v146\n"
"v_and_b32 v197, 0xffff0000, v146\n"
"v_lshlrev_b32 v205, 16, v147\n"
"s_mov_b32 s41, 0x07060302\n"
"v_perm_b32 v206, v184, v182, s41\n"
"v_perm_b32 v207, v188, v186, s41\n"
"v_perm_b32 v208, v185, v183, s41\n"
"v_perm_b32 v209, v189, v187, s41\n"
"ds_write_b128 v254, v[206:209]\n"
"v_perm_b32 v210, v192, v190, s41\n"
"v_perm_b32 v211, v196, v194, s41\n"
"v_perm_b32 v212, v193, v191, s41\n"
"v_perm_b32 v213, v197, v195, s41\n"
"ds_write_b128 v254, v[210:213] offset:4096\n"
"v_perm_b32 v214, v200, v198, s41\n"
"v_perm_b32 v215, v204, v202, s41\n"
"v_perm_b32 v216, v201, v199, s41\n"
"v_perm_b32 v217, v205, v203, s41\n"
"ds_write_b128 v254, v[214:217] offset:8192\n"
"s_waitcnt lgkmcnt(0)\n"
"s_barrier\n"
"s_cmp_eq_u32 s33, 3\n"
"s_cbranch_scc1 FW3_%=\n"
"s_add_u32 s30, s66, 147456\n"
"s_addc_u32 s31, s67, 0\n"
"global_load_dwordx4 a[212:215], v252, s[66:67]\n"
"global_load_dwordx4 a[216:219], v252, s[30:31]\n"
"global_load_dwordx4 a[220:223], v7, s[94:95] offset:1024\n"
"s_mov_b32 s56, 0\n"
"ds_read_b64 v[122:123], v239 offset:1792\n"
"ds_read_b64 v[120:121], v239 offset:1536\n"
"ds_read_b64 v[118:119], v239 offset:1280\n"
"ds_read_b64 v[116:117], v239 offset:1024\n"
"ds_read_b64 v[114:115], v239 offset:256\n"
"ds_read_b64 v[112:113], v239\n"
"ds_read_b64_tr_b16 a[0:1], v237 offset:288\n"
"ds_read_b64_tr_b16 a[2:3], v237 offset:256\n"
"ds_read_b64_tr_b16 a[4:5], v237 offset:800\n"
"ds_read_b64_tr_b16 a[6:7], v237 offset:768\n"
"ds_read_b64_tr_b16 a[8:9], v237 offset:32\n"
"ds_read_b64_tr_b16 a[10:11], v237\n"
"ds_read_b64_tr_b16 a[12:13], v237 offset:544\n"
"ds_read_b64_tr_b16 a[14:15], v237 offset:512\n"
"ds_read_b128 a[16:19], v9 offset:6144\n"
"ds_read_b128 a[20:23], v9 offset:7168\n"
"ds_read_b128 a[24:27], v9 offset:8192\n"
"ds_read_b128 a[28:31], v9 offset:9216\n"
"ds_read_b64_tr_b16 a[32:33], v237 offset:1344\n"
"ds_read_b64_tr_b16 a[34:35], v237 offset:1312\n"
"ds_read_b64_tr_b16 a[36:37], v237 offset:1856\n"
"ds_read_b64_tr_b16 a[38:39], v237 offset:1824\n"
"ds_read_b64_tr_b16 a[40:41], v237 offset:1088\n"
"ds_read_b64_tr_b16 a[42:43], v237 offset:1056\n"
"ds_read_b64_tr_b16 a[44:45], v237 offset:1600\n"
"ds_read_b64_tr_b16 a[46:47], v237 offset:1568\n"
"s_waitcnt lgkmcnt(14)\n"
"v_lshlrev_b32 v16, 16, v112\n"
"v_and_b32 v17, 0xffff0000, v112\n"
"v_lshlrev_b32 v18, 16, v113\n"
"v_and_b32 v19, 0xffff0000, v113\n"
"v_lshlrev_b32 v20, 16, v114\n"
"v_and_b32 v21, 0xffff0000, v114\n"
"v_lshlrev_b32 v22, 16, v115\n"
"v_and_b32 v23, 0xffff0000, v115\n"
"v_lshlrev_b32 v24, 16, v116\n"
"v_and_b32 v25, 0xffff0000, v116\n"
"v_lshlrev_b32 v26, 16, v117\n"
"v_and_b32 v27, 0xffff0000, v117\n"
"v_lshlrev_b32 v28, 16, v118\n"
"v_and_b32 v29, 0xffff0000, v118\n"
"v_lshlrev_b32 v30, 16, v119\n"
"v_and_b32 v31, 0xffff0000, v119\n"
"v_lshlrev_b32 v32, 16, v120\n"
"v_and_b32 v33, 0xffff0000, v120\n"
"v_lshlrev_b32 v34, 16, v121\n"
"v_and_b32 v35, 0xffff0000, v121\n"
"v_lshlrev_b32 v36, 16, v122\n"
"v_and_b32 v37, 0xffff0000, v122\n"
"v_lshlrev_b32 v38, 16, v123\n"
"v_and_b32 v39, 0xffff0000, v123\n"
"FCKA_%=:\n"
"s_waitcnt vmcnt(7)\n"
"v_bitop3_b32 v147, v164, v165, v167 bitop3:0x96\n"
"s_waitcnt vmcnt(6)\n"
"v_bitop3_b32 v148, v168, v169, v171 bitop3:0x96\n"
"s_waitcnt vmcnt(5)\n"
"v_bitop3_b32 v149, v172, v173, v175 bitop3:0x96\n"
"s_waitcnt vmcnt(4)\n"
"v_bitop3_b32 v150, v176, v177, v179 bitop3:0x96\n"
"s_waitcnt vmcnt(3)\n"
"v_xor_b32 v146, v181, v13\n"
"v_xor_b32 v147, v147, v13\n"
"v_bitop3_b32 v146, v146, v148, v13 bitop3:0xf6\n"
"v_bitop3_b32 v147, v147, v149, v13 bitop3:0xf6\n"
"v_bitop3_b32 v146, v146, v150, v13 bitop3:0xf6\n"
"v_or_b32 v146, v146, v147\n"
"v_cmp_ne_u32 vcc, 0, v146\n"
"s_nop 1\n"
"s_cbranch_vccnz FSLA_%=\n"
"FOKA_%=:\n"
"global_load_dword v14, v250, s[60:61]\n"
"s_and_b32 s41, s42, 127\n"
"s_cmp_eq_u32 s41, 124\n"
"s_cbranch_scc0 FMIA_%=\n"
"s_cmp_eq_u32 s36, 0\n"
"s_cbranch_scc1 FMHA_%=\n"
"v_mov_b32 v206, v166\n"
"v_mov_b32 v207, v170\n"
"v_mov_b32 v208, v174\n"
"v_mov_b32 v209, v178\n"
"ds_write_b128 v254, v[206:209] offset:0\n"
"v_lshlrev_b32 v182, 16, v166\n"
"v_and_b32 v184, 0xffff0000, v166\n"
"v_lshlrev_b32 v186, 16, v170\n"
"v_and_b32 v188, 0xffff0000, v170\n"
"v_lshlrev_b32 v183, 16, v174\n"
"v_and_b32 v185, 0xffff0000, v174\n"
"v_lshlrev_b32 v187, 16, v178\n"
"v_and_b32 v189, 0xffff0000, v178\n"
"s_branch FMIA_%=\n"
"FMHA_%=:\n"
"v_mov_b32 v206, v166\n"
"v_mov_b32 v207, v170\n"
"v_mov_b32 v208, v174\n"
"v_mov_b32 v209, v178\n"
"ds_write_b128 v254, v[206:209] offset:8192\n"
"v_lshlrev_b32 v198, 16, v166\n"
"v_and_b32 v200, 0xffff0000, v166\n"
"v_lshlrev_b32 v202, 16, v170\n"
"v_and_b32 v204, 0xffff0000, v170\n"
"v_lshlrev_b32 v199, 16, v174\n"
"v_and_b32 v201, 0xffff0000, v174\n"
"v_lshlrev_b32 v203, 16, v178\n"
"v_and_b32 v205, 0xffff0000, v178\n"
"FMIA_%=:\n"
"s_mov_b32 vcc_lo, 0x88888888\n"
"s_mov_b32 vcc_hi, 0x88888888\n"
"s_mov_b32 s58, 0xffff0000\n"
"s_mov_b32 s59, 0\n"
"v_and_b32 v218, 0xffff0000, v164\n"
"v_lshlrev_b32 v226, 16, v164\n"
"v_cndmask_b32 v180, v180, v15, vcc\n"
"v_rcp_f32 v218, v218\n"
"v_and_b32 v220, 0xffff0000, v165\n"
"v_lshlrev_b32 v228, 16, v165\n"
"v_rcp_f32 v220, v220\n"
"v_add_f32_dpp v180, v180, v180 quad_perm:[1,0,3,2] row_mask:0xf bank_mask:0xf\n"
"v_and_b32 v222, 0xffff0000, v168\n"
"v_lshlrev_b32 v230, 16, v168\n"
"v_rcp_f32 v222, v222\n"
"v_add_f32_dpp v180, v180, v180 quad_perm:[2,3,0,1] row_mask:0xf bank_mask:0xf\n"
"v_and_b32 v224, 0xffff0000, v169\n"
"v_lshlrev_b32 v232, 16, v169\n"
"v_rcp_f32 v224, v224\n"
"v_cvt_pk_bf16_f32 v180, v15, v180\n"
"v_add_f32 v181, 1.0, v180\n"
"v_and_b32 v219, 0xffff0000, v172\n"
"v_lshlrev_b32 v227, 16, v172\n"
"v_rcp_f32 v219, v219\n"
"v_cvt_pk_bf16_f32 v181, v15, v181\n"
"v_mov_b32 v148, v180\n"
"v_mov_b32 v149, v180\n"
"v_cndmask_b32 v150, v180, v181, s[58:59]\n"
"v_and_b32 v221, 0xffff0000, v173\n"
"v_lshlrev_b32 v229, 16, v173\n"
"v_rcp_f32 v221, v221\n"
"v_permlane32_swap_b32 v148, v149\n"
"v_and_b32 v223, 0xffff0000, v176\n"
"v_lshlrev_b32 v231, 16, v176\n"
"v_rcp_f32 v223, v223\n"
"v_mov_b32 v148, v149\n"
"v_and_b32 v225, 0xffff0000, v177\n"
"v_lshlrev_b32 v233, 16, v177\n"
"v_rcp_f32 v225, v225\n"
"v_permlane16_swap_b32 v149, v148\n"
"v_mul_f32 v150, v150, v149\n"
"v_pk_mul_f32 v[226:227], v[226:227], v[218:219] neg_lo:[1,0] neg_hi:[1,0]\n"
"v_pk_mul_f32 v[228:229], v[228:229], v[220:221] neg_lo:[1,0] neg_hi:[1,0]\n"
"v_pk_mul_f32 v[230:231], v[230:231], v[222:223] neg_lo:[1,0] neg_hi:[1,0]\n"
"v_pk_mul_f32 v[232:233], v[232:233], v[224:225] neg_lo:[1,0] neg_hi:[1,0]\n"
"v_readlane_b32 s44, v150, 16\n"
"v_readlane_b32 s45, v150, 0\n"
"v_readlane_b32 s46, v150, 20\n"
"v_readlane_b32 s47, v150, 4\n"
"v_readlane_b32 s48, v150, 24\n"
"v_readlane_b32 s49, v150, 8\n"
"s_lshl_b32 s41, s33, 8\n"
"s_add_u32 m0, s41, 43744\n"
"s_mov_b32 s64, 0x05040100\n"
"s_mov_b32 s65, 0x07060302\n"
"s_add_u32 s30, s41, 43744\n"
"s_add_u32 s31, s41, 6504\n"
"v_pk_fma_f32 v[182:183], v[182:183], v[218:219], v[226:227]\n"
"v_pk_fma_f32 v[184:185], v[184:185], v[220:221], v[228:229]\n"
"v_pk_fma_f32 v[186:187], v[186:187], v[222:223], v[230:231]\n"
"v_pk_fma_f32 v[188:189], v[188:189], v[224:225], v[232:233]\n"
"v_pk_fma_f32 v[182:183], v[182:183], s[44:45], s[44:45] op_sel:[0,0,1] op_sel_hi:[1,0,1]\n"
"v_pk_fma_f32 v[184:185], v[184:185], s[44:45], s[44:45] op_sel:[0,0,1] op_sel_hi:[1,0,1]\n"
"v_pk_fma_f32 v[186:187], v[186:187], s[44:45], s[44:45] op_sel:[0,0,1] op_sel_hi:[1,0,1]\n"
"v_pk_fma_f32 v[188:189], v[188:189], s[44:45], s[44:45] op_sel:[0,0,1] op_sel_hi:[1,0,1]\n"
"v_cvt_pk_bf16_f32 v182, v182, v183\n"
"v_cvt_pk_bf16_f32 v184, v184, v185\n"
"v_cvt_pk_bf16_f32 v186, v186, v187\n"
"v_cvt_pk_bf16_f32 v188, v188, v189\n"
"ds_write_addtid_b32 v182 offset:49152\n"
"v_perm_b32 v206, v184, v182, s64\n"
"ds_write_addtid_b32 v184 offset:50336\n"
"v_perm_b32 v207, v188, v186, s64\n"
"ds_write_addtid_b32 v186 offset:51520\n"
"v_perm_b32 v208, v184, v182, s65\n"
"ds_write_addtid_b32 v188 offset:52704\n"
"v_perm_b32 v209, v188, v186, s65\n"
"s_mov_b32 m0, s31\n"
"ds_write2_b32 v2, v206, v207 offset0:0 offset1:1\n"
"ds_write2_b32 v2, v208, v209 offset0:2 offset1:3\n"
"ds_write_addtid_b32 v182\n"
"ds_write_addtid_b32 v184 offset:1184\n"
"ds_write_addtid_b32 v186 offset:2368\n"
"ds_write_addtid_b32 v188 offset:3552\n"
"v_pk_fma_f32 v[190:191], v[190:191], v[218:219], v[226:227]\n"
"v_pk_fma_f32 v[192:193], v[192:193], v[220:221], v[228:229]\n"
"v_pk_fma_f32 v[194:195], v[194:195], v[222:223], v[230:231]\n"
"v_pk_fma_f32 v[196:197], v[196:197], v[224:225], v[232:233]\n"
"v_add_u32 v146, 4672, v2\n"
"s_mov_b32 m0, s30\n"
"v_pk_fma_f32 v[190:191], v[190:191], s[46:47], s[46:47] op_sel:[0,0,1] op_sel_hi:[1,0,1]\n"
"v_pk_fma_f32 v[192:193], v[192:193], s[46:47], s[46:47] op_sel:[0,0,1] op_sel_hi:[1,0,1]\n"
"v_pk_fma_f32 v[194:195], v[194:195], s[46:47], s[46:47] op_sel:[0,0,1] op_sel_hi:[1,0,1]\n"
"v_pk_fma_f32 v[196:197], v[196:197], s[46:47], s[46:47] op_sel:[0,0,1] op_sel_hi:[1,0,1]\n"
"v_cvt_pk_bf16_f32 v190, v190, v191\n"
"v_cvt_pk_bf16_f32 v192, v192, v193\n"
"v_cvt_pk_bf16_f32 v194, v194, v195\n"
"v_cvt_pk_bf16_f32 v196, v196, v197\n"
"ds_write_addtid_b32 v190 offset:53888\n"
"v_perm_b32 v210, v192, v190, s64\n"
"ds_write_addtid_b32 v192 offset:55072\n"
"v_perm_b32 v211, v196, v194, s64\n"
"ds_write_addtid_b32 v194 offset:56256\n"
"v_perm_b32 v212, v192, v190, s65\n"
"ds_write_addtid_b32 v196 offset:57440\n"
"v_perm_b32 v213, v196, v194, s65\n"
"s_mov_b32 m0, s31\n"
"ds_write2_b32 v146, v210, v211 offset0:0 offset1:1\n"
"ds_write2_b32 v146, v212, v213 offset0:2 offset1:3\n"
"ds_write_addtid_b32 v190 offset:4736\n"
"ds_write_addtid_b32 v192 offset:5920\n"
"ds_write_addtid_b32 v194 offset:7104\n"
"ds_write_addtid_b32 v196 offset:8288\n"
"v_pk_fma_f32 v[198:199], v[198:199], v[218:219], v[226:227]\n"
"v_pk_fma_f32 v[200:201], v[200:201], v[220:221], v[228:229]\n"
"v_pk_fma_f32 v[202:203], v[202:203], v[222:223], v[230:231]\n"
"v_pk_fma_f32 v[204:205], v[204:205], v[224:225], v[232:233]\n"
"v_add_u32 v147, 9344, v2\n"
"s_mov_b32 m0, s30\n"
"v_pk_fma_f32 v[198:199], v[198:199], s[48:49], s[48:49] op_sel:[0,0,1] op_sel_hi:[1,0,1]\n"
"v_pk_fma_f32 v[200:201], v[200:201], s[48:49], s[48:49] op_sel:[0,0,1] op_sel_hi:[1,0,1]\n"
"v_pk_fma_f32 v[202:203], v[202:203], s[48:49], s[48:49] op_sel:[0,0,1] op_sel_hi:[1,0,1]\n"
"v_pk_fma_f32 v[204:205], v[204:205], s[48:49], s[48:49] op_sel:[0,0,1] op_sel_hi:[1,0,1]\n"
"v_cvt_pk_bf16_f32 v198, v198, v199\n"
"v_cvt_pk_bf16_f32 v200, v200, v201\n"
"v_cvt_pk_bf16_f32 v202, v202, v203\n"
"v_cvt_pk_bf16_f32 v204, v204, v205\n"
"ds_write_addtid_b32 v198 offset:58624\n"
"v_perm_b32 v214, v200, v198, s64\n"
"ds_write_addtid_b32 v200 offset:59808\n"
"v_perm_b32 v215, v204, v202, s64\n"
"ds_write_addtid_b32 v202 offset:60992\n"
"v_perm_b32 v216, v200, v198, s65\n"
"ds_write_addtid_b32 v204 offset:62176\n"
"v_perm_b32 v217, v204, v202, s65\n"
"s_mov_b32 m0, s31\n"
"ds_write2_b32 v147, v214, v215 offset0:0 offset1:1\n"
"ds_write2_b32 v147, v216, v217 offset0:2 offset1:3\n"
"ds_write_addtid_b32 v198 offset:9472\n"
"ds_write_addtid_b32 v200 offset:10656\n"
"ds_write_addtid_b32 v202 offset:11840\n"
"ds_write_addtid_b32 v204 offset:13024\n"
"v_readlane_b32 s53, v181, 48\n"
"v_readlane_b32 s54, v181, 52\n"
"v_readlane_b32 s55, v181, 56\n"
"s_cmp_eq_u32 s33, 1\n"
"s_cselect_b32 s57, s54, s53\n"
"s_cmp_eq_u32 s33, 2\n"
"s_cselect_b32 s57, s55, s57\n"
"s_waitcnt lgkmcnt(0)\n"
"s_barrier\n"
"ds_read_b128 v[48:51], v238\n"
"ds_read_b128 v[52:55], v238 offset:64\n"
"ds_read_b128 v[56:59], v238 offset:128\n"
"ds_read_b128 v[60:63], v238 offset:192\n"
"s_and_b32 s41, s42, 127\n"
"s_cmp_eq_u32 s41, 124\n"
"s_cbranch_scc0 FMJ0_%=\n"
"ds_read_b64 v[112:113], v239\n"
"ds_read_b64 v[114:115], v239 offset:256\n"
"ds_read_b64 v[116:117], v239 offset:1024\n"
"ds_read_b64 v[118:119], v239 offset:1280\n"
"ds_read_b64 v[120:121], v239 offset:1536\n"
"ds_read_b64 v[122:123], v239 offset:1792\n"
"s_waitcnt lgkmcnt(0)\n"
"v_lshlrev_b32 v16, 16, v112\n"
"v_and_b32 v17, 0xffff0000, v112\n"
"v_lshlrev_b32 v18, 16, v113\n"
"v_and_b32 v19, 0xffff0000, v113\n"
"v_lshlrev_b32 v20, 16, v114\n"
"v_and_b32 v21, 0xffff0000, v114\n"
"v_lshlrev_b32 v22, 16, v115\n"
"v_and_b32 v23, 0xffff0000, v115\n"
"v_lshlrev_b32 v24, 16, v116\n"
"v_and_b32 v25, 0xffff0000, v116\n"
"v_lshlrev_b32 v26, 16, v117\n"
"v_and_b32 v27, 0xffff0000, v117\n"
"v_lshlrev_b32 v28, 16, v118\n"
"v_and_b32 v29, 0xffff0000, v118\n"
"v_lshlrev_b32 v30, 16, v119\n"
"v_and_b32 v31, 0xffff0000, v119\n"
"v_lshlrev_b32 v32, 16, v120\n"
"v_and_b32 v33, 0xffff0000, v120\n"
"v_lshlrev_b32 v34, 16, v121\n"
"v_and_b32 v35, 0xffff0000, v121\n"
"v_lshlrev_b32 v36, 16, v122\n"
"v_and_b32 v37, 0xffff0000, v122\n"
"v_lshlrev_b32 v38, 16, v123\n"
"v_and_b32 v39, 0xffff0000, v123\n"
"s_nop 1\n"
"FMJ0_%=:\n"
"s_waitcnt lgkmcnt(3)\n"
"v_mfma_f32_16x16x32_bf16 v[16:19], a[0:3], v[48:51], v[16:19]\n"
"ds_read_b128 v[64:67], v3\n"
"ds_read_b128 v[68:71], v238 offset:1184\n"
"v_mfma_f32_16x16x32_bf16 v[20:23], a[4:7], v[48:51], v[20:23]\n"
"ds_read_b128 v[72:75], v238 offset:1248\n"
"ds_read_b128 v[76:79], v238 offset:1312\n"
"s_waitcnt lgkmcnt(6)\n"
"v_mfma_f32_16x16x32_bf16 v[16:19], a[8:11], v[52:55], v[16:19]\n"
"ds_read_b64_tr_b16 a[48:49], v237 offset:2400\n"
"ds_read_b64_tr_b16 a[50:51], v237 offset:2368\n"
"ds_read_b64_tr_b16 a[76:77], v237 offset:3712\n"
"v_mfma_f32_16x16x32_bf16 v[20:23], a[12:15], v[52:55], v[20:23]\n"
"ds_read_b64_tr_b16 a[52:53], v237 offset:2912\n"
"ds_read_b64_tr_b16 a[78:79], v237 offset:3680\n"
"s_waitcnt lgkmcnt(10)\n"
"v_mfma_f32_16x16x32_bf16 v[20:23], a[0:3], v[56:59], v[20:23]\n"
"ds_read_b128 v[80:83], v238 offset:1376\n"
"ds_read_b128 v[84:87], v3 offset:512\n"
"s_waitcnt lgkmcnt(11)\n"
"v_mfma_f32_16x16x32_bf16 v[20:23], a[8:11], v[60:63], v[20:23]\n"
"ds_read_b64_tr_b16 a[54:55], v237 offset:2880\n"
"s_waitcnt lgkmcnt(10)\n"
"v_mfma_f32_16x16x32_bf16 v[188:191], v[64:67], a[16:19], 0\n"
"ds_read_b64_tr_b16 a[56:57], v237 offset:2144\n"
"ds_read_b64_tr_b16 a[58:59], v237 offset:2112\n"
"v_mfma_f32_16x16x32_bf16 v[192:195], v[64:67], a[20:23], 0\n"
"ds_read_b64_tr_b16 a[60:61], v237 offset:2656\n"
"ds_read_b64_tr_b16 a[62:63], v237 offset:2624\n"
"v_mfma_f32_16x16x32_bf16 v[196:199], v[64:67], a[24:27], 0\n"
"ds_read_b64_tr_b16 a[64:65], v237 offset:3456\n"
"ds_read_b64_tr_b16 a[66:67], v237 offset:3424\n"
"v_mfma_f32_16x16x32_bf16 v[200:203], v[64:67], a[28:31], 0\n"
"ds_read_b128 v[88:91], v238 offset:2368\n"
"ds_read_b128 v[92:95], v238 offset:2432\n"
"v_mfma_f32_16x16x32_bf16 v[16:19], a[32:35], v[68:71], v[16:19]\n"
"ds_read_b64_tr_b16 a[68:69], v237 offset:3968\n"
"ds_read_b64_tr_b16 a[70:71], v237 offset:3936\n"
"v_mfma_f32_16x16x32_bf16 v[20:23], a[36:39], v[68:71], v[20:23]\n"
"v_cvt_pk_bf16_f32 v96, v190, v194\n"
"s_waitcnt lgkmcnt(14)\n"
"v_mfma_f32_16x16x32_bf16 v[16:19], a[40:43], v[72:75], v[16:19]\n"
"v_cvt_pk_bf16_f32 v97, v198, v202\n"
"v_cvt_pk_bf16_f32 v98, v191, v195\n"
"v_mfma_f32_16x16x32_bf16 v[20:23], a[44:47], v[72:75], v[20:23]\n"
"ds_read_b128 v[48:51], v238 offset:2496\n"
"ds_read_b128 v[52:55], v238 offset:2560\n"
"v_mfma_f32_16x16x32_bf16 v[20:23], a[32:35], v[76:79], v[20:23]\n"
"v_cvt_pk_bf16_f32 v99, v199, v203\n"
"s_waitcnt lgkmcnt(13)\n"
"v_mfma_f32_16x16x32_bf16 v[20:23], a[40:43], v[80:83], v[20:23]\n"
"ds_read_b128 v[56:59], v3 offset:1024\n"
"ds_read_b128 v[60:63], v238 offset:3552\n"
"v_mfma_f32_16x16x32_bf16 v[204:207], v[84:87], a[16:19], 0\n"
"v_cvt_pk_bf16_f32 v100, v188, v192\n"
"v_cvt_pk_bf16_f32 v101, v196, v200\n"
"v_mfma_f32_16x16x32_bf16 v[208:211], v[84:87], a[20:23], 0\n"
"v_cvt_pk_bf16_f32 v102, v189, v193\n"
"v_cvt_pk_bf16_f32 v103, v197, v201\n"
"v_mfma_f32_16x16x32_bf16 v[212:215], v[84:87], a[24:27], 0\n"
"ds_read_b64_tr_b16 a[72:73], v237 offset:3200\n"
"ds_read_b64_tr_b16 a[74:75], v237 offset:3168\n"
"v_mfma_f32_16x16x32_bf16 v[216:219], v[84:87], a[28:31], 0\n"
"s_waitcnt lgkmcnt(8)\n"
"v_mfma_f32_16x16x32_bf16 v[16:19], a[48:51], v[88:91], v[16:19]\n"
"ds_read_b128 a[80:83], v12\n"
"v_mfma_f32_16x16x32_bf16 v[20:23], a[52:55], v[88:91], v[20:23]\n"
"ds_read_b128 v[64:67], v238 offset:3616\n"
"ds_read_b128 v[68:71], v238 offset:3680\n"
"v_mfma_f32_16x16x32_bf16 v[16:19], a[56:59], v[92:95], v[16:19]\n"
"v_cvt_pk_bf16_f32 v104, v206, v210\n"
"v_cvt_pk_bf16_f32 v105, v214, v218\n"
"v_mfma_f32_16x16x32_bf16 v[20:23], a[60:63], v[92:95], v[20:23]\n"
"v_cvt_pk_bf16_f32 v106, v207, v211\n"
"s_waitcnt lgkmcnt(7)\n"
"v_mfma_f32_16x16x32_bf16 v[20:23], a[48:51], v[48:51], v[20:23]\n"
"ds_read_b128 v[72:75], v238 offset:3744\n"
"ds_read_b128 v[76:79], v3 offset:1536\n"
"v_mfma_f32_16x16x32_bf16 v[20:23], a[56:59], v[52:55], v[20:23]\n"
"v_cvt_pk_bf16_f32 v107, v215, v219\n"
"s_waitcnt lgkmcnt(7)\n"
"v_mfma_f32_16x16x32_bf16 v[188:191], v[56:59], a[16:19], 0\n"
"v_cvt_pk_bf16_f32 v108, v204, v208\n"
"v_cvt_pk_bf16_f32 v109, v212, v216\n"
"v_mfma_f32_16x16x32_bf16 v[192:195], v[56:59], a[20:23], 0\n"
"v_cvt_pk_bf16_f32 v110, v205, v209\n"
"v_cvt_pk_bf16_f32 v111, v213, v217\n"
"v_mfma_f32_16x16x32_bf16 v[196:199], v[56:59], a[24:27], 0\n"
"ds_read_b128 a[84:87], v9 offset:10240\n"
"ds_read_b128 a[88:91], v9 offset:11264\n"
"v_mfma_f32_16x16x32_bf16 v[200:203], v[56:59], a[28:31], 0\n"
"ds_read_b128 v[80:83], v10 offset:512\n"
"ds_read2_b32 v[84:85], v235 offset0:0 offset1:1\n"
"v_mfma_f32_16x16x32_bf16 v[16:19], a[64:67], v[60:63], v[16:19]\n"
"ds_read2_b32 v[86:87], v235 offset0:2 offset1:3\n"
"v_mfma_f32_16x16x32_bf16 v[20:23], a[68:71], v[60:63], v[20:23]\n"
"v_cvt_pk_bf16_f32 v220, v190, v194\n"
"s_waitcnt lgkmcnt(7)\n"
"v_mfma_f32_16x16x32_bf16 v[16:19], a[72:75], v[64:67], v[16:19]\n"
"ds_read_b128 v[88:91], v238 offset:384\n"
"ds_read_b128 v[92:95], v238 offset:448\n"
"v_mfma_f32_16x16x32_bf16 v[20:23], a[76:79], v[64:67], v[20:23]\n"
"v_cvt_pk_bf16_f32 v221, v198, v202\n"
"v_cvt_pk_bf16_f32 v222, v191, v195\n"
"ds_read_b64 a[96:97], v241 offset:256\n"
"v_mfma_f32_16x16x32_bf16 v[20:23], a[64:67], v[68:71], v[20:23]\n"
"ds_read_b128 v[48:51], v238 offset:512\n"
"ds_read_b64 a[98:99], v241 offset:264\n"
"s_waitcnt lgkmcnt(10)\n"
"v_mfma_f32_16x16x32_bf16 v[20:23], a[72:75], v[72:75], v[20:23]\n"
"ds_read_b128 v[52:55], v238 offset:576\n"
"v_cvt_pk_bf16_f32 v223, v199, v203\n"
"ds_read_b64 a[100:101], v241 offset:192\n"
"v_mfma_f32_16x16x32_bf16 v[204:207], v[76:79], a[16:19], 0\n"
"v_cvt_pk_bf16_f32 v224, v188, v192\n"
"v_cvt_pk_bf16_f32 v225, v196, v200\n"
"ds_read_b64 a[102:103], v241 offset:200\n"
"v_mfma_f32_16x16x32_bf16 v[208:211], v[76:79], a[20:23], 0\n"
"v_cvt_pk_bf16_f32 v226, v189, v193\n"
"v_cvt_pk_bf16_f32 v227, v197, v201\n"
"v_mfma_f32_16x16x32_bf16 v[212:215], v[76:79], a[24:27], 0\n"
"ds_read_b128 v[56:59], v238 offset:640\n"
"ds_read_b128 v[60:63], v238 offset:704\n"
"v_mfma_f32_16x16x32_bf16 v[216:219], v[76:79], a[28:31], 0\n"
"s_waitcnt lgkmcnt(10)\n"
"v_mfma_f32_16x16x32_bf16 v[20:23], a[80:83], v[80:83], v[20:23]\n"
"ds_read2_b32 v[64:65], v235 offset0:128 offset1:129\n"
"ds_read2_b32 v[66:67], v235 offset0:130 offset1:131\n"
"v_mfma_f32_16x16x32_bf16 v[188:191], v[84:87], a[84:87], 0\n"
"ds_read_b128 v[68:71], v238 offset:1568\n"
"v_cvt_pk_bf16_f32 v228, v206, v210\n"
"v_mfma_f32_16x16x32_bf16 v[192:195], v[84:87], a[88:91], 0\n"
"v_cvt_pk_bf16_f32 v229, v214, v218\n"
"v_mul_f32 v160, s57, v16\n"
"s_waitcnt lgkmcnt(11)\n"
"v_mfma_f32_16x16x32_bf16 v[24:27], a[4:7], v[88:91], v[24:27]\n"
"ds_read_b128 v[72:75], v238 offset:1632\n"
"ds_read_b128 v[76:79], v238 offset:1696\n"
"v_mfma_f32_16x16x32_bf16 v[24:27], a[12:15], v[92:95], v[24:27]\n"
"v_cvt_pk_bf16_f32 v230, v207, v211\n"
"v_mul_f32 v161, s57, v17\n"
"s_waitcnt lgkmcnt(9)\n"
"v_mfma_f32_16x16x32_bf16 v[24:27], a[0:3], v[48:51], v[24:27]\n"
"v_cvt_pk_bf16_f32 v231, v215, v219\n"
"v_cvt_pk_bf16_f32 v40, v204, v208\n"
"v_mfma_f32_16x16x32_bf16 v[28:31], a[4:7], v[48:51], v[28:31]\n"
"ds_read_b128 v[80:83], v238 offset:1760\n"
"ds_read_b128 v[84:87], v238 offset:1824\n"
"v_mul_f32 v162, s57, v18\n"
"v_mfma_f32_16x16x32_bf16 v[24:27], a[8:11], v[52:55], v[24:27]\n"
"v_cvt_pk_bf16_f32 v41, v212, v216\n"
"v_cvt_pk_bf16_f32 v42, v205, v209\n"
"v_mfma_f32_16x16x32_bf16 v[28:31], a[12:15], v[52:55], v[28:31]\n"
"v_cvt_pk_bf16_f32 v43, v213, v217\n"
"v_mul_f32 v163, s57, v19\n"
"s_waitcnt lgkmcnt(7)\n"
"v_mfma_f32_16x16x32_bf16 v[28:31], a[0:3], v[56:59], v[28:31]\n"
"ds_read_b128 v[88:91], v238 offset:1888\n"
"ds_read_b128 v[92:95], v3 offset:2048\n"
"v_mfma_f32_16x16x32_bf16 v[28:31], a[8:11], v[60:63], v[28:31]\n"
"v_cvt_pk_bf16_f32 v44, v188, v192\n"
"v_mul_f32 v164, s57, v20\n"
"s_waitcnt lgkmcnt(6)\n"
"v_mfma_f32_16x16x32_bf16 v[204:207], v[64:67], a[84:87], 0\n"
"v_cvt_pk_bf16_f32 v45, v189, v193\n"
"v_cvt_pk_bf16_f32 v46, v190, v194\n"
"v_mfma_f32_16x16x32_bf16 v[208:211], v[64:67], a[88:91], 0\n"
"v_cvt_pk_bf16_f32 v47, v191, v195\n"
"ds_read_b64 a[92:93], v241 offset:320\n"
"v_mul_f32 v165, s57, v21\n"
"v_mfma_f32_16x16x32_bf16 v[24:27], a[36:39], v[68:71], v[24:27]\n"
"ds_read_b64 a[94:95], v241 offset:328\n"
"s_waitcnt lgkmcnt(6)\n"
"v_mfma_f32_16x16x32_bf16 v[24:27], a[44:47], v[72:75], v[24:27]\n"
"s_nop 0\n"
"v_cvt_pk_bf16_f32 v136, v204, v208\n"
"v_cvt_pk_bf16_f32 v137, v205, v209\n"
"v_mfma_f32_16x16x32_bf16 v[24:27], a[32:35], v[76:79], v[24:27]\n"
"v_cvt_pk_bf16_f32 v138, v206, v210\n"
"v_cvt_pk_bf16_f32 v139, v207, v211\n"
"v_mul_f32 v166, s57, v22\n"
"v_mfma_f32_16x16x32_bf16 v[28:31], a[36:39], v[76:79], v[28:31]\n"
"ds_read_b64 a[104:105], v241 offset:128\n"
"s_waitcnt lgkmcnt(5)\n"
"v_mfma_f32_16x16x32_bf16 v[24:27], a[40:43], v[80:83], v[24:27]\n"
"ds_read_b64 a[106:107], v241 offset:136\n"
"ds_read_b64 a[108:109], v241 offset:64\n"
"v_mul_f32 v167, s57, v23\n"
"v_mfma_f32_16x16x32_bf16 v[28:31], a[44:47], v[80:83], v[28:31]\n"
"ds_read_b128 v[124:127], v9 offset:3072\n"
"ds_read_u16 v128, v129\n"
"v_mfma_f32_16x16x32_bf16 v[28:31], a[32:35], v[84:87], v[28:31]\n"
"ds_read_b64 a[110:111], v241 offset:72\n"
"v_cvt_pk_bf16_f32 v170, v160, v161\n"
"s_waitcnt lgkmcnt(8)\n"
"v_mfma_f32_16x16x32_bf16 v[28:31], a[40:43], v[88:91], v[28:31]\n"
"ds_read_b64 a[112:113], v241\n"
"s_waitcnt lgkmcnt(7)\n"
"v_mfma_f32_16x16x32_bf16 v[24:27], v[96:99], a[92:95], v[24:27]\n"
"ds_read_b64 a[114:115], v241 offset:8\n"
"v_cvt_pk_bf16_f32 v171, v162, v163\n"
"v_mfma_f32_16x16x32_bf16 v[24:27], v[100:103], a[96:99], v[24:27]\n"
"ds_read2_b32 a[116:117], v245 offset0:0 offset1:1\n"
"v_mfma_f32_16x16x32_bf16 v[24:27], v[104:107], a[100:103], v[24:27]\n"
"ds_read2_b32 a[118:119], v245 offset0:2 offset1:3\n"
"ds_read2_b32 a[120:121], v245 offset0:16 offset1:17\n"
"v_cvt_pk_bf16_f32 v172, v164, v165\n"
"v_mfma_f32_16x16x32_bf16 v[28:31], v[104:107], a[92:95], v[28:31]\n"
"ds_read2_b32 a[122:123], v245 offset0:18 offset1:19\n"
"s_waitcnt lgkmcnt(10)\n"
"v_mfma_f32_16x16x32_bf16 v[24:27], v[108:111], a[104:107], v[24:27]\n"
"v_cvt_pk_bf16_f32 v173, v166, v167\n"
"v_mfma_f32_16x16x32_bf16 v[28:31], v[108:111], a[96:99], v[28:31]\n"
"v_add_u32 v174, 0, v244\n"
"s_waitcnt lgkmcnt(6)\n"
"v_mfma_f32_16x16x32_bf16 v[24:27], v[220:223], a[108:111], v[24:27]\n"
"v_permlane16_swap_b32 v170, v172\n"
"v_permlane16_swap_b32 v171, v173\n"
"v_mfma_f32_16x16x32_bf16 v[28:31], v[220:223], a[100:103], v[28:31]\n"
"global_store_dwordx4 v174, v[170:173], s[96:97] sc1\n"
"s_waitcnt lgkmcnt(4)\n"
"v_mfma_f32_16x16x32_bf16 v[24:27], v[224:227], a[112:115], v[24:27]\n"
"ds_read_b128 v[48:51], v238 offset:2752\n"
"ds_read_b128 v[52:55], v238 offset:2816\n"
"v_mfma_f32_16x16x32_bf16 v[28:31], v[224:227], a[104:107], v[28:31]\n"
"v_cvt_pk_bf16_f32 v140, v16, v17\n"
"v_cvt_pk_bf16_f32 v141, v18, v19\n"
"v_mfma_f32_16x16x32_bf16 v[28:31], v[228:231], a[108:111], v[28:31]\n"
"ds_read_b128 v[64:67], v238 offset:2880\n"
"ds_read_b128 v[68:71], v238 offset:2944\n"
"v_mfma_f32_16x16x32_bf16 v[28:31], v[40:43], a[112:115], v[28:31]\n"
"v_cvt_pk_bf16_f32 v142, v20, v21\n"
"s_waitcnt lgkmcnt(6)\n"
"v_mfma_f32_16x16x32_bf16 v[24:27], v[44:47], a[116:119], v[24:27]\n"
"v_cvt_pk_bf16_f32 v143, v22, v23\n"
"s_waitcnt lgkmcnt(4)\n"
"v_mfma_f32_16x16x32_bf16 v[28:31], v[44:47], a[120:123], v[28:31]\n"
"v_add_u32 v144, 0, v240\n"
"v_mfma_f32_16x16x32_bf16 v[28:31], v[136:139], a[116:119], v[28:31]\n"
"ds_read_b128 v[72:75], v238 offset:3008\n"
"ds_read_b128 v[76:79], v238 offset:3072\n"
"v_permlane16_swap_b32 v140, v142\n"
"v_mfma_f32_16x16x32_bf16 v[188:191], v[92:95], a[16:19], 0\n"
"v_lshl_or_b32 v128, v128, 16, v128\n"
"v_and_b32 v124, v128, v124\n"
"v_mfma_f32_16x16x32_bf16 v[192:195], v[92:95], a[20:23], 0\n"
"ds_read_b128 v[80:83], v10 offset:2048\n"
"v_and_b32 v125, v128, v125\n"
"v_permlane16_swap_b32 v141, v143\n"
"v_mfma_f32_16x16x32_bf16 v[196:199], v[92:95], a[24:27], 0\n"
"v_and_b32 v126, v128, v126\n"
"v_and_b32 v127, v128, v127\n"
"v_mfma_f32_16x16x32_bf16 v[200:203], v[92:95], a[28:31], 0\n"
"ds_read_b128 v[92:95], v10 offset:992\n"
"global_store_dwordx4 v144, v[140:143], s[96:97] sc1\n"
"s_waitcnt lgkmcnt(6)\n"
"v_mfma_f32_16x16x32_bf16 v[24:27], a[52:55], v[48:51], v[24:27]\n"
"ds_read_b128 v[48:51], v10 offset:2560\n"
"v_mfma_f32_16x16x32_bf16 v[24:27], a[60:63], v[52:55], v[24:27]\n"
"ds_read_b128 v[52:55], v10 offset:1504\n"
"s_waitcnt lgkmcnt(6)\n"
"v_mfma_f32_16x16x32_bf16 v[24:27], a[48:51], v[64:67], v[24:27]\n"
"v_cvt_pk_bf16_f32 v4, v190, v194\n"
"v_cvt_pk_bf16_f32 v5, v198, v202\n"
"v_mfma_f32_16x16x32_bf16 v[28:31], a[52:55], v[64:67], v[28:31]\n"
"ds_read2_b32 v[64:65], v11 offset0:0 offset1:1\n"
"ds_read2_b32 v[66:67], v11 offset0:2 offset1:3\n"
"v_mfma_f32_16x16x32_bf16 v[24:27], a[56:59], v[68:71], v[24:27]\n"
"v_cvt_pk_bf16_f32 v6, v191, v195\n"
"v_cvt_pk_bf16_f32 v7, v199, v203\n"
"v_mfma_f32_16x16x32_bf16 v[28:31], a[60:63], v[68:71], v[28:31]\n"
"ds_read_b128 v[68:71], v238 offset:3936\n"
"s_waitcnt lgkmcnt(7)\n"
"v_mfma_f32_16x16x32_bf16 v[28:31], a[48:51], v[72:75], v[28:31]\n"
"v_cvt_pk_bf16_f32 v112, v188, v192\n"
"v_cvt_pk_bf16_f32 v113, v196, v200\n"
"v_mfma_f32_16x16x32_bf16 v[28:31], a[56:59], v[76:79], v[28:31]\n"
"v_cvt_pk_bf16_f32 v114, v189, v193\n"
"s_waitcnt lgkmcnt(5)\n"
"v_mfma_f32_16x16x32_bf16 v[24:27], a[80:83], v[80:83], v[24:27]\n"
"ds_read_b128 v[80:83], v238 offset:4000\n"
"v_cvt_pk_bf16_f32 v115, v197, v201\n"
"v_mfma_f32_16x16x32_bf16 v[24:27], v[124:127], v[92:95], v[24:27]\n"
"ds_read_b128 v[92:95], v238 offset:4064\n"
"s_waitcnt lgkmcnt(5)\n"
"v_mfma_f32_16x16x32_bf16 v[28:31], a[80:83], v[48:51], v[28:31]\n"
"ds_read_b128 v[48:51], v238 offset:4128\n"
"v_mfma_f32_16x16x32_bf16 v[28:31], v[124:127], v[52:55], v[28:31]\n"
"ds_read_b128 v[52:55], v238 offset:4192\n"
"s_waitcnt lgkmcnt(4)\n"
"v_mfma_f32_16x16x32_bf16 v[204:207], v[64:67], a[84:87], 0\n"
"v_mfma_f32_16x16x32_bf16 v[208:211], v[64:67], a[88:91], 0\n"
"ds_read_b128 v[64:67], v238 offset:4256\n"
"v_mfma_f32_16x16x32_bf16 v[24:27], a[68:71], v[68:71], v[24:27]\n"
"ds_read_b128 v[68:71], v3 offset:2560\n"
"s_waitcnt lgkmcnt(4)\n"
"v_mfma_f32_16x16x32_bf16 v[24:27], a[76:79], v[80:83], v[24:27]\n"
"s_nop 2\n"
"v_cvt_pk_bf16_f32 v96, v204, v208\n"
"v_cvt_pk_bf16_f32 v97, v205, v209\n"
"v_mfma_f32_16x16x32_bf16 v[24:27], a[64:67], v[92:95], v[24:27]\n"
"v_cvt_pk_bf16_f32 v98, v206, v210\n"
"v_cvt_pk_bf16_f32 v99, v207, v211\n"
"v_mfma_f32_16x16x32_bf16 v[28:31], a[68:71], v[92:95], v[28:31]\n"
"ds_read_b128 v[80:83], v238 offset:768\n"
"s_waitcnt lgkmcnt(3)\n"
"v_mfma_f32_16x16x32_bf16 v[24:27], a[72:75], v[48:51], v[24:27]\n"
"ds_read_b128 v[92:95], v238 offset:832\n"
"v_mfma_f32_16x16x32_bf16 v[28:31], a[76:79], v[48:51], v[28:31]\n"
"v_mfma_f32_16x16x32_bf16 v[28:31], a[64:67], v[52:55], v[28:31]\n"
"s_waitcnt lgkmcnt(2)\n"
"v_mfma_f32_16x16x32_bf16 v[28:31], a[72:75], v[64:67], v[28:31]\n"
"ds_read_b128 v[48:51], v238 offset:896\n"
"v_mfma_f32_16x16x32_bf16 v[188:191], v[68:71], a[16:19], 0\n"
"v_mfma_f32_16x16x32_bf16 v[192:195], v[68:71], a[20:23], 0\n"
"v_mul_f32 v160, s57, v24\n"
"v_mul_f32 v161, s57, v25\n"
"v_mfma_f32_16x16x32_bf16 v[196:199], v[68:71], a[24:27], 0\n"
"v_mul_f32 v162, s57, v26\n"
"v_mfma_f32_16x16x32_bf16 v[200:203], v[68:71], a[28:31], 0\n"
"ds_read_b128 v[68:71], v238 offset:960\n"
"v_mul_f32 v163, s57, v27\n"
"v_mfma_f32_16x16x32_bf16 v[32:35], a[4:7], v[56:59], v[32:35]\n"
"ds_read2_b32 v[56:57], v11 offset0:128 offset1:129\n"
"ds_read2_b32 v[58:59], v11 offset0:130 offset1:131\n"
"v_mfma_f32_16x16x32_bf16 v[32:35], a[12:15], v[60:63], v[32:35]\n"
"ds_read_b128 v[60:63], v238 offset:1952\n"
"s_waitcnt lgkmcnt(5)\n"
"v_mfma_f32_16x16x32_bf16 v[32:35], a[0:3], v[80:83], v[32:35]\n"
"v_cvt_pk_bf16_f32 v116, v190, v194\n"
"v_cvt_pk_bf16_f32 v117, v198, v202\n"
"v_mfma_f32_16x16x32_bf16 v[36:39], a[4:7], v[80:83], v[36:39]\n"
"ds_read_b128 v[80:83], v238 offset:2016\n"
"v_cvt_pk_bf16_f32 v118, v191, v195\n"
"v_mfma_f32_16x16x32_bf16 v[32:35], a[8:11], v[92:95], v[32:35]\n"
"v_cvt_pk_bf16_f32 v119, v199, v203\n"
"v_cvt_pk_bf16_f32 v120, v188, v192\n"
"v_mfma_f32_16x16x32_bf16 v[36:39], a[12:15], v[92:95], v[36:39]\n"
"ds_read_b128 v[92:95], v238 offset:2080\n"
"s_waitcnt lgkmcnt(5)\n"
"v_mfma_f32_16x16x32_bf16 v[36:39], a[0:3], v[48:51], v[36:39]\n"
"ds_read_b128 v[48:51], v238 offset:2144\n"
"v_cvt_pk_bf16_f32 v121, v196, v200\n"
"v_mfma_f32_16x16x32_bf16 v[36:39], a[8:11], v[68:71], v[36:39]\n"
"ds_read_b128 v[68:71], v10 offset:3072\n"
"s_waitcnt lgkmcnt(4)\n"
"v_mfma_f32_16x16x32_bf16 v[204:207], v[56:59], a[84:87], 0\n"
"v_cvt_pk_bf16_f32 v122, v189, v193\n"
"v_cvt_pk_bf16_f32 v123, v197, v201\n"
"v_mfma_f32_16x16x32_bf16 v[208:211], v[56:59], a[88:91], 0\n"
"ds_read_b128 v[56:59], v10 offset:2016\n"
"s_waitcnt vmcnt(5)\n"
"global_store_dwordx4 v251, a[212:215], s[96:97] sc1\n"
"v_mfma_f32_16x16x32_bf16 v[32:35], a[36:39], v[84:87], v[32:35]\n"
"ds_read_b128 v[84:87], v10 offset:3584\n"
"v_mul_f32 v164, s57, v28\n"
"v_mfma_f32_16x16x32_bf16 v[32:35], a[44:47], v[88:91], v[32:35]\n"
"ds_read_b128 v[88:91], v10 offset:2528\n"
"v_cvt_pk_bf16_f32 v100, v204, v208\n"
"v_mfma_f32_16x16x32_bf16 v[32:35], a[32:35], v[60:63], v[32:35]\n"
"v_cvt_pk_bf16_f32 v101, v205, v209\n"
"v_cvt_pk_bf16_f32 v102, v206, v210\n"
"v_mfma_f32_16x16x32_bf16 v[36:39], a[36:39], v[60:63], v[36:39]\n"
"ds_read_b128 v[60:63], v238 offset:3136\n"
"s_waitcnt lgkmcnt(6)\n"
"v_mfma_f32_16x16x32_bf16 v[32:35], a[40:43], v[80:83], v[32:35]\n"
"v_cvt_pk_bf16_f32 v103, v207, v211\n"
"ds_read2_b32 a[124:125], v245 offset0:32 offset1:33\n"
"v_mfma_f32_16x16x32_bf16 v[36:39], a[44:47], v[80:83], v[36:39]\n"
"ds_read_b128 v[80:83], v238 offset:3200\n"
"ds_read2_b32 a[126:127], v245 offset0:34 offset1:35\n"
"v_mfma_f32_16x16x32_bf16 v[36:39], a[32:35], v[92:95], v[36:39]\n"
"ds_read_b128 v[92:95], v238 offset:3264\n"
"s_waitcnt lgkmcnt(8)\n"
"v_mfma_f32_16x16x32_bf16 v[36:39], a[40:43], v[48:51], v[36:39]\n"
"ds_read2_b32 a[128:129], v245 offset0:48 offset1:49\n"
"ds_read2_b32 a[130:131], v245 offset0:50 offset1:51\n"
"v_mfma_f32_16x16x32_bf16 v[32:35], a[80:83], v[68:71], v[32:35]\n"
"ds_read_b128 v[48:51], v238 offset:3328\n"
"s_waitcnt lgkmcnt(9)\n"
"v_mfma_f32_16x16x32_bf16 v[32:35], v[124:127], v[56:59], v[32:35]\n"
"ds_read_b128 v[68:71], v238 offset:4320\n"
"v_mul_f32 v165, s57, v29\n"
"v_mfma_f32_16x16x32_bf16 v[36:39], a[80:83], v[84:87], v[36:39]\n"
"s_waitcnt vmcnt(5)\n"
"global_store_dwordx4 v251, a[216:219], s[96:97] offset:1024 sc1\n"
"s_waitcnt lgkmcnt(8)\n"
"v_mfma_f32_16x16x32_bf16 v[36:39], v[124:127], v[88:91], v[36:39]\n"
"v_mul_f32 v166, s57, v30\n"
"v_mul_f32 v167, s57, v31\n"
"v_mfma_f32_16x16x32_bf16 v[32:35], a[52:55], v[72:75], v[32:35]\n"
"v_cvt_pk_bf16_f32 v176, v160, v161\n"
"v_mfma_f32_16x16x32_bf16 v[32:35], a[60:63], v[76:79], v[32:35]\n"
"v_cvt_pk_bf16_f32 v177, v162, v163\n"
"v_mfma_f32_16x16x32_bf16 v[32:35], a[48:51], v[60:63], v[32:35]\n"
"v_cvt_pk_bf16_f32 v178, v164, v165\n"
"v_cvt_pk_bf16_f32 v179, v166, v167\n"
"v_mfma_f32_16x16x32_bf16 v[36:39], a[52:55], v[60:63], v[36:39]\n"
"s_waitcnt lgkmcnt(4)\n"
"v_mfma_f32_16x16x32_bf16 v[32:35], a[56:59], v[80:83], v[32:35]\n"
"v_add_u32 v180, 1179648, v244\n"
"v_mfma_f32_16x16x32_bf16 v[36:39], a[60:63], v[80:83], v[36:39]\n"
"v_permlane16_swap_b32 v176, v178\n"
"v_permlane16_swap_b32 v177, v179\n"
"v_mfma_f32_16x16x32_bf16 v[36:39], a[48:51], v[92:95], v[36:39]\n"
"s_waitcnt vmcnt(5)\n"
"global_store_dwordx4 v8, a[220:223], s[96:97] sc1\n"
"s_waitcnt lgkmcnt(0)\n"
"v_mfma_f32_16x16x32_bf16 v[36:39], a[56:59], v[48:51], v[36:39]\n"
"global_store_dwordx4 v180, v[176:179], s[96:97] sc1\n"
"v_cvt_pk_bf16_f32 v146, v24, v25\n"
"v_mfma_f32_16x16x32_bf16 v[32:35], v[220:223], a[92:95], v[32:35]\n"
"v_cvt_pk_bf16_f32 v147, v26, v27\n"
"v_mfma_f32_16x16x32_bf16 v[32:35], v[224:227], a[96:99], v[32:35]\n"
"v_cvt_pk_bf16_f32 v148, v28, v29\n"
"v_mfma_f32_16x16x32_bf16 v[32:35], v[228:231], a[100:103], v[32:35]\n"
"v_cvt_pk_bf16_f32 v149, v30, v31\n"
"v_add_u32 v150, 1179648, v240\n"
"v_mfma_f32_16x16x32_bf16 v[36:39], v[228:231], a[92:95], v[36:39]\n"
"v_permlane16_swap_b32 v146, v148\n"
"v_mfma_f32_16x16x32_bf16 v[32:35], v[40:43], a[104:107], v[32:35]\n"
"v_permlane16_swap_b32 v147, v149\n"
"v_mfma_f32_16x16x32_bf16 v[36:39], v[40:43], a[96:99], v[36:39]\n"
"global_store_dwordx4 v150, v[146:149], s[96:97] sc1\n"
"v_mfma_f32_16x16x32_bf16 v[32:35], v[4:7], a[108:111], v[32:35]\n"
"v_mfma_f32_16x16x32_bf16 v[36:39], v[4:7], a[100:103], v[36:39]\n"
"v_mfma_f32_16x16x32_bf16 v[32:35], v[112:115], a[112:115], v[32:35]\n"
"v_mfma_f32_16x16x32_bf16 v[36:39], v[112:115], a[104:107], v[36:39]\n"
"v_mfma_f32_16x16x32_bf16 v[36:39], v[116:119], a[108:111], v[36:39]\n"
"v_mfma_f32_16x16x32_bf16 v[36:39], v[120:123], a[112:115], v[36:39]\n"
"v_mfma_f32_16x16x32_bf16 v[32:35], v[44:47], a[124:127], v[32:35]\n"
"ds_read_b128 v[56:59], v238 offset:4384\n"
"ds_read_b128 v[84:87], v238 offset:4448\n"
"v_mfma_f32_16x16x32_bf16 v[36:39], v[44:47], a[128:131], v[36:39]\n"
"v_mfma_f32_16x16x32_bf16 v[32:35], v[136:139], a[120:123], v[32:35]\n"
"v_mfma_f32_16x16x32_bf16 v[36:39], v[136:139], a[124:127], v[36:39]\n"
"ds_read_b128 v[88:91], v238 offset:4512\n"
"v_mfma_f32_16x16x32_bf16 v[32:35], v[96:99], a[116:119], v[32:35]\n"
"v_mfma_f32_16x16x32_bf16 v[36:39], v[96:99], a[120:123], v[36:39]\n"
"v_mfma_f32_16x16x32_bf16 v[36:39], v[100:103], a[116:119], v[36:39]\n"
"v_mfma_f32_16x16x32_bf16 v[32:35], a[68:71], v[52:55], v[32:35]\n"
"v_mfma_f32_16x16x32_bf16 v[32:35], a[76:79], v[64:67], v[32:35]\n"
"v_mfma_f32_16x16x32_bf16 v[32:35], a[64:67], v[68:71], v[32:35]\n"
"v_mfma_f32_16x16x32_bf16 v[36:39], a[68:71], v[68:71], v[36:39]\n"
"s_waitcnt lgkmcnt(1)\n"
"v_mfma_f32_16x16x32_bf16 v[32:35], a[72:75], v[56:59], v[32:35]\n"
"v_mfma_f32_16x16x32_bf16 v[36:39], a[76:79], v[56:59], v[36:39]\n"
"v_mfma_f32_16x16x32_bf16 v[36:39], a[64:67], v[84:87], v[36:39]\n"
"s_waitcnt lgkmcnt(0)\n"
"v_mfma_f32_16x16x32_bf16 v[36:39], a[72:75], v[88:91], v[36:39]\n"
"s_nop 3\n"
"v_mul_f32 v160, s57, v32\n"
"v_mul_f32 v161, s57, v33\n"
"v_mul_f32 v162, s57, v34\n"
"v_mul_f32 v163, s57, v35\n"
"v_mul_f32 v164, s57, v36\n"
"v_mul_f32 v165, s57, v37\n"
"v_mul_f32 v166, s57, v38\n"
"v_mul_f32 v167, s57, v39\n"
"v_cvt_pk_bf16_f32 v182, v160, v161\n"
"v_cvt_pk_bf16_f32 v183, v162, v163\n"
"v_cvt_pk_bf16_f32 v184, v164, v165\n"
"v_cvt_pk_bf16_f32 v185, v166, v167\n"
"v_add_u32 v186, 1769472, v244\n"
"v_permlane16_swap_b32 v182, v184\n"
"v_permlane16_swap_b32 v183, v185\n"
"global_store_dwordx4 v186, v[182:185], s[96:97] sc1\n"
"v_cvt_pk_bf16_f32 v152, v32, v33\n"
"v_cvt_pk_bf16_f32 v153, v34, v35\n"
"v_cvt_pk_bf16_f32 v154, v36, v37\n"
"v_cvt_pk_bf16_f32 v155, v38, v39\n"
"v_add_u32 v156, 1769472, v240\n"
"v_permlane16_swap_b32 v152, v154\n"
"v_permlane16_swap_b32 v153, v155\n"
"global_store_dwordx4 v156, v[152:155], s[96:97] sc1\n"
"s_branch FFIN_%=\n"
"FW3_%=:\n"
"s_mov_b32 s56, 0\n"
"ds_read_b64 v[122:123], v239 offset:8960\n"
"ds_read_b64 v[120:121], v239 offset:8704\n"
"ds_read_b64 v[118:119], v239 offset:4864\n"
"ds_read_b64 v[116:117], v239 offset:4608\n"
"ds_read_b64 v[114:115], v239 offset:768\n"
"ds_read_b64 v[112:113], v239 offset:512\n"
"ds_read_b64_tr_b16 a[0:1], v237 offset:800\n"
"ds_read_b64_tr_b16 a[2:3], v237 offset:768\n"
"ds_read_b64_tr_b16 a[4:5], v237 offset:544\n"
"ds_read_b64_tr_b16 a[6:7], v237 offset:512\n"
"ds_read_b64_tr_b16 a[8:9], v237 offset:288\n"
"ds_read_b64_tr_b16 a[10:11], v237 offset:256\n"
"ds_read_b64_tr_b16 a[12:13], v237 offset:32\n"
"ds_read_b64_tr_b16 a[14:15], v237\n"
"ds_read_b128 a[16:19], v236 offset:6144\n"
"ds_read_b128 a[20:23], v236 offset:7168\n"
"ds_read_b128 a[24:27], v236 offset:8192\n"
"ds_read_b128 a[28:31], v236 offset:9216\n"
"ds_read_b64_tr_b16 a[32:33], v237 offset:1856\n"
"ds_read_b64_tr_b16 a[34:35], v237 offset:1824\n"
"ds_read_b64_tr_b16 a[36:37], v237 offset:1600\n"
"ds_read_b64_tr_b16 a[38:39], v237 offset:1568\n"
"ds_read_b64_tr_b16 a[40:41], v237 offset:1344\n"
"ds_read_b64_tr_b16 a[42:43], v237 offset:1312\n"
"ds_read_b64_tr_b16 a[44:45], v237 offset:1088\n"
"ds_read_b64_tr_b16 a[46:47], v237 offset:1056\n"
"ds_read_b64_tr_b16 a[48:49], v237 offset:2912\n"
"ds_read_b64_tr_b16 a[50:51], v237 offset:2880\n"
"ds_read_b64_tr_b16 a[52:53], v237 offset:2656\n"
"ds_read_b64_tr_b16 a[54:55], v237 offset:2624\n"
"ds_read_b64_tr_b16 a[56:57], v237 offset:2400\n"
"ds_read_b64_tr_b16 a[58:59], v237 offset:2368\n"
"ds_read_b64_tr_b16 a[60:61], v237 offset:2144\n"
"ds_read_b64_tr_b16 a[62:63], v237 offset:2112\n"
"s_waitcnt lgkmcnt(14)\n"
"v_lshlrev_b32 v16, 16, v112\n"
"v_and_b32 v17, 0xffff0000, v112\n"
"v_lshlrev_b32 v18, 16, v113\n"
"v_and_b32 v19, 0xffff0000, v113\n"
"v_lshlrev_b32 v20, 16, v114\n"
"v_and_b32 v21, 0xffff0000, v114\n"
"v_lshlrev_b32 v22, 16, v115\n"
"v_and_b32 v23, 0xffff0000, v115\n"
"v_lshlrev_b32 v24, 16, v116\n"
"v_and_b32 v25, 0xffff0000, v116\n"
"v_lshlrev_b32 v26, 16, v117\n"
"v_and_b32 v27, 0xffff0000, v117\n"
"v_lshlrev_b32 v28, 16, v118\n"
"v_and_b32 v29, 0xffff0000, v118\n"
"v_lshlrev_b32 v30, 16, v119\n"
"v_and_b32 v31, 0xffff0000, v119\n"
"v_lshlrev_b32 v32, 16, v120\n"
"v_and_b32 v33, 0xffff0000, v120\n"
"v_lshlrev_b32 v34, 16, v121\n"
"v_and_b32 v35, 0xffff0000, v121\n"
"v_lshlrev_b32 v36, 16, v122\n"
"v_and_b32 v37, 0xffff0000, v122\n"
"v_lshlrev_b32 v38, 16, v123\n"
"v_and_b32 v39, 0xffff0000, v123\n"
"FCKB_%=:\n"
"s_waitcnt vmcnt(4)\n"
"v_bitop3_b32 v147, v164, v165, v167 bitop3:0x96\n"
"s_waitcnt vmcnt(3)\n"
"v_bitop3_b32 v148, v168, v169, v171 bitop3:0x96\n"
"s_waitcnt vmcnt(2)\n"
"v_bitop3_b32 v149, v172, v173, v175 bitop3:0x96\n"
"s_waitcnt vmcnt(1)\n"
"v_bitop3_b32 v150, v176, v177, v179 bitop3:0x96\n"
"s_waitcnt vmcnt(0)\n"
"v_xor_b32 v146, v181, v13\n"
"v_xor_b32 v147, v147, v13\n"
"v_bitop3_b32 v146, v146, v148, v13 bitop3:0xf6\n"
"v_bitop3_b32 v147, v147, v149, v13 bitop3:0xf6\n"
"v_bitop3_b32 v146, v146, v150, v13 bitop3:0xf6\n"
"v_or_b32 v146, v146, v147\n"
"v_cmp_ne_u32 vcc, 0, v146\n"
"s_nop 1\n"
"s_cbranch_vccnz FSLB_%=\n"
"FOKB_%=:\n"
"global_load_dword v14, v250, s[60:61]\n"
"s_and_b32 s41, s42, 127\n"
"s_cmp_eq_u32 s41, 124\n"
"s_cbranch_scc0 FMIB_%=\n"
"s_cmp_eq_u32 s36, 0\n"
"s_cbranch_scc1 FMHB_%=\n"
"v_mov_b32 v206, v166\n"
"v_mov_b32 v207, v170\n"
"v_mov_b32 v208, v174\n"
"v_mov_b32 v209, v178\n"
"ds_write_b128 v254, v[206:209] offset:0\n"
"v_lshlrev_b32 v182, 16, v166\n"
"v_and_b32 v184, 0xffff0000, v166\n"
"v_lshlrev_b32 v186, 16, v170\n"
"v_and_b32 v188, 0xffff0000, v170\n"
"v_lshlrev_b32 v183, 16, v174\n"
"v_and_b32 v185, 0xffff0000, v174\n"
"v_lshlrev_b32 v187, 16, v178\n"
"v_and_b32 v189, 0xffff0000, v178\n"
"s_branch FMIB_%=\n"
"FMHB_%=:\n"
"v_mov_b32 v206, v166\n"
"v_mov_b32 v207, v170\n"
"v_mov_b32 v208, v174\n"
"v_mov_b32 v209, v178\n"
"ds_write_b128 v254, v[206:209] offset:8192\n"
"v_lshlrev_b32 v198, 16, v166\n"
"v_and_b32 v200, 0xffff0000, v166\n"
"v_lshlrev_b32 v202, 16, v170\n"
"v_and_b32 v204, 0xffff0000, v170\n"
"v_lshlrev_b32 v199, 16, v174\n"
"v_and_b32 v201, 0xffff0000, v174\n"
"v_lshlrev_b32 v203, 16, v178\n"
"v_and_b32 v205, 0xffff0000, v178\n"
"FMIB_%=:\n"
"s_mov_b32 vcc_lo, 0x88888888\n"
"s_mov_b32 vcc_hi, 0x88888888\n"
"s_mov_b32 s58, 0xffff0000\n"
"s_mov_b32 s59, 0\n"
"v_and_b32 v218, 0xffff0000, v164\n"
"v_lshlrev_b32 v226, 16, v164\n"
"v_cndmask_b32 v180, v180, v15, vcc\n"
"v_rcp_f32 v218, v218\n"
"v_and_b32 v220, 0xffff0000, v165\n"
"v_lshlrev_b32 v228, 16, v165\n"
"v_rcp_f32 v220, v220\n"
"v_add_f32_dpp v180, v180, v180 quad_perm:[1,0,3,2] row_mask:0xf bank_mask:0xf\n"
"v_and_b32 v222, 0xffff0000, v168\n"
"v_lshlrev_b32 v230, 16, v168\n"
"v_rcp_f32 v222, v222\n"
"v_add_f32_dpp v180, v180, v180 quad_perm:[2,3,0,1] row_mask:0xf bank_mask:0xf\n"
"v_and_b32 v224, 0xffff0000, v169\n"
"v_lshlrev_b32 v232, 16, v169\n"
"v_rcp_f32 v224, v224\n"
"v_cvt_pk_bf16_f32 v180, v15, v180\n"
"v_add_f32 v181, 1.0, v180\n"
"v_and_b32 v219, 0xffff0000, v172\n"
"v_lshlrev_b32 v227, 16, v172\n"
"v_rcp_f32 v219, v219\n"
"v_cvt_pk_bf16_f32 v181, v15, v181\n"
"v_mov_b32 v148, v180\n"
"v_mov_b32 v149, v180\n"
"v_cndmask_b32 v150, v180, v181, s[58:59]\n"
"v_and_b32 v221, 0xffff0000, v173\n"
"v_lshlrev_b32 v229, 16, v173\n"
"v_rcp_f32 v221, v221\n"
"v_permlane32_swap_b32 v148, v149\n"
"v_and_b32 v223, 0xffff0000, v176\n"
"v_lshlrev_b32 v231, 16, v176\n"
"v_rcp_f32 v223, v223\n"
"v_mov_b32 v148, v149\n"
"v_and_b32 v225, 0xffff0000, v177\n"
"v_lshlrev_b32 v233, 16, v177\n"
"v_rcp_f32 v225, v225\n"
"v_permlane16_swap_b32 v149, v148\n"
"v_mul_f32 v150, v150, v149\n"
"v_pk_mul_f32 v[226:227], v[226:227], v[218:219] neg_lo:[1,0] neg_hi:[1,0]\n"
"v_pk_mul_f32 v[228:229], v[228:229], v[220:221] neg_lo:[1,0] neg_hi:[1,0]\n"
"v_pk_mul_f32 v[230:231], v[230:231], v[222:223] neg_lo:[1,0] neg_hi:[1,0]\n"
"v_pk_mul_f32 v[232:233], v[232:233], v[224:225] neg_lo:[1,0] neg_hi:[1,0]\n"
"v_readlane_b32 s44, v150, 16\n"
"v_readlane_b32 s45, v150, 0\n"
"v_readlane_b32 s46, v150, 20\n"
"v_readlane_b32 s47, v150, 4\n"
"v_readlane_b32 s48, v150, 24\n"
"v_readlane_b32 s49, v150, 8\n"
"s_lshl_b32 s41, s33, 8\n"
"s_add_u32 m0, s41, 43744\n"
"s_mov_b32 s64, 0x05040100\n"
"s_mov_b32 s65, 0x07060302\n"
"s_add_u32 s30, s41, 43744\n"
"s_add_u32 s31, s41, 6504\n"
"v_pk_fma_f32 v[182:183], v[182:183], v[218:219], v[226:227]\n"
"v_pk_fma_f32 v[184:185], v[184:185], v[220:221], v[228:229]\n"
"v_pk_fma_f32 v[186:187], v[186:187], v[222:223], v[230:231]\n"
"v_pk_fma_f32 v[188:189], v[188:189], v[224:225], v[232:233]\n"
"v_pk_fma_f32 v[182:183], v[182:183], s[44:45], s[44:45] op_sel:[0,0,1] op_sel_hi:[1,0,1]\n"
"v_pk_fma_f32 v[184:185], v[184:185], s[44:45], s[44:45] op_sel:[0,0,1] op_sel_hi:[1,0,1]\n"
"v_pk_fma_f32 v[186:187], v[186:187], s[44:45], s[44:45] op_sel:[0,0,1] op_sel_hi:[1,0,1]\n"
"v_pk_fma_f32 v[188:189], v[188:189], s[44:45], s[44:45] op_sel:[0,0,1] op_sel_hi:[1,0,1]\n"
"v_cvt_pk_bf16_f32 v182, v182, v183\n"
"v_cvt_pk_bf16_f32 v184, v184, v185\n"
"v_cvt_pk_bf16_f32 v186, v186, v187\n"
"v_cvt_pk_bf16_f32 v188, v188, v189\n"
"ds_write_addtid_b32 v182 offset:49152\n"
"v_perm_b32 v206, v184, v182, s64\n"
"ds_write_addtid_b32 v184 offset:50336\n"
"v_perm_b32 v207, v188, v186, s64\n"
"ds_write_addtid_b32 v186 offset:51520\n"
"v_perm_b32 v208, v184, v182, s65\n"
"ds_write_addtid_b32 v188 offset:52704\n"
"v_perm_b32 v209, v188, v186, s65\n"
"s_mov_b32 m0, s31\n"
"ds_write2_b32 v2, v206, v207 offset0:0 offset1:1\n"
"ds_write2_b32 v2, v208, v209 offset0:2 offset1:3\n"
"ds_write_addtid_b32 v182\n"
"ds_write_addtid_b32 v184 offset:1184\n"
"ds_write_addtid_b32 v186 offset:2368\n"
"ds_write_addtid_b32 v188 offset:3552\n"
"v_pk_fma_f32 v[190:191], v[190:191], v[218:219], v[226:227]\n"
"v_pk_fma_f32 v[192:193], v[192:193], v[220:221], v[228:229]\n"
"v_pk_fma_f32 v[194:195], v[194:195], v[222:223], v[230:231]\n"
"v_pk_fma_f32 v[196:197], v[196:197], v[224:225], v[232:233]\n"
"v_add_u32 v146, 4672, v2\n"
"s_mov_b32 m0, s30\n"
"v_pk_fma_f32 v[190:191], v[190:191], s[46:47], s[46:47] op_sel:[0,0,1] op_sel_hi:[1,0,1]\n"
"v_pk_fma_f32 v[192:193], v[192:193], s[46:47], s[46:47] op_sel:[0,0,1] op_sel_hi:[1,0,1]\n"
"v_pk_fma_f32 v[194:195], v[194:195], s[46:47], s[46:47] op_sel:[0,0,1] op_sel_hi:[1,0,1]\n"
"v_pk_fma_f32 v[196:197], v[196:197], s[46:47], s[46:47] op_sel:[0,0,1] op_sel_hi:[1,0,1]\n"
"v_cvt_pk_bf16_f32 v190, v190, v191\n"
"v_cvt_pk_bf16_f32 v192, v192, v193\n"
"v_cvt_pk_bf16_f32 v194, v194, v195\n"
"v_cvt_pk_bf16_f32 v196, v196, v197\n"
"ds_write_addtid_b32 v190 offset:53888\n"
"v_perm_b32 v210, v192, v190, s64\n"
"ds_write_addtid_b32 v192 offset:55072\n"
"v_perm_b32 v211, v196, v194, s64\n"
"ds_write_addtid_b32 v194 offset:56256\n"
"v_perm_b32 v212, v192, v190, s65\n"
"ds_write_addtid_b32 v196 offset:57440\n"
"v_perm_b32 v213, v196, v194, s65\n"
"s_mov_b32 m0, s31\n"
"ds_write2_b32 v146, v210, v211 offset0:0 offset1:1\n"
"ds_write2_b32 v146, v212, v213 offset0:2 offset1:3\n"
"ds_write_addtid_b32 v190 offset:4736\n"
"ds_write_addtid_b32 v192 offset:5920\n"
"ds_write_addtid_b32 v194 offset:7104\n"
"ds_write_addtid_b32 v196 offset:8288\n"
"v_pk_fma_f32 v[198:199], v[198:199], v[218:219], v[226:227]\n"
"v_pk_fma_f32 v[200:201], v[200:201], v[220:221], v[228:229]\n"
"v_pk_fma_f32 v[202:203], v[202:203], v[222:223], v[230:231]\n"
"v_pk_fma_f32 v[204:205], v[204:205], v[224:225], v[232:233]\n"
"v_add_u32 v147, 9344, v2\n"
"s_mov_b32 m0, s30\n"
"v_pk_fma_f32 v[198:199], v[198:199], s[48:49], s[48:49] op_sel:[0,0,1] op_sel_hi:[1,0,1]\n"
"v_pk_fma_f32 v[200:201], v[200:201], s[48:49], s[48:49] op_sel:[0,0,1] op_sel_hi:[1,0,1]\n"
"v_pk_fma_f32 v[202:203], v[202:203], s[48:49], s[48:49] op_sel:[0,0,1] op_sel_hi:[1,0,1]\n"
"v_pk_fma_f32 v[204:205], v[204:205], s[48:49], s[48:49] op_sel:[0,0,1] op_sel_hi:[1,0,1]\n"
"v_cvt_pk_bf16_f32 v198, v198, v199\n"
"v_cvt_pk_bf16_f32 v200, v200, v201\n"
"v_cvt_pk_bf16_f32 v202, v202, v203\n"
"v_cvt_pk_bf16_f32 v204, v204, v205\n"
"ds_write_addtid_b32 v198 offset:58624\n"
"v_perm_b32 v214, v200, v198, s64\n"
"ds_write_addtid_b32 v200 offset:59808\n"
"v_perm_b32 v215, v204, v202, s64\n"
"ds_write_addtid_b32 v202 offset:60992\n"
"v_perm_b32 v216, v200, v198, s65\n"
"ds_write_addtid_b32 v204 offset:62176\n"
"v_perm_b32 v217, v204, v202, s65\n"
"s_mov_b32 m0, s31\n"
"ds_write2_b32 v147, v214, v215 offset0:0 offset1:1\n"
"ds_write2_b32 v147, v216, v217 offset0:2 offset1:3\n"
"ds_write_addtid_b32 v198 offset:9472\n"
"ds_write_addtid_b32 v200 offset:10656\n"
"ds_write_addtid_b32 v202 offset:11840\n"
"ds_write_addtid_b32 v204 offset:13024\n"
"v_readlane_b32 s53, v181, 48\n"
"v_readlane_b32 s54, v181, 52\n"
"v_readlane_b32 s55, v181, 56\n"
"s_waitcnt lgkmcnt(0)\n"
"s_barrier\n"
"s_cmp_lg_u32 %[blk], 0\n"
"s_cbranch_scc1 FNE_%=\n"
"s_mov_b64 exec, 1\n"
"global_store_dword v15, v13, s[96:97] sc1\n"
"s_mov_b64 exec, -1\n"
"FNE_%=:\n"
"ds_read_b128 v[48:51], v238 offset:128\n"
"ds_read_b128 v[52:55], v238 offset:192\n"
"ds_read_b128 v[56:59], v238 offset:256\n"
"ds_read_b128 v[60:63], v238 offset:320\n"
"s_and_b32 s41, s42, 127\n"
"s_cmp_eq_u32 s41, 124\n"
"s_cbranch_scc0 FMJ1_%=\n"
"ds_read_b64 v[112:113], v239 offset:512\n"
"ds_read_b64 v[114:115], v239 offset:768\n"
"ds_read_b64 v[116:117], v239 offset:4608\n"
"ds_read_b64 v[118:119], v239 offset:4864\n"
"ds_read_b64 v[120:121], v239 offset:8704\n"
"ds_read_b64 v[122:123], v239 offset:8960\n"
"s_waitcnt lgkmcnt(0)\n"
"v_lshlrev_b32 v16, 16, v112\n"
"v_and_b32 v17, 0xffff0000, v112\n"
"v_lshlrev_b32 v18, 16, v113\n"
"v_and_b32 v19, 0xffff0000, v113\n"
"v_lshlrev_b32 v20, 16, v114\n"
"v_and_b32 v21, 0xffff0000, v114\n"
"v_lshlrev_b32 v22, 16, v115\n"
"v_and_b32 v23, 0xffff0000, v115\n"
"v_lshlrev_b32 v24, 16, v116\n"
"v_and_b32 v25, 0xffff0000, v116\n"
"v_lshlrev_b32 v26, 16, v117\n"
"v_and_b32 v27, 0xffff0000, v117\n"
"v_lshlrev_b32 v28, 16, v118\n"
"v_and_b32 v29, 0xffff0000, v118\n"
"v_lshlrev_b32 v30, 16, v119\n"
"v_and_b32 v31, 0xffff0000, v119\n"
"v_lshlrev_b32 v32, 16, v120\n"
"v_and_b32 v33, 0xffff0000, v120\n"
"v_lshlrev_b32 v34, 16, v121\n"
"v_and_b32 v35, 0xffff0000, v121\n"
"v_lshlrev_b32 v36, 16, v122\n"
"v_and_b32 v37, 0xffff0000, v122\n"
"v_lshlrev_b32 v38, 16, v123\n"
"v_and_b32 v39, 0xffff0000, v123\n"
"s_nop 1\n"
"FMJ1_%=:\n"
"s_waitcnt lgkmcnt(3)\n"
"v_mfma_f32_16x16x32_bf16 v[16:19], a[0:3], v[48:51], v[16:19]\n"
"ds_read_b128 v[64:67], v238 offset:384\n"
"ds_read_b128 v[68:71], v238 offset:448\n"
"s_waitcnt lgkmcnt(4)\n"
"v_mfma_f32_16x16x32_bf16 v[16:19], a[4:7], v[52:55], v[16:19]\n"
"ds_read_b128 v[72:75], v3\n"
"s_waitcnt lgkmcnt(4)\n"
"v_mfma_f32_16x16x32_bf16 v[16:19], a[8:11], v[56:59], v[16:19]\n"
"ds_read_b128 v[76:79], v238 offset:1312\n"
"ds_read_b128 v[80:83], v238 offset:1376\n"
"v_mfma_f32_16x16x32_bf16 v[20:23], a[0:3], v[56:59], v[20:23]\n"
"ds_read_b128 v[84:87], v238 offset:1440\n"
"ds_read_b128 a[64:67], v236\n"
"s_waitcnt lgkmcnt(7)\n"
"v_mfma_f32_16x16x32_bf16 v[16:19], a[12:15], v[60:63], v[16:19]\n"
"ds_read_b64_tr_b16 a[68:69], v237 offset:3968\n"
"ds_read_b64_tr_b16 a[70:71], v237 offset:3936\n"
"v_mfma_f32_16x16x32_bf16 v[20:23], a[4:7], v[60:63], v[20:23]\n"
"ds_read_b128 v[88:91], v238 offset:1504\n"
"s_waitcnt lgkmcnt(8)\n"
"v_mfma_f32_16x16x32_bf16 v[20:23], a[8:11], v[64:67], v[20:23]\n"
"ds_read_b128 v[92:95], v238 offset:1568\n"
"ds_read_b64_tr_b16 a[72:73], v237 offset:3712\n"
"v_mfma_f32_16x16x32_bf16 v[20:23], a[12:15], v[68:71], v[20:23]\n"
"ds_read_b64_tr_b16 a[74:75], v237 offset:3680\n"
"s_waitcnt lgkmcnt(9)\n"
"v_mfma_f32_16x16x32_bf16 v[188:191], v[72:75], a[16:19], 0\n"
"ds_read_b128 v[48:51], v238 offset:1632\n"
"ds_read_b128 v[52:55], v3 offset:512\n"
"v_mfma_f32_16x16x32_bf16 v[192:195], v[72:75], a[20:23], 0\n"
"ds_read_b64_tr_b16 a[76:77], v237 offset:3456\n"
"ds_read_b64_tr_b16 a[78:79], v237 offset:3424\n"
"v_mfma_f32_16x16x32_bf16 v[196:199], v[72:75], a[24:27], 0\n"
"ds_read_b64_tr_b16 a[80:81], v237 offset:3200\n"
"ds_read_b64_tr_b16 a[82:83], v237 offset:3168\n"
"v_mfma_f32_16x16x32_bf16 v[200:203], v[72:75], a[28:31], 0\n"
"ds_read_b64 a[84:85], v241 offset:64\n"
"ds_read_b64 a[86:87], v241 offset:72\n"
"v_mfma_f32_16x16x32_bf16 v[16:19], a[32:35], v[76:79], v[16:19]\n"
"ds_read_b64 a[88:89], v241 offset:192\n"
"s_waitcnt lgkmcnt(14)\n"
"v_mfma_f32_16x16x32_bf16 v[16:19], a[36:39], v[80:83], v[16:19]\n"
"ds_read_b128 v[56:59], v238 offset:2496\n"
"ds_read_b128 v[60:63], v238 offset:2560\n"
"v_mfma_f32_16x16x32_bf16 v[16:19], a[40:43], v[84:87], v[16:19]\n"
"v_cvt_pk_bf16_f32 v96, v190, v194\n"
"v_cvt_pk_bf16_f32 v97, v198, v202\n"
"v_mfma_f32_16x16x32_bf16 v[20:23], a[32:35], v[84:87], v[20:23]\n"
"ds_read_b128 v[64:67], v238 offset:2624\n"
"s_waitcnt lgkmcnt(14)\n"
"v_mfma_f32_16x16x32_bf16 v[16:19], a[44:47], v[88:91], v[16:19]\n"
"ds_read_b128 v[68:71], v238 offset:2688\n"
"v_cvt_pk_bf16_f32 v98, v191, v195\n"
"v_mfma_f32_16x16x32_bf16 v[20:23], a[36:39], v[88:91], v[20:23]\n"
"v_cvt_pk_bf16_f32 v99, v199, v203\n"
"v_cvt_pk_bf16_f32 v100, v188, v192\n"
"v_mfma_f32_16x16x32_bf16 v[20:23], a[40:43], v[92:95], v[20:23]\n"
"v_cvt_pk_bf16_f32 v101, v196, v200\n"
"s_waitcnt lgkmcnt(11)\n"
"v_mfma_f32_16x16x32_bf16 v[20:23], a[44:47], v[48:51], v[20:23]\n"
"ds_read_b128 v[72:75], v238 offset:2752\n"
"ds_read_b128 v[76:79], v238 offset:2816\n"
"v_mfma_f32_16x16x32_bf16 v[204:207], v[52:55], a[16:19], 0\n"
"v_cvt_pk_bf16_f32 v102, v189, v193\n"
"v_cvt_pk_bf16_f32 v103, v197, v201\n"
"v_mfma_f32_16x16x32_bf16 v[208:211], v[52:55], a[20:23], 0\n"
"ds_read_b128 v[80:83], v10 offset:1024\n"
"ds_read_b128 v[84:87], v10 offset:1536\n"
"v_mfma_f32_16x16x32_bf16 v[212:215], v[52:55], a[24:27], 0\n"
"ds_read_b64 a[90:91], v241 offset:200\n"
"ds_read_b64 a[92:93], v241\n"
"v_mfma_f32_16x16x32_bf16 v[216:219], v[52:55], a[28:31], 0\n"
"ds_read_b128 v[88:91], v238 offset:3680\n"
"s_waitcnt lgkmcnt(9)\n"
"v_mfma_f32_16x16x32_bf16 v[16:19], a[48:51], v[56:59], v[16:19]\n"
"ds_read_b128 v[92:95], v238 offset:3744\n"
"ds_read_b64 a[94:95], v241 offset:8\n"
"v_mfma_f32_16x16x32_bf16 v[16:19], a[52:55], v[60:63], v[16:19]\n"
"ds_read_b128 v[48:51], v238 offset:3808\n"
"s_waitcnt lgkmcnt(10)\n"
"v_mfma_f32_16x16x32_bf16 v[16:19], a[56:59], v[64:67], v[16:19]\n"
"ds_read_b128 v[52:55], v238 offset:3872\n"
"v_cvt_pk_bf16_f32 v104, v206, v210\n"
"v_mfma_f32_16x16x32_bf16 v[20:23], a[48:51], v[64:67], v[20:23]\n"
"v_cvt_pk_bf16_f32 v105, v214, v218\n"
"v_cvt_pk_bf16_f32 v106, v207, v211\n"
"v_mfma_f32_16x16x32_bf16 v[16:19], a[60:63], v[68:71], v[16:19]\n"
"v_cvt_pk_bf16_f32 v107, v215, v219\n"
"v_cvt_pk_bf16_f32 v108, v204, v208\n"
"v_mfma_f32_16x16x32_bf16 v[20:23], a[52:55], v[68:71], v[20:23]\n"
"ds_read_b128 v[56:59], v238 offset:3936\n"
"s_waitcnt lgkmcnt(10)\n"
"v_mfma_f32_16x16x32_bf16 v[20:23], a[56:59], v[72:75], v[20:23]\n"
"ds_read_b128 v[60:63], v238 offset:4000\n"
"v_cvt_pk_bf16_f32 v109, v212, v216\n"
"v_mfma_f32_16x16x32_bf16 v[20:23], a[60:63], v[76:79], v[20:23]\n"
"v_cvt_pk_bf16_f32 v110, v205, v209\n"
"s_waitcnt lgkmcnt(9)\n"
"v_mfma_f32_16x16x32_bf16 v[16:19], a[64:67], v[80:83], v[16:19]\n"
"v_cvt_pk_bf16_f32 v111, v213, v217\n"
"ds_read_b64 a[96:97], v241 offset:128\n"
"v_mfma_f32_16x16x32_bf16 v[20:23], a[64:67], v[84:87], v[20:23]\n"
"s_waitcnt lgkmcnt(6)\n"
"v_mfma_f32_16x16x32_bf16 v[16:19], a[68:71], v[88:91], v[16:19]\n"
"ds_read_b64 a[98:99], v241 offset:136\n"
"v_mfma_f32_16x16x32_bf16 v[16:19], a[72:75], v[92:95], v[16:19]\n"
"s_waitcnt lgkmcnt(4)\n"
"v_mfma_f32_16x16x32_bf16 v[16:19], a[76:79], v[48:51], v[16:19]\n"
"ds_read_b64_tr_b16 a[100:101], v237 offset:5024\n"
"ds_read_b64_tr_b16 a[102:103], v237 offset:4992\n"
"v_mfma_f32_16x16x32_bf16 v[20:23], a[68:71], v[48:51], v[20:23]\n"
"ds_read_b128 v[64:67], v238 offset:4864\n"
"ds_read_b128 v[68:71], v238 offset:4928\n"
"v_mfma_f32_16x16x32_bf16 v[16:19], a[80:83], v[52:55], v[16:19]\n"
"ds_read_b64_tr_b16 a[104:105], v237 offset:4768\n"
"ds_read_b64_tr_b16 a[106:107], v237 offset:4736\n"
"v_mfma_f32_16x16x32_bf16 v[20:23], a[72:75], v[52:55], v[20:23]\n"
"ds_read_b128 v[72:75], v238 offset:4992\n"
"s_waitcnt lgkmcnt(9)\n"
"v_mfma_f32_16x16x32_bf16 v[20:23], a[76:79], v[56:59], v[20:23]\n"
"ds_read_b128 v[76:79], v238 offset:5056\n"
"ds_read_b64_tr_b16 a[108:109], v237 offset:4512\n"
"v_mfma_f32_16x16x32_bf16 v[20:23], a[80:83], v[60:63], v[20:23]\n"
"ds_read_b64_tr_b16 a[110:111], v237 offset:4480\n"
"v_mfma_f32_16x16x32_bf16 v[16:19], v[96:99], a[84:87], v[16:19]\n"
"ds_read_b64_tr_b16 a[112:113], v237 offset:4256\n"
"v_mfma_f32_16x16x32_bf16 v[20:23], v[96:99], a[88:91], v[20:23]\n"
"ds_read_b128 v[80:83], v238 offset:5120\n"
"ds_read_b128 v[84:87], v238 offset:5184\n"
"v_mfma_f32_16x16x32_bf16 v[16:19], v[100:103], a[92:95], v[16:19]\n"
"ds_read_b64_tr_b16 a[114:115], v237 offset:4224\n"
"ds_read_b64_tr_b16 a[140:141], v237 offset:6624\n"
"s_waitcnt lgkmcnt(14)\n"
"v_mfma_f32_16x16x32_bf16 v[20:23], v[100:103], a[96:99], v[20:23]\n"
"ds_read_b128 v[88:91], v3 offset:4672\n"
"ds_read_b128 v[92:95], v238 offset:6048\n"
"ds_read_b64_tr_b16 a[142:143], v237 offset:6592\n"
"v_mfma_f32_16x16x32_bf16 v[20:23], v[104:107], a[84:87], v[20:23]\n"
"ds_read_b64_tr_b16 a[116:117], v237 offset:6080\n"
"ds_read_b64_tr_b16 a[118:119], v237 offset:6048\n"
"ds_read_b64_tr_b16 a[144:145], v237 offset:6368\n"
"v_mfma_f32_16x16x32_bf16 v[20:23], v[108:111], a[92:95], v[20:23]\n"
"ds_read_b64_tr_b16 a[146:147], v237 offset:6336\n"
"s_waitcnt lgkmcnt(14)\n"
"v_mfma_f32_16x16x32_bf16 v[24:27], a[100:103], v[64:67], v[24:27]\n"
"ds_read_b64_tr_b16 a[120:121], v237 offset:5824\n"
"v_mfma_f32_16x16x32_bf16 v[24:27], a[104:107], v[68:71], v[24:27]\n"
"v_mul_f32 v160, s53, v16\n"
"s_waitcnt lgkmcnt(13)\n"
"v_mfma_f32_16x16x32_bf16 v[24:27], a[108:111], v[72:75], v[24:27]\n"
"ds_read_b128 v[48:51], v238 offset:6112\n"
"ds_read_b128 v[52:55], v238 offset:6176\n"
"v_mul_f32 v161, s53, v17\n"
"v_mfma_f32_16x16x32_bf16 v[28:31], a[100:103], v[72:75], v[28:31]\n"
"ds_read_b64_tr_b16 a[122:123], v237 offset:5792\n"
"v_mul_f32 v162, s53, v18\n"
"s_waitcnt lgkmcnt(12)\n"
"v_mfma_f32_16x16x32_bf16 v[24:27], a[112:115], v[76:79], v[24:27]\n"
"ds_read_b64_tr_b16 a[124:125], v237 offset:5568\n"
"ds_read_b64_tr_b16 a[126:127], v237 offset:5536\n"
"ds_read_b64_tr_b16 a[156:157], v237 offset:7936\n"
"v_mfma_f32_16x16x32_bf16 v[28:31], a[104:107], v[76:79], v[28:31]\n"
"ds_read_b128 v[56:59], v238 offset:6240\n"
"ds_read_b64_tr_b16 a[158:159], v237 offset:7904\n"
"v_mul_f32 v163, s53, v19\n"
"v_mfma_f32_16x16x32_bf16 v[28:31], a[108:111], v[80:83], v[28:31]\n"
"ds_read_b128 v[60:63], v238 offset:6304\n"
"ds_read_b64_tr_b16 a[128:129], v237 offset:5312\n"
"ds_read_b64_tr_b16 a[160:161], v237 offset:7680\n"
"v_mfma_f32_16x16x32_bf16 v[28:31], a[112:115], v[84:87], v[28:31]\n"
"ds_read_b64_tr_b16 a[130:131], v237 offset:5280\n"
"ds_read_b64_tr_b16 a[162:163], v237 offset:7648\n"
"v_mul_f32 v164, s53, v20\n"
"s_waitcnt lgkmcnt(14)\n"
"v_mfma_f32_16x16x32_bf16 v[188:191], v[88:91], a[16:19], 0\n"
"ds_read_b128 v[64:67], v238 offset:6368\n"
"ds_read_b128 v[68:71], v3 offset:5184\n"
"ds_read_b64_tr_b16 a[164:165], v237 offset:7424\n"
"v_mfma_f32_16x16x32_bf16 v[192:195], v[88:91], a[20:23], 0\n"
"ds_read_b64_tr_b16 a[132:133], v237 offset:7136\n"
"ds_read_b64_tr_b16 a[134:135], v237 offset:7104\n"
"ds_read_b64_tr_b16 a[166:167], v237 offset:7392\n"
"v_mfma_f32_16x16x32_bf16 v[196:199], v[88:91], a[24:27], 0\n"
"ds_read_b64_tr_b16 a[136:137], v237 offset:6880\n"
"ds_read_b64_tr_b16 a[138:139], v237 offset:6848\n"
"v_mul_f32 v165, s53, v21\n"
"v_mfma_f32_16x16x32_bf16 v[200:203], v[88:91], a[28:31], 0\n"
"ds_read_b128 a[148:151], v236 offset:1024\n"
"v_mul_f32 v166, s53, v22\n"
"v_mfma_f32_16x16x32_bf16 v[24:27], a[116:119], v[92:95], v[24:27]\n"
"v_mul_f32 v167, s53, v23\n"
"s_waitcnt lgkmcnt(14)\n"
"v_mfma_f32_16x16x32_bf16 v[24:27], a[120:123], v[48:51], v[24:27]\n"
"ds_read_b128 v[72:75], v238 offset:7232\n"
"ds_read_b128 v[76:79], v238 offset:7296\n"
"v_mfma_f32_16x16x32_bf16 v[24:27], a[124:127], v[52:55], v[24:27]\n"
"v_cvt_pk_bf16_f32 v220, v190, v194\n"
"v_cvt_pk_bf16_f32 v221, v198, v202\n"
"ds_read_b64 a[176:177], v241 offset:832\n"
"v_cvt_pk_bf16_f32 v170, v160, v161\n"
"v_mfma_f32_16x16x32_bf16 v[28:31], a[116:119], v[52:55], v[28:31]\n"
"ds_read_b128 v[80:83], v238 offset:7360\n"
"s_waitcnt lgkmcnt(14)\n"
"v_mfma_f32_16x16x32_bf16 v[24:27], a[128:131], v[56:59], v[24:27]\n"
"ds_read_b128 v[84:87], v238 offset:7424\n"
"v_cvt_pk_bf16_f32 v222, v191, v195\n"
"ds_read_b64 a[178:179], v241 offset:840\n"
"v_mfma_f32_16x16x32_bf16 v[28:31], a[120:123], v[56:59], v[28:31]\n"
"v_cvt_pk_bf16_f32 v223, v199, v203\n"
"v_cvt_pk_bf16_f32 v224, v188, v192\n"
"v_cvt_pk_bf16_f32 v171, v162, v163\n"
"v_mfma_f32_16x16x32_bf16 v[28:31], a[124:127], v[60:63], v[28:31]\n"
"v_cvt_pk_bf16_f32 v225, v196, v200\n"
"ds_read_b64_tr_b16 a[188:189], v237 offset:8992\n"
"v_cvt_pk_bf16_f32 v172, v164, v165\n"
"s_waitcnt lgkmcnt(14)\n"
"v_mfma_f32_16x16x32_bf16 v[28:31], a[128:131], v[64:67], v[28:31]\n"
"ds_read_b128 v[88:91], v238 offset:7488\n"
"ds_read_b128 v[92:95], v238 offset:7552\n"
"ds_read_b64_tr_b16 a[190:191], v237 offset:8960\n"
"v_mfma_f32_16x16x32_bf16 v[204:207], v[68:71], a[16:19], 0\n"
"v_cvt_pk_bf16_f32 v226, v189, v193\n"
"v_cvt_pk_bf16_f32 v227, v197, v201\n"
"ds_read_b64_tr_b16 a[192:193], v237 offset:8736\n"
"v_mfma_f32_16x16x32_bf16 v[208:211], v[68:71], a[20:23], 0\n"
"ds_read_b128 v[48:51], v10 offset:5696\n"
"ds_read_b128 v[52:55], v10 offset:6208\n"
"ds_read_b64_tr_b16 a[194:195], v237 offset:8704\n"
"v_cvt_pk_bf16_f32 v173, v166, v167\n"
"v_mfma_f32_16x16x32_bf16 v[212:215], v[68:71], a[24:27], 0\n"
"ds_read_b64_tr_b16 a[152:153], v237 offset:8192\n"
"ds_read_b64_tr_b16 a[154:155], v237 offset:8160\n"
"v_add_u32 v174, 589824, v244\n"
"v_mfma_f32_16x16x32_bf16 v[216:219], v[68:71], a[28:31], 0\n"
"ds_read_b128 v[56:59], v238 offset:8416\n"
"s_waitcnt lgkmcnt(14)\n"
"v_mfma_f32_16x16x32_bf16 v[24:27], a[132:135], v[72:75], v[24:27]\n"
"ds_read_b128 v[60:63], v238 offset:8480\n"
"v_permlane16_swap_b32 v170, v172\n"
"v_mfma_f32_16x16x32_bf16 v[24:27], a[136:139], v[76:79], v[24:27]\n"
"ds_read_b128 v[64:67], v238 offset:8544\n"
"s_waitcnt lgkmcnt(14)\n"
"v_mfma_f32_16x16x32_bf16 v[24:27], a[140:143], v[80:83], v[24:27]\n"
"ds_read_b128 v[68:71], v238 offset:8608\n"
"v_cvt_pk_bf16_f32 v228, v206, v210\n"
"v_permlane16_swap_b32 v171, v173\n"
"v_mfma_f32_16x16x32_bf16 v[28:31], a[132:135], v[80:83], v[28:31]\n"
"v_cvt_pk_bf16_f32 v229, v214, v218\n"
"v_cvt_pk_bf16_f32 v230, v207, v211\n"
"global_store_dwordx4 v174, v[170:173], s[96:97] sc1\n"
"v_mfma_f32_16x16x32_bf16 v[24:27], a[144:147], v[84:87], v[24:27]\n"
"v_cvt_pk_bf16_f32 v231, v215, v219\n"
"v_cvt_pk_bf16_f32 v40, v204, v208\n"
"v_mfma_f32_16x16x32_bf16 v[28:31], a[136:139], v[84:87], v[28:31]\n"
"ds_read_b128 v[72:75], v238 offset:8672\n"
"v_cvt_pk_bf16_f32 v140, v16, v17\n"
"s_waitcnt lgkmcnt(12)\n"
"v_mfma_f32_16x16x32_bf16 v[28:31], a[140:143], v[88:91], v[28:31]\n"
"ds_read_b128 v[76:79], v238 offset:8736\n"
"v_cvt_pk_bf16_f32 v41, v212, v216\n"
"v_cvt_pk_bf16_f32 v141, v18, v19\n"
"v_mfma_f32_16x16x32_bf16 v[28:31], a[144:147], v[92:95], v[28:31]\n"
"v_cvt_pk_bf16_f32 v42, v205, v209\n"
"s_waitcnt lgkmcnt(9)\n"
"v_mfma_f32_16x16x32_bf16 v[24:27], a[148:151], v[48:51], v[24:27]\n"
"v_cvt_pk_bf16_f32 v43, v213, v217\n"
"ds_read_b64 a[168:169], v241 offset:896\n"
"v_cvt_pk_bf16_f32 v142, v20, v21\n"
"v_mfma_f32_16x16x32_bf16 v[28:31], a[148:151], v[52:55], v[28:31]\n"
"v_cvt_pk_bf16_f32 v143, v22, v23\n"
"s_waitcnt lgkmcnt(5)\n"
"v_mfma_f32_16x16x32_bf16 v[24:27], a[152:155], v[56:59], v[24:27]\n"
"ds_read_b64 a[170:171], v241 offset:904\n"
"v_add_u32 v144, 589824, v240\n"
"v_mfma_f32_16x16x32_bf16 v[24:27], a[156:159], v[60:63], v[24:27]\n"
"v_permlane16_swap_b32 v140, v142\n"
"s_waitcnt lgkmcnt(4)\n"
"v_mfma_f32_16x16x32_bf16 v[24:27], a[160:163], v[64:67], v[24:27]\n"
"ds_read_b64 a[172:173], v241 offset:1024\n"
"ds_read_b64 a[174:175], v241 offset:1032\n"
"v_mfma_f32_16x16x32_bf16 v[28:31], a[152:155], v[64:67], v[28:31]\n"
"ds_read_b128 v[80:83], v238 offset:9600\n"
"ds_read_b128 v[84:87], v238 offset:9664\n"
"v_permlane16_swap_b32 v141, v143\n"
"v_mfma_f32_16x16x32_bf16 v[24:27], a[164:167], v[68:71], v[24:27]\n"
"ds_read_b64 a[180:181], v241 offset:960\n"
"ds_read_b64 a[182:183], v241 offset:968\n"
"global_store_dwordx4 v144, v[140:143], s[96:97] sc1\n"
"v_mfma_f32_16x16x32_bf16 v[28:31], a[156:159], v[68:71], v[28:31]\n"
"ds_read_b128 v[88:91], v238 offset:9728\n"
"s_waitcnt lgkmcnt(9)\n"
"v_mfma_f32_16x16x32_bf16 v[28:31], a[160:163], v[72:75], v[28:31]\n"
"ds_read_b128 v[92:95], v238 offset:9792\n"
"ds_read_b64_tr_b16 a[184:185], v237 offset:9248\n"
"v_mfma_f32_16x16x32_bf16 v[28:31], a[164:167], v[76:79], v[28:31]\n"
"ds_read_b64_tr_b16 a[186:187], v237 offset:9216\n"
"s_waitcnt lgkmcnt(10)\n"
"v_mfma_f32_16x16x32_bf16 v[24:27], v[220:223], a[168:171], v[24:27]\n"
"ds_read_b64_tr_b16 a[196:197], v237 offset:8480\n"
"s_waitcnt lgkmcnt(9)\n"
"v_mfma_f32_16x16x32_bf16 v[28:31], v[220:223], a[172:175], v[28:31]\n"
"ds_read_b128 v[48:51], v238 offset:9856\n"
"ds_read_b128 v[52:55], v238 offset:9920\n"
"v_mfma_f32_16x16x32_bf16 v[24:27], v[224:227], a[176:179], v[24:27]\n"
"ds_read_b64_tr_b16 a[198:199], v237 offset:8448\n"
"ds_read_b64_tr_b16 a[224:225], v237 offset:10848\n"
"s_waitcnt lgkmcnt(9)\n"
"v_mfma_f32_16x16x32_bf16 v[28:31], v[224:227], a[180:183], v[28:31]\n"
"ds_read_b128 v[56:59], v3 offset:9344\n"
"ds_read_b128 v[60:63], v238 offset:10784\n"
"ds_read_b64_tr_b16 a[226:227], v237 offset:10816\n"
"v_mfma_f32_16x16x32_bf16 v[28:31], v[228:231], a[168:171], v[28:31]\n"
"ds_read_b64_tr_b16 a[200:201], v237 offset:10304\n"
"ds_read_b64_tr_b16 a[202:203], v237 offset:10272\n"
"ds_read_b64_tr_b16 a[228:229], v237 offset:10592\n"
"v_mfma_f32_16x16x32_bf16 v[28:31], v[40:43], a[176:179], v[28:31]\n"
"ds_read_b64_tr_b16 a[230:231], v237 offset:10560\n"
"s_waitcnt lgkmcnt(12)\n"
"v_mfma_f32_16x16x32_bf16 v[32:35], a[184:187], v[80:83], v[32:35]\n"
"ds_read_b64_tr_b16 a[204:205], v237 offset:10048\n"
"v_mfma_f32_16x16x32_bf16 v[32:35], a[188:191], v[84:87], v[32:35]\n"
"v_mul_f32 v160, s54, v24\n"
"v_mfma_f32_16x16x32_bf16 v[32:35], a[192:195], v[88:91], v[32:35]\n"
"ds_read_b128 v[64:67], v238 offset:10848\n"
"ds_read_b128 v[68:71], v238 offset:10912\n"
"v_mul_f32 v161, s54, v25\n"
"v_mfma_f32_16x16x32_bf16 v[36:39], a[184:187], v[88:91], v[36:39]\n"
"ds_read_b64_tr_b16 a[206:207], v237 offset:10016\n"
"v_mul_f32 v162, s54, v26\n"
"s_waitcnt lgkmcnt(12)\n"
"v_mfma_f32_16x16x32_bf16 v[32:35], a[196:199], v[92:95], v[32:35]\n"
"ds_read_b64_tr_b16 a[208:209], v237 offset:9792\n"
"ds_read_b64_tr_b16 a[210:211], v237 offset:9760\n"
"ds_read_b64_tr_b16 a[240:241], v237 offset:12160\n"
"v_mfma_f32_16x16x32_bf16 v[36:39], a[188:191], v[92:95], v[36:39]\n"
"ds_read_b128 v[72:75], v238 offset:10976\n"
"ds_read_b64_tr_b16 a[242:243], v237 offset:12128\n"
"v_mfma_f32_16x16x32_bf16 v[36:39], a[192:195], v[48:51], v[36:39]\n"
"ds_read_b128 v[76:79], v238 offset:11040\n"
"ds_read_b64_tr_b16 a[212:213], v237 offset:9536\n"
"ds_read_b64_tr_b16 a[244:245], v237 offset:11904\n"
"v_mfma_f32_16x16x32_bf16 v[36:39], a[196:199], v[52:55], v[36:39]\n"
"ds_read_b64_tr_b16 a[214:215], v237 offset:9504\n"
"ds_read_b64_tr_b16 a[246:247], v237 offset:11872\n"
"s_waitcnt lgkmcnt(14)\n"
"v_mfma_f32_16x16x32_bf16 v[188:191], v[56:59], a[16:19], 0\n"
"ds_read_b128 v[80:83], v238 offset:11104\n"
"ds_read_b128 v[84:87], v3 offset:9856\n"
"ds_read_b64_tr_b16 a[248:249], v237 offset:11648\n"
"v_mfma_f32_16x16x32_bf16 v[192:195], v[56:59], a[20:23], 0\n"
"ds_read_b64_tr_b16 a[216:217], v237 offset:11360\n"
"ds_read_b64_tr_b16 a[218:219], v237 offset:11328\n"
"ds_read_b64_tr_b16 a[250:251], v237 offset:11616\n"
"v_mfma_f32_16x16x32_bf16 v[196:199], v[56:59], a[24:27], 0\n"
"ds_read_b64_tr_b16 a[220:221], v237 offset:11104\n"
"ds_read_b64_tr_b16 a[222:223], v237 offset:11072\n"
"v_mul_f32 v163, s54, v27\n"
"v_mfma_f32_16x16x32_bf16 v[200:203], v[56:59], a[28:31], 0\n"
"ds_read_b128 a[232:235], v236 offset:2048\n"
"v_mul_f32 v164, s54, v28\n"
"v_mfma_f32_16x16x32_bf16 v[32:35], a[200:203], v[60:63], v[32:35]\n"
"v_mul_f32 v165, s54, v29\n"
"s_waitcnt lgkmcnt(14)\n"
"v_mfma_f32_16x16x32_bf16 v[32:35], a[204:207], v[64:67], v[32:35]\n"
"ds_read_b128 v[88:91], v238 offset:11968\n"
"ds_read_b128 v[92:95], v238 offset:12032\n"
"v_mfma_f32_16x16x32_bf16 v[32:35], a[208:211], v[68:71], v[32:35]\n"
"v_cvt_pk_bf16_f32 v44, v190, v194\n"
"v_cvt_pk_bf16_f32 v45, v198, v202\n"
"ds_read_b64 a[4:5], v241 offset:1664\n"
"v_mfma_f32_16x16x32_bf16 v[36:39], a[200:203], v[68:71], v[36:39]\n"
"ds_read_b128 v[48:51], v238 offset:12096\n"
"s_waitcnt lgkmcnt(14)\n"
"v_mfma_f32_16x16x32_bf16 v[32:35], a[212:215], v[72:75], v[32:35]\n"
"ds_read_b128 v[52:55], v238 offset:12160\n"
"v_cvt_pk_bf16_f32 v46, v191, v195\n"
"ds_read_b64 a[6:7], v241 offset:1672\n"
"v_mfma_f32_16x16x32_bf16 v[36:39], a[204:207], v[72:75], v[36:39]\n"
"v_cvt_pk_bf16_f32 v47, v199, v203\n"
"v_cvt_pk_bf16_f32 v136, v188, v192\n"
"v_mul_f32 v166, s54, v30\n"
"v_mfma_f32_16x16x32_bf16 v[36:39], a[208:211], v[76:79], v[36:39]\n"
"v_cvt_pk_bf16_f32 v137, v196, v200\n"
"v_mul_f32 v167, s54, v31\n"
"s_waitcnt lgkmcnt(13)\n"
"v_mfma_f32_16x16x32_bf16 v[36:39], a[212:215], v[80:83], v[36:39]\n"
"ds_read_b128 v[56:59], v238 offset:12224\n"
"ds_read_b128 v[60:63], v238 offset:12288\n"
"v_mfma_f32_16x16x32_bf16 v[204:207], v[84:87], a[16:19], 0\n"
"v_cvt_pk_bf16_f32 v138, v189, v193\n"
"v_cvt_pk_bf16_f32 v139, v197, v201\n"
"v_cvt_pk_bf16_f32 v176, v160, v161\n"
"v_mfma_f32_16x16x32_bf16 v[208:211], v[84:87], a[20:23], 0\n"
"ds_read_b128 v[64:67], v10 offset:10368\n"
"ds_read_b128 v[68:71], v10 offset:10880\n"
"v_cvt_pk_bf16_f32 v177, v162, v163\n"
"v_mfma_f32_16x16x32_bf16 v[212:215], v[84:87], a[24:27], 0\n"
"ds_read_b64_tr_b16 a[236:237], v237 offset:12416\n"
"ds_read_b64_tr_b16 a[238:239], v237 offset:12384\n"
"v_cvt_pk_bf16_f32 v178, v164, v165\n"
"v_mfma_f32_16x16x32_bf16 v[216:219], v[84:87], a[28:31], 0\n"
"ds_read_b128 v[72:75], v238 offset:13152\n"
"s_waitcnt lgkmcnt(11)\n"
"v_mfma_f32_16x16x32_bf16 v[32:35], a[216:219], v[88:91], v[32:35]\n"
"ds_read_b128 v[76:79], v238 offset:13216\n"
"v_cvt_pk_bf16_f32 v179, v166, v167\n"
"v_mfma_f32_16x16x32_bf16 v[32:35], a[220:223], v[92:95], v[32:35]\n"
"ds_read_b128 v[80:83], v238 offset:13280\n"
"s_waitcnt lgkmcnt(10)\n"
"v_mfma_f32_16x16x32_bf16 v[32:35], a[224:227], v[48:51], v[32:35]\n"
"ds_read_b128 v[84:87], v238 offset:13344\n"
"v_cvt_pk_bf16_f32 v4, v206, v210\n"
"v_add_u32 v180, 590080, v244\n"
"v_mfma_f32_16x16x32_bf16 v[36:39], a[216:219], v[48:51], v[36:39]\n"
"v_cvt_pk_bf16_f32 v5, v214, v218\n"
"v_cvt_pk_bf16_f32 v6, v207, v211\n"
"v_mfma_f32_16x16x32_bf16 v[32:35], a[228:231], v[52:55], v[32:35]\n"
"v_cvt_pk_bf16_f32 v7, v215, v219\n"
"v_cvt_pk_bf16_f32 v112, v204, v208\n"
"v_permlane16_swap_b32 v176, v178\n"
"v_mfma_f32_16x16x32_bf16 v[36:39], a[220:223], v[52:55], v[36:39]\n"
"ds_read_b128 v[88:91], v238 offset:13408\n"
"v_permlane16_swap_b32 v177, v179\n"
"s_waitcnt lgkmcnt(9)\n"
"v_mfma_f32_16x16x32_bf16 v[36:39], a[224:227], v[56:59], v[36:39]\n"
"ds_read_b128 v[92:95], v238 offset:13472\n"
"v_cvt_pk_bf16_f32 v113, v212, v216\n"
"global_store_dwordx4 v180, v[176:179], s[96:97] sc1\n"
"v_mfma_f32_16x16x32_bf16 v[36:39], a[228:231], v[60:63], v[36:39]\n"
"v_cvt_pk_bf16_f32 v114, v205, v209\n"
"s_waitcnt lgkmcnt(8)\n"
"v_mfma_f32_16x16x32_bf16 v[32:35], a[232:235], v[64:67], v[32:35]\n"
"v_cvt_pk_bf16_f32 v115, v213, v217\n"
"ds_read_b64 a[252:253], v241 offset:1728\n"
"v_mfma_f32_16x16x32_bf16 v[36:39], a[232:235], v[68:71], v[36:39]\n"
"v_cvt_pk_bf16_f32 v146, v24, v25\n"
"s_waitcnt lgkmcnt(5)\n"
"v_mfma_f32_16x16x32_bf16 v[32:35], a[236:239], v[72:75], v[32:35]\n"
"ds_read_b64 a[254:255], v241 offset:1736\n"
"v_cvt_pk_bf16_f32 v147, v26, v27\n"
"v_mfma_f32_16x16x32_bf16 v[32:35], a[240:243], v[76:79], v[32:35]\n"
"v_cvt_pk_bf16_f32 v148, v28, v29\n"
"s_waitcnt lgkmcnt(4)\n"
"v_mfma_f32_16x16x32_bf16 v[32:35], a[244:247], v[80:83], v[32:35]\n"
"ds_read_b64 a[0:1], v241 offset:1856\n"
"ds_read_b64 a[2:3], v241 offset:1864\n"
"v_cvt_pk_bf16_f32 v149, v30, v31\n"
"v_mfma_f32_16x16x32_bf16 v[36:39], a[236:239], v[80:83], v[36:39]\n"
"ds_read_b64 a[8:9], v241 offset:1792\n"
"v_add_u32 v150, 589888, v240\n"
"v_mfma_f32_16x16x32_bf16 v[32:35], a[248:251], v[84:87], v[32:35]\n"
"ds_read_b64 a[10:11], v241 offset:1800\n"
"v_permlane16_swap_b32 v146, v148\n"
"v_permlane16_swap_b32 v147, v149\n"
"v_mfma_f32_16x16x32_bf16 v[36:39], a[240:243], v[84:87], v[36:39]\n"
"global_store_dwordx4 v150, v[146:149], s[96:97] sc1\n"
"s_waitcnt lgkmcnt(6)\n"
"v_mfma_f32_16x16x32_bf16 v[36:39], a[244:247], v[88:91], v[36:39]\n"
"v_mfma_f32_16x16x32_bf16 v[36:39], a[248:251], v[92:95], v[36:39]\n"
"s_waitcnt lgkmcnt(4)\n"
"v_mfma_f32_16x16x32_bf16 v[32:35], v[44:47], a[252:255], v[32:35]\n"
"s_waitcnt lgkmcnt(2)\n"
"v_mfma_f32_16x16x32_bf16 v[36:39], v[44:47], a[0:3], v[36:39]\n"
"v_mfma_f32_16x16x32_bf16 v[32:35], v[136:139], a[4:7], v[32:35]\n"
"s_waitcnt lgkmcnt(0)\n"
"v_mfma_f32_16x16x32_bf16 v[36:39], v[136:139], a[8:11], v[36:39]\n"
"v_mfma_f32_16x16x32_bf16 v[36:39], v[4:7], a[252:255], v[36:39]\n"
"v_mfma_f32_16x16x32_bf16 v[36:39], v[112:115], a[4:7], v[36:39]\n"
"s_nop 3\n"
"v_mul_f32 v160, s55, v32\n"
"v_mul_f32 v161, s55, v33\n"
"v_mul_f32 v162, s55, v34\n"
"v_mul_f32 v163, s55, v35\n"
"v_mul_f32 v164, s55, v36\n"
"v_mul_f32 v165, s55, v37\n"
"v_mul_f32 v166, s55, v38\n"
"v_mul_f32 v167, s55, v39\n"
"v_cvt_pk_bf16_f32 v182, v160, v161\n"
"v_cvt_pk_bf16_f32 v183, v162, v163\n"
"v_cvt_pk_bf16_f32 v184, v164, v165\n"
"v_cvt_pk_bf16_f32 v185, v166, v167\n"
"v_add_u32 v186, 590336, v244\n"
"v_permlane16_swap_b32 v182, v184\n"
"v_permlane16_swap_b32 v183, v185\n"
"global_store_dwordx4 v186, v[182:185], s[96:97] sc1\n"
"v_cvt_pk_bf16_f32 v152, v32, v33\n"
"v_cvt_pk_bf16_f32 v153, v34, v35\n"
"v_cvt_pk_bf16_f32 v154, v36, v37\n"
"v_cvt_pk_bf16_f32 v155, v38, v39\n"
"v_add_u32 v156, 589952, v240\n"
"v_permlane16_swap_b32 v152, v154\n"
"v_permlane16_swap_b32 v153, v155\n"
"global_store_dwordx4 v156, v[152:155], s[96:97] sc1\n"
"s_branch FFIN_%=\n"
"FSLA_%=:\n"
"global_load_dwordx4 v[164:167], v243, s[96:97] sc1\n"
"global_load_dwordx4 v[168:171], v243, s[96:97] offset:1024 sc1\n"
"global_load_dwordx4 v[172:175], v243, s[96:97] offset:2048 sc1\n"
"global_load_dwordx4 v[176:179], v243, s[96:97] offset:3072 sc1\n"
"global_load_dwordx2 v[180:181], v242, s[96:97] sc1\n"
"s_add_u32 s56, s56, 1\n"
"s_waitcnt vmcnt(0)\n"
"s_cmp_gt_u32 s56, 2000000\n"
"s_cbranch_scc1 FOKA_%=\n"
"s_branch FCKA_%=\n"
"FSLB_%=:\n"
"global_load_dwordx4 v[164:167], v243, s[96:97] sc1\n"
"global_load_dwordx4 v[168:171], v243, s[96:97] offset:1024 sc1\n"
"global_load_dwordx4 v[172:175], v243, s[96:97] offset:2048 sc1\n"
"global_load_dwordx4 v[176:179], v243, s[96:97] offset:3072 sc1\n"
"global_load_dwordx2 v[180:181], v242, s[96:97] sc1\n"
"s_add_u32 s56, s56, 1\n"
"s_waitcnt vmcnt(0)\n"
"s_cmp_gt_u32 s56, 2000000\n"
"s_cbranch_scc1 FOKB_%=\n"
"s_branch FCKB_%=\n"
"FSLC_%=:\n"
"global_load_dwordx4 v[160:163], v196, s[26:27] sc1\n"
"global_load_dwordx4 v[164:167], v196, s[28:29] sc1\n"
"global_load_dwordx4 v[168:171], v196, s[30:31] sc1\n"
"global_load_dwordx4 v[172:175], v197, s[26:27] sc1\n"
"global_load_dwordx4 v[176:179], v197, s[28:29] sc1\n"
"global_load_dwordx4 v[180:183], v197, s[30:31] sc1\n"
"global_load_dwordx4 v[184:187], v198, s[26:27] sc1\n"
"global_load_dwordx4 v[188:191], v198, s[28:29] sc1\n"
"global_load_dwordx4 v[192:195], v198, s[30:31] sc1\n"
"s_add_u32 s60, s60, 1\n"
"s_waitcnt vmcnt(0)\n"
"s_cmp_gt_u32 s60, 2000000\n"
"s_cbranch_scc1 FOKC_%=\n"
"s_branch FCKC_%=\n"
"FAUX_%=:\n"
"s_lshl_b32 s41, s39, 3\n"
"s_add_u32 s41, s41, s40\n"
"s_sub_u32 s41, s41, 24\n"
"s_min_u32 s42, s41, 53\n"
"s_mul_i32 s43, s42, 43\n"
"s_lshr_b32 s43, s43, 7\n"
"s_mul_i32 s44, s43, 3\n"
"s_sub_u32 s44, s42, s44\n"
"s_cmp_lt_u32 s43, 9\n"
"s_cselect_b32 s45, 18, 27\n"
"s_cselect_b32 s46, 27, 36\n"
"s_add_u32 s45, s45, s43\n"
"s_add_u32 s46, s46, s43\n"
"s_mul_i32 s47, s44, 192\n"
"v_lshrrev_b32 v2, 3, %[tid]\n"
"v_lshl_add_u32 v2, v2, 1, s47\n"
"v_lshlrev_b32 v3, 1, v2\n"
"global_load_dword v16, v3, s[86:87]\n"
"global_load_dword v17, v3, s[86:87] offset:128\n"
"global_load_dword v18, v3, s[86:87] offset:256\n"
"v_mul_u32_u24 v2, 6912, v2\n"
"v_and_b32 v3, 7, %[tid]\n"
"v_lshl_add_u32 v20, v3, 4, v2\n"
"v_add_u32 v21, 6912, v20\n"
"v_add_u32 v22, 442368, v20\n"
"v_add_u32 v23, 449280, v20\n"
"v_add_u32 v24, 884736, v20\n"
"v_add_u32 v25, 891648, v20\n"
"s_lshl_b32 s47, s43, 7\n"
"s_add_u32 s26, s88, s47\n"
"s_addc_u32 s27, s89, 0\n"
"global_load_dwordx4 v[28:31], v20, s[26:27]\n"
"global_load_dwordx4 v[32:35], v21, s[26:27]\n"
"global_load_dwordx4 v[36:39], v22, s[26:27]\n"
"global_load_dwordx4 v[40:43], v23, s[26:27]\n"
"global_load_dwordx4 v[44:47], v24, s[26:27]\n"
"global_load_dwordx4 v[48:51], v25, s[26:27]\n"
"s_lshl_b32 s47, s45, 7\n"
"s_add_u32 s28, s88, s47\n"
"s_addc_u32 s29, s89, 0\n"
"global_load_dwordx4 v[52:55], v20, s[28:29]\n"
"global_load_dwordx4 v[56:59], v21, s[28:29]\n"
"global_load_dwordx4 v[60:63], v22, s[28:29]\n"
"global_load_dwordx4 v[64:67], v23, s[28:29]\n"
"global_load_dwordx4 v[68:71], v24, s[28:29]\n"
"global_load_dwordx4 v[72:75], v25, s[28:29]\n"
"s_lshl_b32 s47, s46, 7\n"
"s_add_u32 s30, s88, s47\n"
"s_addc_u32 s31, s89, 0\n"
"global_load_dwordx4 v[76:79], v20, s[30:31]\n"
"global_load_dwordx4 v[80:83], v21, s[30:31]\n"
"global_load_dwordx4 v[84:87], v22, s[30:31]\n"
"global_load_dwordx4 v[88:91], v23, s[30:31]\n"
"global_load_dwordx4 v[92:95], v24, s[30:31]\n"
"global_load_dwordx4 v[96:99], v25, s[30:31]\n"
"s_mov_b32 s48, 0x80000080\n"
"s_mov_b32 s49, 0x00800000\n"
"s_and_b32 s58, %[blk], 63\n"
"s_lshl_b32 s58, s58, 4\n"
"s_lshr_b32 s59, %[blk], 6\n"
"s_lshl_b32 s59, s59, 2\n"
"s_add_u32 s59, s59, s33\n"
"s_lshl_b32 s59, s59, 10\n"
"s_add_u32 s58, s58, s59\n"
"s_add_u32 s58, s58, 32047104\n"
"s_mov_b64 s[50:51], 1\n"
"s_mov_b64 s[64:65], 2\n"
"v_mov_b32 v157, 0x05040100\n"
"v_mov_b32 v158, 0x07060302\n"
"v_cndmask_b32 v157, v157, v158, s[48:49]\n"
"v_mov_b32 v140, 0\n"
"v_mov_b32 v141, 0\n"
"v_mov_b32 v142, 0\n"
"v_mov_b32 v143, 0\n"
"s_waitcnt vmcnt(24)\n"
"v_dot2c_f32_bf16 v140, 0x3f803f80, v4\n"
"v_dot2c_f32_bf16 v141, v4, v4\n"
"s_waitcnt vmcnt(23)\n"
"v_dot2c_f32_bf16 v142, 0x3f803f80, v8\n"
"v_dot2c_f32_bf16 v143, v8, v8\n"
"v_dot2c_f32_bf16 v140, 0x3f803f80, v5\n"
"v_dot2c_f32_bf16 v141, v5, v5\n"
"v_dot2c_f32_bf16 v142, 0x3f803f80, v9\n"
"v_dot2c_f32_bf16 v143, v9, v9\n"
"v_dot2c_f32_bf16 v140, 0x3f803f80, v6\n"
"v_dot2c_f32_bf16 v141, v6, v6\n"
"v_dot2c_f32_bf16 v142, 0x3f803f80, v10\n"
"v_dot2c_f32_bf16 v143, v10, v10\n"
"v_dot2c_f32_bf16 v140, 0x3f803f80, v7\n"
"v_dot2c_f32_bf16 v141, v7, v7\n"
"v_dot2c_f32_bf16 v142, 0x3f803f80, v11\n"
"v_dot2c_f32_bf16 v143, v11, v11\n"
"s_nop 1\n"
"v_permlane32_swap_b32 v140, v142\n"
"v_permlane32_swap_b32 v141, v143\n"
"v_add_f32 v140, v140, v142\n"
"v_add_f32 v141, v141, v143\n"
"s_waitcnt vmcnt(22)\n"
"v_dot2c_f32_bf16 v140, 0x3f803f80, v12\n"
"v_dot2c_f32_bf16 v141, v12, v12\n"
"s_waitcnt vmcnt(21)\n"
"v_add_u32 v13, 1, v13\n"
"s_nop 0\n"
"v_permlane16_swap_b32 v140, v141\n"
"v_add_f32 v140, v140, v141\n"
"v_cndmask_b32 v156, v4, v7, s[48:49]\n"
"v_mov_b32 v154, v12\n"
"v_add_f32_dpp v140, v140, v140 row_ror:8 row_mask:0xf bank_mask:0xf bound_ctrl:1\n"
"v_cndmask_b32 v152, v8, v11, s[48:49]\n"
"v_lshl_add_u32 v145, v1, 14, s58\n"
"v_add_f32_dpp v140, v140, v140 row_ror:4 row_mask:0xf bank_mask:0xf bound_ctrl:1\n"
"v_permlane32_swap_b32 v154, v155\n"
"v_cndmask_b32 v156, v156, v12, s[50:51]\n"
"v_add_f32_dpp v140, v140, v140 row_ror:2 row_mask:0xf bank_mask:0xf bound_ctrl:1\n"
"s_nop 0\n"
"v_cndmask_b32 v152, v152, v155, s[50:51]\n"
"v_add_f32_dpp v140, v140, v140 row_ror:1 row_mask:0xf bank_mask:0xf bound_ctrl:1\n"
"v_perm_b32 v152, v152, v156, v157\n"
"v_mul_f32 v140, 0x3ae38e39, v140\n"
"v_mov_b32 v141, v140\n"
"s_nop 1\n"
"v_permlane16_swap_b32 v140, v141\n"
"v_fma_f32 v141, -v140, v140, v141\n"
"v_max_f32 v141, 0, v141\n"
"v_cvt_pk_bf16_f32 v141, v15, v141\n"
"v_add_f32 v141, 0x3727c5ac, v141\n"
"v_cvt_pk_bf16_f32 v141, v15, v141\n"
"v_sqrt_f32 v141, v141\n"
"s_nop 0\n"
"v_cvt_pk_bf16_f32 v150, v140, v141\n"
"v_mov_b32 v151, v150\n"
"s_nop 1\n"
"v_permlane32_swap_b32 v150, v151\n"
"v_bitop3_b32 v153, v150, v151, v13 bitop3:0x96\n"
"s_mov_b32 exec_lo, 0x80010083\n"
"s_mov_b32 exec_hi, 0x00800100\n"
"global_store_dwordx4 v145, v[150:153], s[96:97] sc1\n"
"s_mov_b64 exec, -1\n"
"s_sub_u32 s47, s40, 24\n"
"s_lshl_b32 s47, s47, 2\n"
"s_add_u32 s47, s47, s33\n"
"s_lshl_b32 s47, s47, 11\n"
"s_add_u32 s47, s47, 0x8000\n"
"v_and_b32 v139, 15, v1\n"
"v_lshl_add_u32 v139, v139, 7, s47\n"
"v_min_u32 v139, END_%=-FPC_%=-4, v139\n"
"global_load_dword v14, v139, s[18:19]\n"
"s_waitcnt vmcnt(22)\n"
"v_lshlrev_b32 v100, 16, v16\n"
"s_waitcnt vmcnt(21)\n"
"v_lshlrev_b32 v104, 16, v17\n"
"s_waitcnt vmcnt(20)\n"
"v_lshlrev_b32 v108, 16, v18\n"
"v_and_b32 v101, 0xffff0000, v16\n"
"v_and_b32 v105, 0xffff0000, v17\n"
"v_and_b32 v109, 0xffff0000, v18\n"
"v_mul_f32 v102, 0xbfb8aa3b, v100\n"
"v_mul_f32 v106, 0xbfb8aa3b, v104\n"
"v_mul_f32 v110, 0xbfb8aa3b, v108\n"
"v_mul_f32 v103, 0xbfb8aa3b, v101\n"
"v_mul_f32 v107, 0xbfb8aa3b, v105\n"
"v_mul_f32 v111, 0xbfb8aa3b, v109\n"
"v_exp_f32 v102, v102\n"
"v_exp_f32 v106, v106\n"
"v_exp_f32 v110, v110\n"
"v_exp_f32 v103, v103\n"
"v_exp_f32 v107, v107\n"
"v_exp_f32 v111, v111\n"
"v_add_f32 v102, 1.0, v102\n"
"v_add_f32 v106, 1.0, v106\n"
"v_add_f32 v110, 1.0, v110\n"
"v_add_f32 v103, 1.0, v103\n"
"v_add_f32 v107, 1.0, v107\n"
"v_add_f32 v111, 1.0, v111\n"
"v_rcp_f32 v102, v102\n"
"v_rcp_f32 v106, v106\n"
"v_rcp_f32 v110, v110\n"
"v_rcp_f32 v103, v103\n"
"v_rcp_f32 v107, v107\n"
"v_rcp_f32 v111, v111\n"
"v_pk_mul_f32 v[100:101], v[100:101], v[102:103]\n"
"v_pk_mul_f32 v[104:105], v[104:105], v[106:107]\n"
"v_pk_mul_f32 v[108:109], v[108:109], v[110:111]\n"
"v_cvt_pk_bf16_f32 v16, v100, v101\n"
"v_cvt_pk_bf16_f32 v17, v104, v105\n"
"v_cvt_pk_bf16_f32 v18, v108, v109\n"
"s_mov_b32 s47, 0x05040100\n"
"s_mov_b32 s48, 0x07060302\n"
"v_and_b32 v2, 8, v1\n"
"v_cmp_ne_u32 s[50:51], 0, v2\n"
"v_and_b32 v2, 7, v1\n"
"v_lshrrev_b32 v3, 3, v1\n"
"v_lshl_add_u32 v2, v2, 3, v3\n"
"s_mul_i32 s49, s44, 3456\n"
"v_add_u32 v2, s49, v2\n"
"v_lshlrev_b32 v2, 3, v2\n"
"v_add_u32 v2, 36372480, v2\n"
"v_lshlrev_b32 v3, 2, v1\n"
"v_mov_b32 v112, 0\n"
"v_mov_b32 v113, 0\n"
"v_mov_b32 v114, 0\n"
"v_mov_b32 v115, 0\n"
"v_mov_b32 v116, 0\n"
"v_mov_b32 v117, 0\n"
"v_mov_b32 v118, 0\n"
"v_mov_b32 v119, 0\n"
"s_waitcnt vmcnt(18)\n"
"v_perm_b32 v120, v32, v28, s47\n"
"v_perm_b32 v121, v32, v28, s48\n"
"v_dot2c_f32_bf16 v112, v120, v16\n"
"v_dot2c_f32_bf16 v113, v121, v16\n"
"v_perm_b32 v122, v33, v29, s47\n"
"v_perm_b32 v123, v33, v29, s48\n"
"v_dot2c_f32_bf16 v114, v122, v16\n"
"v_dot2c_f32_bf16 v115, v123, v16\n"
"v_perm_b32 v120, v34, v30, s47\n"
"v_perm_b32 v121, v34, v30, s48\n"
"v_dot2c_f32_bf16 v116, v120, v16\n"
"v_dot2c_f32_bf16 v117, v121, v16\n"
"v_perm_b32 v122, v35, v31, s47\n"
"v_perm_b32 v123, v35, v31, s48\n"
"v_dot2c_f32_bf16 v118, v122, v16\n"
"v_dot2c_f32_bf16 v119, v123, v16\n"
"s_waitcnt vmcnt(16)\n"
"v_perm_b32 v120, v40, v36, s47\n"
"v_perm_b32 v121, v40, v36, s48\n"
"v_dot2c_f32_bf16 v112, v120, v17\n"
"v_dot2c_f32_bf16 v113, v121, v17\n"
"v_perm_b32 v122, v41, v37, s47\n"
"v_perm_b32 v123, v41, v37, s48\n"
"v_dot2c_f32_bf16 v114, v122, v17\n"
"v_dot2c_f32_bf16 v115, v123, v17\n"
"v_perm_b32 v120, v42, v38, s47\n"
"v_perm_b32 v121, v42, v38, s48\n"
"v_dot2c_f32_bf16 v116, v120, v17\n"
"v_dot2c_f32_bf16 v117, v121, v17\n"
"v_perm_b32 v122, v43, v39, s47\n"
"v_perm_b32 v123, v43, v39, s48\n"
"v_dot2c_f32_bf16 v118, v122, v17\n"
"v_dot2c_f32_bf16 v119, v123, v17\n"
"s_waitcnt vmcnt(14)\n"
"v_perm_b32 v120, v48, v44, s47\n"
"v_perm_b32 v121, v48, v44, s48\n"
"v_dot2c_f32_bf16 v112, v120, v18\n"
"v_dot2c_f32_bf16 v113, v121, v18\n"
"v_perm_b32 v122, v49, v45, s47\n"
"v_perm_b32 v123, v49, v45, s48\n"
"v_dot2c_f32_bf16 v114, v122, v18\n"
"v_dot2c_f32_bf16 v115, v123, v18\n"
"v_perm_b32 v120, v50, v46, s47\n"
"v_perm_b32 v121, v50, v46, s48\n"
"v_dot2c_f32_bf16 v116, v120, v18\n"
"v_dot2c_f32_bf16 v117, v121, v18\n"
"v_perm_b32 v122, v51, v47, s47\n"
"v_perm_b32 v123, v51, v47, s48\n"
"v_dot2c_f32_bf16 v118, v122, v18\n"
"v_dot2c_f32_bf16 v119, v123, v18\n"
"v_permlane32_swap_b32 v112, v116\n"
"v_permlane32_swap_b32 v113, v117\n"
"v_permlane32_swap_b32 v114, v118\n"
"v_permlane32_swap_b32 v115, v119\n"
"v_add_f32 v112, v112, v116\n"
"v_add_f32 v113, v113, v117\n"
"v_add_f32 v114, v114, v118\n"
"v_add_f32 v115, v115, v119\n"
"s_nop 0\n"
"v_permlane16_swap_b32 v112, v114\n"
"v_permlane16_swap_b32 v113, v115\n"
"v_add_f32 v112, v112, v114\n"
"v_add_f32 v113, v113, v115\n"
"s_nop 0\n"
"v_add_f32_dpp v112, v112, v112 row_ror:8 row_mask:0xf bank_mask:0xf\n"
"v_add_f32_dpp v113, v113, v113 row_ror:8 row_mask:0xf bank_mask:0xf\n"
"s_lshl_b32 m0, s33, 8\n"
"v_cndmask_b32 v124, v112, v113, s[50:51]\n"
"ds_write_addtid_b32 v124\n"
"v_mov_b32 v112, 0\n"
"v_mov_b32 v113, 0\n"
"v_mov_b32 v114, 0\n"
"v_mov_b32 v115, 0\n"
"v_mov_b32 v116, 0\n"
"v_mov_b32 v117, 0\n"
"v_mov_b32 v118, 0\n"
"v_mov_b32 v119, 0\n"
"s_waitcnt vmcnt(12)\n"
"v_perm_b32 v120, v56, v52, s47\n"
"v_perm_b32 v121, v56, v52, s48\n"
"v_dot2c_f32_bf16 v112, v120, v16\n"
"v_dot2c_f32_bf16 v113, v121, v16\n"
"v_perm_b32 v122, v57, v53, s47\n"
"v_perm_b32 v123, v57, v53, s48\n"
"v_dot2c_f32_bf16 v114, v122, v16\n"
"v_dot2c_f32_bf16 v115, v123, v16\n"
"v_perm_b32 v120, v58, v54, s47\n"
"v_perm_b32 v121, v58, v54, s48\n"
"v_dot2c_f32_bf16 v116, v120, v16\n"
"v_dot2c_f32_bf16 v117, v121, v16\n"
"v_perm_b32 v122, v59, v55, s47\n"
"v_perm_b32 v123, v59, v55, s48\n"
"v_dot2c_f32_bf16 v118, v122, v16\n"
"v_dot2c_f32_bf16 v119, v123, v16\n"
"s_waitcnt vmcnt(10)\n"
"v_perm_b32 v120, v64, v60, s47\n"
"v_perm_b32 v121, v64, v60, s48\n"
"v_dot2c_f32_bf16 v112, v120, v17\n"
"v_dot2c_f32_bf16 v113, v121, v17\n"
"v_perm_b32 v122, v65, v61, s47\n"
"v_perm_b32 v123, v65, v61, s48\n"
"v_dot2c_f32_bf16 v114, v122, v17\n"
"v_dot2c_f32_bf16 v115, v123, v17\n"
"v_perm_b32 v120, v66, v62, s47\n"
"v_perm_b32 v121, v66, v62, s48\n"
"v_dot2c_f32_bf16 v116, v120, v17\n"
"v_dot2c_f32_bf16 v117, v121, v17\n"
"v_perm_b32 v122, v67, v63, s47\n"
"v_perm_b32 v123, v67, v63, s48\n"
"v_dot2c_f32_bf16 v118, v122, v17\n"
"v_dot2c_f32_bf16 v119, v123, v17\n"
"s_waitcnt vmcnt(8)\n"
"v_perm_b32 v120, v72, v68, s47\n"
"v_perm_b32 v121, v72, v68, s48\n"
"v_dot2c_f32_bf16 v112, v120, v18\n"
"v_dot2c_f32_bf16 v113, v121, v18\n"
"v_perm_b32 v122, v73, v69, s47\n"
"v_perm_b32 v123, v73, v69, s48\n"
"v_dot2c_f32_bf16 v114, v122, v18\n"
"v_dot2c_f32_bf16 v115, v123, v18\n"
"v_perm_b32 v120, v74, v70, s47\n"
"v_perm_b32 v121, v74, v70, s48\n"
"v_dot2c_f32_bf16 v116, v120, v18\n"
"v_dot2c_f32_bf16 v117, v121, v18\n"
"v_perm_b32 v122, v75, v71, s47\n"
"v_perm_b32 v123, v75, v71, s48\n"
"v_dot2c_f32_bf16 v118, v122, v18\n"
"v_dot2c_f32_bf16 v119, v123, v18\n"
"v_permlane32_swap_b32 v112, v116\n"
"v_permlane32_swap_b32 v113, v117\n"
"v_permlane32_swap_b32 v114, v118\n"
"v_permlane32_swap_b32 v115, v119\n"
"v_add_f32 v112, v112, v116\n"
"v_add_f32 v113, v113, v117\n"
"v_add_f32 v114, v114, v118\n"
"v_add_f32 v115, v115, v119\n"
"s_nop 0\n"
"v_permlane16_swap_b32 v112, v114\n"
"v_permlane16_swap_b32 v113, v115\n"
"v_add_f32 v112, v112, v114\n"
"v_add_f32 v113, v113, v115\n"
"s_nop 0\n"
"v_add_f32_dpp v112, v112, v112 row_ror:8 row_mask:0xf bank_mask:0xf\n"
"v_add_f32_dpp v113, v113, v113 row_ror:8 row_mask:0xf bank_mask:0xf\n"
"s_lshl_b32 m0, s33, 8\n"
"v_cndmask_b32 v125, v112, v113, s[50:51]\n"
"ds_write_addtid_b32 v125 offset:1024\n"
"s_waitcnt lgkmcnt(0)\n"
"s_barrier\n"
"s_cmp_lt_u32 s33, 0\n"
"s_cbranch_scc1 FAa_%=\n"
"s_cmp_gt_u32 s33, 1\n"
"s_cbranch_scc1 FAa_%=\n"
"s_cmp_gt_u32 s41, 53\n"
"s_cbranch_scc1 FAa_%=\n"
"s_lshl_b32 s49, s33, 10\n"
"v_add_u32 v100, s49, v3\n"
"ds_read_b32 v104, v100\n"
"ds_read_b32 v105, v100 offset:256\n"
"ds_read_b32 v106, v100 offset:512\n"
"ds_read_b32 v107, v100 offset:768\n"
"s_cmp_eq_u32 s33, 1\n"
"s_cselect_b32 s49, s45, s43\n"
"s_cmp_eq_u32 s33, 2\n"
"s_cselect_b32 s49, s46, s49\n"
"s_lshl_b32 s49, s49, 9\n"
"v_add_u32 v101, s49, v2\n"
"v_mov_b32 v103, v13\n"
"s_waitcnt lgkmcnt(2)\n"
"v_add_f32 v104, v104, v105\n"
"s_waitcnt lgkmcnt(0)\n"
"v_add_f32 v106, v106, v107\n"
"v_add_f32 v102, v104, v106\n"
"global_store_dwordx2 v101, v[102:103], s[96:97] sc1\n"
"FAa_%=:\n"
"s_waitcnt lgkmcnt(0)\n"
"s_cmp_lt_u32 s39, 4\n"
"s_cselect_b32 s52, s90, s92\n"
"s_cselect_b32 s53, s91, s93\n"
"s_and_b32 s53, s53, 0xffff\n"
"s_mov_b32 s54, 5308416\n"
"s_mov_b32 s55, 0x20000\n"
"s_and_b32 s56, s39, 3\n"
"s_mul_i32 s56, s56, 2304\n"
"s_sub_u32 s57, s40, 24\n"
"s_mul_i32 s57, s57, 288\n"
"s_add_u32 s56, s56, s57\n"
"s_mul_i32 s57, s33, 1327104\n"
"s_add_u32 s56, s56, s57\n"
"s_mul_i32 s58, s33, 36864\n"
"s_add_u32 s58, s58, 10240\n"
"v_add_u32 v138, 0, v1\n"
"v_mul_u32_u24 v139, 3641, v138\n"
"v_lshrrev_b32 v139, 16, v139\n"
"v_mul_u32_u24 v139, 8928, v139\n"
"v_lshl_add_u32 v128, v138, 4, v139\n"
"v_add_u32 v138, 64, v1\n"
"v_mul_u32_u24 v139, 3641, v138\n"
"v_lshrrev_b32 v139, 16, v139\n"
"v_mul_u32_u24 v139, 8928, v139\n"
"v_lshl_add_u32 v129, v138, 4, v139\n"
"v_subrev_u32 v129, 1024, v129\n"
"v_add_u32 v138, 128, v1\n"
"v_mul_u32_u24 v139, 3641, v138\n"
"v_lshrrev_b32 v139, 16, v139\n"
"v_mul_u32_u24 v139, 8928, v139\n"
"v_lshl_add_u32 v130, v138, 4, v139\n"
"v_subrev_u32 v130, 2048, v130\n"
"v_add_u32 v138, 192, v1\n"
"v_mul_u32_u24 v139, 3641, v138\n"
"v_lshrrev_b32 v139, 16, v139\n"
"v_mul_u32_u24 v139, 8928, v139\n"
"v_lshl_add_u32 v131, v138, 4, v139\n"
"v_subrev_u32 v131, 3072, v131\n"
"v_add_u32 v138, 256, v1\n"
"v_mul_u32_u24 v139, 3641, v138\n"
"v_lshrrev_b32 v139, 16, v139\n"
"v_mul_u32_u24 v139, 8928, v139\n"
"v_lshl_add_u32 v132, v138, 4, v139\n"
"v_add_u32 v138, 320, v1\n"
"v_mul_u32_u24 v139, 3641, v138\n"
"v_lshrrev_b32 v139, 16, v139\n"
"v_mul_u32_u24 v139, 8928, v139\n"
"v_lshl_add_u32 v133, v138, 4, v139\n"
"v_subrev_u32 v133, 1024, v133\n"
"v_add_u32 v138, 384, v1\n"
"v_mul_u32_u24 v139, 3641, v138\n"
"v_lshrrev_b32 v139, 16, v139\n"
"v_mul_u32_u24 v139, 8928, v139\n"
"v_lshl_add_u32 v134, v138, 4, v139\n"
"v_subrev_u32 v134, 2048, v134\n"
"v_add_u32 v138, 448, v1\n"
"v_mul_u32_u24 v139, 3641, v138\n"
"v_lshrrev_b32 v139, 16, v139\n"
"v_mul_u32_u24 v139, 8928, v139\n"
"v_lshl_add_u32 v135, v138, 4, v139\n"
"v_subrev_u32 v135, 3072, v135\n"
"v_add_u32 v138, 512, v1\n"
"v_mul_u32_u24 v139, 3641, v138\n"
"v_lshrrev_b32 v139, 16, v139\n"
"v_mul_u32_u24 v139, 8928, v139\n"
"v_lshl_add_u32 v136, v138, 4, v139\n"
"v_min_u32 v138, 31, v1\n"
"v_add_u32 v138, 256, v138\n"
"v_mul_u32_u24 v139, 3641, v138\n"
"v_lshrrev_b32 v139, 16, v139\n"
"v_mul_u32_u24 v139, 8928, v139\n"
"v_lshl_add_u32 v137, v138, 4, v139\n"
"s_add_u32 s59, s56, 0\n"
"s_add_u32 m0, s58, 0\n"
"s_nop 0\n"
"buffer_load_dwordx4 v128, s[52:55], s59 offen lds\n"
"buffer_load_dwordx4 v129, s[52:55], s59 offen offset:1024 lds\n"
"buffer_load_dwordx4 v130, s[52:55], s59 offen offset:2048 lds\n"
"buffer_load_dwordx4 v131, s[52:55], s59 offen offset:3072 lds\n"
"s_add_u32 m0, s58, 4096\n"
"s_nop 0\n"
"buffer_load_dwordx4 v132, s[52:55], s59 offen lds\n"
"buffer_load_dwordx4 v133, s[52:55], s59 offen offset:1024 lds\n"
"buffer_load_dwordx4 v134, s[52:55], s59 offen offset:2048 lds\n"
"buffer_load_dwordx4 v135, s[52:55], s59 offen offset:3072 lds\n"
"s_add_u32 m0, s58, 8192\n"
"s_nop 0\n"
"buffer_load_dwordx4 v136, s[52:55], s59 offen lds\n"
"s_add_u32 s59, s56, 294912\n"
"s_add_u32 m0, s58, 9216\n"
"s_nop 0\n"
"buffer_load_dwordx4 v128, s[52:55], s59 offen lds\n"
"buffer_load_dwordx4 v129, s[52:55], s59 offen offset:1024 lds\n"
"buffer_load_dwordx4 v130, s[52:55], s59 offen offset:2048 lds\n"
"buffer_load_dwordx4 v131, s[52:55], s59 offen offset:3072 lds\n"
"s_add_u32 m0, s58, 13312\n"
"s_nop 0\n"
"buffer_load_dwordx4 v132, s[52:55], s59 offen lds\n"
"buffer_load_dwordx4 v133, s[52:55], s59 offen offset:1024 lds\n"
"buffer_load_dwordx4 v134, s[52:55], s59 offen offset:2048 lds\n"
"buffer_load_dwordx4 v135, s[52:55], s59 offen offset:3072 lds\n"
"s_add_u32 m0, s58, 17408\n"
"s_nop 0\n"
"buffer_load_dwordx4 v136, s[52:55], s59 offen lds\n"
"v_mov_b32 v112, 0\n"
"v_mov_b32 v113, 0\n"
"v_mov_b32 v114, 0\n"
"v_mov_b32 v115, 0\n"
"v_mov_b32 v116, 0\n"
"v_mov_b32 v117, 0\n"
"v_mov_b32 v118, 0\n"
"v_mov_b32 v119, 0\n"
"s_waitcnt vmcnt(24)\n"
"v_perm_b32 v120, v80, v76, s47\n"
"v_perm_b32 v121, v80, v76, s48\n"
"v_dot2c_f32_bf16 v112, v120, v16\n"
"v_dot2c_f32_bf16 v113, v121, v16\n"
"v_perm_b32 v122, v81, v77, s47\n"
"v_perm_b32 v123, v81, v77, s48\n"
"v_dot2c_f32_bf16 v114, v122, v16\n"
"v_dot2c_f32_bf16 v115, v123, v16\n"
"v_perm_b32 v120, v82, v78, s47\n"
"v_perm_b32 v121, v82, v78, s48\n"
"v_dot2c_f32_bf16 v116, v120, v16\n"
"v_dot2c_f32_bf16 v117, v121, v16\n"
"v_perm_b32 v122, v83, v79, s47\n"
"v_perm_b32 v123, v83, v79, s48\n"
"v_dot2c_f32_bf16 v118, v122, v16\n"
"v_dot2c_f32_bf16 v119, v123, v16\n"
"s_waitcnt vmcnt(22)\n"
"v_perm_b32 v120, v88, v84, s47\n"
"v_perm_b32 v121, v88, v84, s48\n"
"v_dot2c_f32_bf16 v112, v120, v17\n"
"v_dot2c_f32_bf16 v113, v121, v17\n"
"v_perm_b32 v122, v89, v85, s47\n"
"v_perm_b32 v123, v89, v85, s48\n"
"v_dot2c_f32_bf16 v114, v122, v17\n"
"v_dot2c_f32_bf16 v115, v123, v17\n"
"v_perm_b32 v120, v90, v86, s47\n"
"v_perm_b32 v121, v90, v86, s48\n"
"v_dot2c_f32_bf16 v116, v120, v17\n"
"v_dot2c_f32_bf16 v117, v121, v17\n"
"v_perm_b32 v122, v91, v87, s47\n"
"v_perm_b32 v123, v91, v87, s48\n"
"v_dot2c_f32_bf16 v118, v122, v17\n"
"v_dot2c_f32_bf16 v119, v123, v17\n"
"s_waitcnt vmcnt(20)\n"
"v_perm_b32 v120, v96, v92, s47\n"
"v_perm_b32 v121, v96, v92, s48\n"
"v_dot2c_f32_bf16 v112, v120, v18\n"
"v_dot2c_f32_bf16 v113, v121, v18\n"
"v_perm_b32 v122, v97, v93, s47\n"
"v_perm_b32 v123, v97, v93, s48\n"
"v_dot2c_f32_bf16 v114, v122, v18\n"
"v_dot2c_f32_bf16 v115, v123, v18\n"
"v_perm_b32 v120, v98, v94, s47\n"
"v_perm_b32 v121, v98, v94, s48\n"
"v_dot2c_f32_bf16 v116, v120, v18\n"
"v_dot2c_f32_bf16 v117, v121, v18\n"
"v_perm_b32 v122, v99, v95, s47\n"
"v_perm_b32 v123, v99, v95, s48\n"
"v_dot2c_f32_bf16 v118, v122, v18\n"
"v_dot2c_f32_bf16 v119, v123, v18\n"
"v_permlane32_swap_b32 v112, v116\n"
"v_permlane32_swap_b32 v113, v117\n"
"v_permlane32_swap_b32 v114, v118\n"
"v_permlane32_swap_b32 v115, v119\n"
"v_add_f32 v112, v112, v116\n"
"v_add_f32 v113, v113, v117\n"
"v_add_f32 v114, v114, v118\n"
"v_add_f32 v115, v115, v119\n"
"s_nop 0\n"
"v_permlane16_swap_b32 v112, v114\n"
"v_permlane16_swap_b32 v113, v115\n"
"v_add_f32 v112, v112, v114\n"
"v_add_f32 v113, v113, v115\n"
"s_nop 0\n"
"v_add_f32_dpp v112, v112, v112 row_ror:8 row_mask:0xf bank_mask:0xf\n"
"v_add_f32_dpp v113, v113, v113 row_ror:8 row_mask:0xf bank_mask:0xf\n"
"s_lshl_b32 m0, s33, 8\n"
"v_cndmask_b32 v126, v112, v113, s[50:51]\n"
"ds_write_addtid_b32 v126 offset:2048\n"
"s_waitcnt lgkmcnt(0)\n"
"s_barrier\n"
"s_cmp_lt_u32 s33, 2\n"
"s_cbranch_scc1 FAb_%=\n"
"s_cmp_gt_u32 s33, 2\n"
"s_cbranch_scc1 FAb_%=\n"
"s_cmp_gt_u32 s41, 53\n"
"s_cbranch_scc1 FAb_%=\n"
"s_lshl_b32 s49, s33, 10\n"
"v_add_u32 v100, s49, v3\n"
"ds_read_b32 v104, v100\n"
"ds_read_b32 v105, v100 offset:256\n"
"ds_read_b32 v106, v100 offset:512\n"
"ds_read_b32 v107, v100 offset:768\n"
"s_cmp_eq_u32 s33, 1\n"
"s_cselect_b32 s49, s45, s43\n"
"s_cmp_eq_u32 s33, 2\n"
"s_cselect_b32 s49, s46, s49\n"
"s_lshl_b32 s49, s49, 9\n"
"v_add_u32 v101, s49, v2\n"
"v_mov_b32 v103, v13\n"
"s_waitcnt lgkmcnt(2)\n"
"v_add_f32 v104, v104, v105\n"
"s_waitcnt lgkmcnt(0)\n"
"v_add_f32 v106, v106, v107\n"
"v_add_f32 v102, v104, v106\n"
"global_store_dwordx2 v101, v[102:103], s[96:97] sc1\n"
"FAb_%=:\n"
"s_waitcnt lgkmcnt(0)\n"
"v_add_u32 v2, 0, v1\n"
"v_min_u32 v2, 143, v2\n"
"v_cmp_gt_u32 vcc, 72, v2\n"
"s_mul_i32 s47, s33, 72\n"
"v_add_u32 v2, s47, v2\n"
"v_mov_b32 v3, 12672\n"
"v_mov_b32 v4, 18432\n"
"v_cndmask_b32 v3, v3, v4, vcc\n"
"v_lshl_add_u32 v196, v2, 4, v3\n"
"v_mov_b32 v3, 3936\n"
"v_mov_b32 v4, 3072\n"
"v_cndmask_b32 v3, v3, v4, vcc\n"
"v_lshl_add_u32 v200, v2, 2, v3\n"
"v_cndmask_b32 v204, 0, 1.0, vcc\n"
"v_add_u32 v2, 64, v1\n"
"v_min_u32 v2, 143, v2\n"
"v_cmp_gt_u32 vcc, 72, v2\n"
"s_mul_i32 s47, s33, 72\n"
"v_add_u32 v2, s47, v2\n"
"v_mov_b32 v3, 12672\n"
"v_mov_b32 v4, 18432\n"
"v_cndmask_b32 v3, v3, v4, vcc\n"
"v_lshl_add_u32 v197, v2, 4, v3\n"
"v_mov_b32 v3, 3936\n"
"v_mov_b32 v4, 3072\n"
"v_cndmask_b32 v3, v3, v4, vcc\n"
"v_lshl_add_u32 v201, v2, 2, v3\n"
"v_cndmask_b32 v205, 0, 1.0, vcc\n"
"v_add_u32 v2, 128, v1\n"
"v_min_u32 v2, 143, v2\n"
"v_cmp_gt_u32 vcc, 72, v2\n"
"s_mul_i32 s47, s33, 72\n"
"v_add_u32 v2, s47, v2\n"
"v_mov_b32 v3, 12672\n"
"v_mov_b32 v4, 18432\n"
"v_cndmask_b32 v3, v3, v4, vcc\n"
"v_lshl_add_u32 v198, v2, 4, v3\n"
"v_mov_b32 v3, 3936\n"
"v_mov_b32 v4, 3072\n"
"v_cndmask_b32 v3, v3, v4, vcc\n"
"v_lshl_add_u32 v202, v2, 2, v3\n"
"v_cndmask_b32 v206, 0, 1.0, vcc\n"
"s_add_u32 s26, s96, 36372480\n"
"s_addc_u32 s27, s97, 0\n"
"s_add_u32 s28, s96, 36400128\n"
"s_addc_u32 s29, s97, 0\n"
"s_add_u32 s30, s96, 36427776\n"
"s_addc_u32 s31, s97, 0\n"
"s_add_u32 s59, s56, 589824\n"
"s_add_u32 m0, s58, 18432\n"
"s_nop 0\n"
"buffer_load_dwordx4 v128, s[52:55], s59 offen lds\n"
"buffer_load_dwordx4 v129, s[52:55], s59 offen offset:1024 lds\n"
"buffer_load_dwordx4 v130, s[52:55], s59 offen offset:2048 lds\n"
"buffer_load_dwordx4 v131, s[52:55], s59 offen offset:3072 lds\n"
"s_add_u32 m0, s58, 22528\n"
"s_nop 0\n"
"buffer_load_dwordx4 v132, s[52:55], s59 offen lds\n"
"buffer_load_dwordx4 v133, s[52:55], s59 offen offset:1024 lds\n"
"buffer_load_dwordx4 v134, s[52:55], s59 offen offset:2048 lds\n"
"buffer_load_dwordx4 v135, s[52:55], s59 offen offset:3072 lds\n"
"s_add_u32 m0, s58, 26624\n"
"s_nop 0\n"
"buffer_load_dwordx4 v136, s[52:55], s59 offen lds\n"
"global_load_dwordx4 v[160:163], v196, s[26:27] sc1\n"
"global_load_dwordx4 v[164:167], v196, s[28:29] sc1\n"
"global_load_dwordx4 v[168:171], v196, s[30:31] sc1\n"
"global_load_dwordx4 v[172:175], v197, s[26:27] sc1\n"
"global_load_dwordx4 v[176:179], v197, s[28:29] sc1\n"
"global_load_dwordx4 v[180:183], v197, s[30:31] sc1\n"
"global_load_dwordx4 v[184:187], v198, s[26:27] sc1\n"
"global_load_dwordx4 v[188:191], v198, s[28:29] sc1\n"
"global_load_dwordx4 v[192:195], v198, s[30:31] sc1\n"
"s_add_u32 s59, s56, 884736\n"
"s_add_u32 m0, s58, 27648\n"
"s_nop 0\n"
"buffer_load_dwordx4 v128, s[52:55], s59 offen lds\n"
"buffer_load_dwordx4 v129, s[52:55], s59 offen offset:1024 lds\n"
"buffer_load_dwordx4 v130, s[52:55], s59 offen offset:2048 lds\n"
"buffer_load_dwordx4 v131, s[52:55], s59 offen offset:3072 lds\n"
"s_add_u32 m0, s58, 31744\n"
"s_nop 0\n"
"buffer_load_dwordx4 v132, s[52:55], s59 offen lds\n"
"buffer_load_dwordx4 v133, s[52:55], s59 offen offset:1024 lds\n"
"buffer_load_dwordx4 v134, s[52:55], s59 offen offset:2048 lds\n"
"buffer_load_dwordx4 v135, s[52:55], s59 offen offset:3072 lds\n"
"s_add_u32 m0, s58, 35840\n"
"s_nop 0\n"
"buffer_load_dwordx4 v136, s[52:55], s59 offen lds\n"
"s_add_u32 s59, s56, 1179648\n"
"buffer_load_dwordx4 v[140:143], v128, s[52:55], s59 offen\n"
"buffer_load_dwordx4 v[144:147], v129, s[52:55], s59 offen offset:1024\n"
"buffer_load_dwordx4 v[148:151], v130, s[52:55], s59 offen offset:2048\n"
"buffer_load_dwordx4 v[152:155], v131, s[52:55], s59 offen offset:3072\n"
"buffer_load_dwordx4 v[156:159], v137, s[52:55], s59 offen\n"
"v_lshrrev_b32 v2, 2, v1\n"
"v_mul_u32_u24 v2, 288, v2\n"
"v_and_b32 v3, 3, v1\n"
"v_lshl_add_u32 v2, v3, 3, v2\n"
"v_add_u32 v207, s58, v2\n"
"v_and_b32 v2, 1, v1\n"
"v_mul_u32_u24 v2, 1152, v2\n"
"v_lshrrev_b32 v3, 4, v1\n"
"v_lshl_add_u32 v2, v3, 3, v2\n"
"s_mul_i32 s47, s33, 288\n"
"s_add_u32 s47, s47, 3072\n"
"v_add_u32 v208, s47, v2\n"
"v_lshl_add_u32 v209, v1, 4, s58\n"
"s_mul_i32 s47, s33, 1152\n"
"s_add_u32 s47, s47, 5376\n"
"v_lshl_add_u32 v210, v1, 3, s47\n"
"v_min_u32 v2, 143, %[tid]\n"
"v_lshlrev_b32 v211, 3, v2\n"
"s_lshr_b32 s47, s39, 2\n"
"s_mul_i32 s47, s47, 4608\n"
"s_and_b32 s48, s39, 3\n"
"s_mul_i32 s48, s48, 1152\n"
"s_add_u32 s47, s47, s48\n"
"s_sub_u32 s48, s40, 24\n"
"s_mul_i32 s49, s48, 144\n"
"s_add_u32 s47, s47, s49\n"
"s_lshl_b32 s47, s47, 3\n"
"s_add_u32 s47, s47, 36241408\n"
"v_add_u32 v212, s47, v211\n"
"v_add_u32 v211, 5376, v211\n"
"s_lshl_b32 s47, s39, 3\n"
"s_add_u32 s47, s47, s48\n"
"s_lshl_b32 s47, s47, 4\n"
"s_add_u32 s47, s47, 13312\n"
"v_mov_b32 v2, s47\n"
"v_mov_b32 v253, 2147483632\n"
"v_cmp_eq_u32 vcc, 0, %[tid]\n"
"s_nop 1\n"
"v_cndmask_b32 v253, v253, v2, vcc\n"
"s_mov_b32 s60, 0\n"
"FCKC_%=:\n"
"s_waitcnt vmcnt(22)\n"
"v_xor_b32 v2, v161, v13\n"
"v_xor_b32 v3, v163, v13\n"
"s_waitcnt vmcnt(21)\n"
"v_bitop3_b32 v2, v2, v165, v13 bitop3:0xf6\n"
"v_bitop3_b32 v3, v3, v167, v13 bitop3:0xf6\n"
"s_waitcnt vmcnt(20)\n"
"v_bitop3_b32 v2, v2, v169, v13 bitop3:0xf6\n"
"v_bitop3_b32 v3, v3, v171, v13 bitop3:0xf6\n"
"s_waitcnt vmcnt(19)\n"
"v_bitop3_b32 v2, v2, v173, v13 bitop3:0xf6\n"
"v_bitop3_b32 v3, v3, v175, v13 bitop3:0xf6\n"
"s_waitcnt vmcnt(18)\n"
"v_bitop3_b32 v2, v2, v177, v13 bitop3:0xf6\n"
"v_bitop3_b32 v3, v3, v179, v13 bitop3:0xf6\n"
"s_waitcnt vmcnt(17)\n"
"v_bitop3_b32 v2, v2, v181, v13 bitop3:0xf6\n"
"v_bitop3_b32 v3, v3, v183, v13 bitop3:0xf6\n"
"s_waitcnt vmcnt(16)\n"
"v_bitop3_b32 v2, v2, v185, v13 bitop3:0xf6\n"
"v_bitop3_b32 v3, v3, v187, v13 bitop3:0xf6\n"
"s_waitcnt vmcnt(15)\n"
"v_bitop3_b32 v2, v2, v189, v13 bitop3:0xf6\n"
"v_bitop3_b32 v3, v3, v191, v13 bitop3:0xf6\n"
"s_waitcnt vmcnt(14)\n"
"v_bitop3_b32 v2, v2, v193, v13 bitop3:0xf6\n"
"v_bitop3_b32 v3, v3, v195, v13 bitop3:0xf6\n"
"v_or_b32 v2, v2, v3\n"
"v_cmp_ne_u32 vcc, 0, v2\n"
"s_nop 1\n"
"s_cbranch_vccnz FSLC_%=\n"
"FOKC_%=:\n"
"v_add_f32 v160, v160, v164\n"
"v_add_f32 v172, v172, v176\n"
"v_add_f32 v184, v184, v188\n"
"v_add_f32 v162, v162, v166\n"
"v_add_f32 v174, v174, v178\n"
"v_add_f32 v186, v186, v190\n"
"v_add_f32 v160, v160, v168\n"
"v_add_f32 v172, v172, v180\n"
"v_add_f32 v184, v184, v192\n"
"v_add_f32 v162, v162, v170\n"
"v_add_f32 v174, v174, v182\n"
"v_add_f32 v186, v186, v194\n"
"v_cvt_pk_bf16_f32 v160, v160, v162\n"
"v_cvt_pk_bf16_f32 v172, v172, v174\n"
"v_cvt_pk_bf16_f32 v184, v184, v186\n"
"v_and_b32 v161, 0xffff0000, v160\n"
"v_and_b32 v173, 0xffff0000, v172\n"
"v_and_b32 v185, 0xffff0000, v184\n"
"v_lshlrev_b32 v160, 16, v160\n"
"v_lshlrev_b32 v172, 16, v172\n"
"v_lshlrev_b32 v184, 16, v184\n"
"v_add_f32 v161, v204, v161\n"
"v_add_f32 v173, v205, v173\n"
"v_add_f32 v185, v206, v185\n"
"v_add_f32 v160, v204, v160\n"
"v_add_f32 v172, v205, v172\n"
"v_add_f32 v184, v206, v184\n"
"v_cvt_pk_bf16_f32 v160, v160, v161\n"
"v_cvt_pk_bf16_f32 v172, v172, v173\n"
"v_cvt_pk_bf16_f32 v184, v184, v185\n"
"ds_write_b32 v200, v160\n"
"ds_write_b32 v201, v172\n"
"ds_write_b32 v202, v184\n"
"s_waitcnt lgkmcnt(0)\n"
"ds_read_b64 v[214:215], v208\n"
"ds_read_b64 v[216:217], v208 offset:32\n"
"ds_read_b64 v[218:219], v208 offset:64\n"
"ds_read_b64 v[220:221], v208 offset:96\n"
"ds_read_b64 v[222:223], v208 offset:128\n"
"ds_read_b64 v[224:225], v208 offset:160\n"
"ds_read_b64 v[226:227], v208 offset:192\n"
"ds_read_b64 v[228:229], v208 offset:224\n"
"ds_read_b64 v[230:231], v208 offset:256\n"
"ds_read_b64_tr_b16 a[0:1], v207\n"
"ds_read_b64_tr_b16 a[2:3], v207 offset:32\n"
"ds_read_b64_tr_b16 a[4:5], v207 offset:64\n"
"ds_read_b64_tr_b16 a[6:7], v207 offset:96\n"
"ds_read_b64_tr_b16 a[8:9], v207 offset:128\n"
"ds_read_b64_tr_b16 a[10:11], v207 offset:160\n"
"ds_read_b64_tr_b16 a[12:13], v207 offset:192\n"
"ds_read_b64_tr_b16 a[14:15], v207 offset:224\n"
"ds_read_b64_tr_b16 a[16:17], v207 offset:256\n"
"s_waitcnt lgkmcnt(8)\n"
"v_mfma_f32_16x16x16_bf16 v[28:31], v[214:215], a[0:1], 0\n"
"ds_read_b64_tr_b16 a[18:19], v207 offset:4608\n"
"s_waitcnt lgkmcnt(8)\n"
"v_mfma_f32_16x16x16_bf16 v[32:35], v[214:215], a[2:3], 0\n"
"ds_read_b64_tr_b16 a[20:21], v207 offset:4640\n"
"s_waitcnt lgkmcnt(8)\n"
"v_mfma_f32_16x16x16_bf16 v[36:39], v[214:215], a[4:5], 0\n"
"ds_read_b64_tr_b16 a[22:23], v207 offset:4672\n"
"s_waitcnt lgkmcnt(8)\n"
"v_mfma_f32_16x16x16_bf16 v[40:43], v[214:215], a[6:7], 0\n"
"ds_read_b64_tr_b16 a[24:25], v207 offset:4704\n"
"s_waitcnt lgkmcnt(8)\n"
"v_mfma_f32_16x16x16_bf16 v[44:47], v[214:215], a[8:9], 0\n"
"ds_read_b64_tr_b16 a[26:27], v207 offset:4736\n"
"s_waitcnt lgkmcnt(8)\n"
"v_mfma_f32_16x16x16_bf16 v[48:51], v[214:215], a[10:11], 0\n"
"ds_read_b64_tr_b16 a[28:29], v207 offset:4768\n"
"s_waitcnt lgkmcnt(8)\n"
"v_mfma_f32_16x16x16_bf16 v[52:55], v[214:215], a[12:13], 0\n"
"ds_read_b64_tr_b16 a[30:31], v207 offset:4800\n"
"s_waitcnt lgkmcnt(8)\n"
"v_mfma_f32_16x16x16_bf16 v[56:59], v[214:215], a[14:15], 0\n"
"ds_read_b64_tr_b16 a[32:33], v207 offset:4832\n"
"s_waitcnt lgkmcnt(8)\n"
"v_mfma_f32_16x16x16_bf16 v[60:63], v[214:215], a[16:17], 0\n"
"ds_read_b64_tr_b16 a[34:35], v207 offset:4864\n"
"s_waitcnt lgkmcnt(8)\n"
"v_mfma_f32_16x16x16_bf16 v[28:31], v[216:217], a[18:19], v[28:31]\n"
"ds_read_b64_tr_b16 a[0:1], v207 offset:9216\n"
"s_waitcnt lgkmcnt(8)\n"
"v_mfma_f32_16x16x16_bf16 v[32:35], v[216:217], a[20:21], v[32:35]\n"
"ds_read_b64_tr_b16 a[2:3], v207 offset:9248\n"
"s_waitcnt lgkmcnt(8)\n"
"v_mfma_f32_16x16x16_bf16 v[36:39], v[216:217], a[22:23], v[36:39]\n"
"ds_read_b64_tr_b16 a[4:5], v207 offset:9280\n"
"s_waitcnt lgkmcnt(8)\n"
"v_mfma_f32_16x16x16_bf16 v[40:43], v[216:217], a[24:25], v[40:43]\n"
"ds_read_b64_tr_b16 a[6:7], v207 offset:9312\n"
"s_waitcnt lgkmcnt(8)\n"
"v_mfma_f32_16x16x16_bf16 v[44:47], v[216:217], a[26:27], v[44:47]\n"
"ds_read_b64_tr_b16 a[8:9], v207 offset:9344\n"
"s_waitcnt lgkmcnt(8)\n"
"v_mfma_f32_16x16x16_bf16 v[48:51], v[216:217], a[28:29], v[48:51]\n"
"ds_read_b64_tr_b16 a[10:11], v207 offset:9376\n"
"s_waitcnt lgkmcnt(8)\n"
"v_mfma_f32_16x16x16_bf16 v[52:55], v[216:217], a[30:31], v[52:55]\n"
"ds_read_b64_tr_b16 a[12:13], v207 offset:9408\n"
"s_waitcnt lgkmcnt(8)\n"
"v_mfma_f32_16x16x16_bf16 v[56:59], v[216:217], a[32:33], v[56:59]\n"
"ds_read_b64_tr_b16 a[14:15], v207 offset:9440\n"
"s_waitcnt lgkmcnt(8)\n"
"v_mfma_f32_16x16x16_bf16 v[60:63], v[216:217], a[34:35], v[60:63]\n"
"ds_read_b64_tr_b16 a[16:17], v207 offset:9472\n"
"s_waitcnt lgkmcnt(8)\n"
"v_mfma_f32_16x16x16_bf16 v[28:31], v[218:219], a[0:1], v[28:31]\n"
"ds_read_b64_tr_b16 a[18:19], v207 offset:13824\n"
"s_waitcnt lgkmcnt(8)\n"
"v_mfma_f32_16x16x16_bf16 v[32:35], v[218:219], a[2:3], v[32:35]\n"
"ds_read_b64_tr_b16 a[20:21], v207 offset:13856\n"
"s_waitcnt lgkmcnt(8)\n"
"v_mfma_f32_16x16x16_bf16 v[36:39], v[218:219], a[4:5], v[36:39]\n"
"ds_read_b64_tr_b16 a[22:23], v207 offset:13888\n"
"s_waitcnt lgkmcnt(8)\n"
"v_mfma_f32_16x16x16_bf16 v[40:43], v[218:219], a[6:7], v[40:43]\n"
"ds_read_b64_tr_b16 a[24:25], v207 offset:13920\n"
"s_waitcnt lgkmcnt(8)\n"
"v_mfma_f32_16x16x16_bf16 v[44:47], v[218:219], a[8:9], v[44:47]\n"
"ds_read_b64_tr_b16 a[26:27], v207 offset:13952\n"
"s_waitcnt lgkmcnt(8)\n"
"v_mfma_f32_16x16x16_bf16 v[48:51], v[218:219], a[10:11], v[48:51]\n"
"ds_read_b64_tr_b16 a[28:29], v207 offset:13984\n"
"s_waitcnt lgkmcnt(8)\n"
"v_mfma_f32_16x16x16_bf16 v[52:55], v[218:219], a[12:13], v[52:55]\n"
"ds_read_b64_tr_b16 a[30:31], v207 offset:14016\n"
"s_waitcnt lgkmcnt(8)\n"
"v_mfma_f32_16x16x16_bf16 v[56:59], v[218:219], a[14:15], v[56:59]\n"
"ds_read_b64_tr_b16 a[32:33], v207 offset:14048\n"
"s_waitcnt lgkmcnt(8)\n"
"v_mfma_f32_16x16x16_bf16 v[60:63], v[218:219], a[16:17], v[60:63]\n"
"ds_read_b64_tr_b16 a[34:35], v207 offset:14080\n"
"s_waitcnt lgkmcnt(8)\n"
"v_mfma_f32_16x16x16_bf16 v[28:31], v[220:221], a[18:19], v[28:31]\n"
"ds_read_b64_tr_b16 a[0:1], v207 offset:18432\n"
"s_waitcnt lgkmcnt(8)\n"
"v_mfma_f32_16x16x16_bf16 v[32:35], v[220:221], a[20:21], v[32:35]\n"
"ds_read_b64_tr_b16 a[2:3], v207 offset:18464\n"
"s_waitcnt lgkmcnt(8)\n"
"v_mfma_f32_16x16x16_bf16 v[36:39], v[220:221], a[22:23], v[36:39]\n"
"ds_read_b64_tr_b16 a[4:5], v207 offset:18496\n"
"s_waitcnt lgkmcnt(8)\n"
"v_mfma_f32_16x16x16_bf16 v[40:43], v[220:221], a[24:25], v[40:43]\n"
"ds_read_b64_tr_b16 a[6:7], v207 offset:18528\n"
"s_waitcnt lgkmcnt(8)\n"
"v_mfma_f32_16x16x16_bf16 v[44:47], v[220:221], a[26:27], v[44:47]\n"
"ds_read_b64_tr_b16 a[8:9], v207 offset:18560\n"
"s_waitcnt lgkmcnt(8)\n"
"v_mfma_f32_16x16x16_bf16 v[48:51], v[220:221], a[28:29], v[48:51]\n"
"ds_read_b64_tr_b16 a[10:11], v207 offset:18592\n"
"s_waitcnt lgkmcnt(8)\n"
"v_mfma_f32_16x16x16_bf16 v[52:55], v[220:221], a[30:31], v[52:55]\n"
"ds_read_b64_tr_b16 a[12:13], v207 offset:18624\n"
"s_waitcnt lgkmcnt(8)\n"
"v_mfma_f32_16x16x16_bf16 v[56:59], v[220:221], a[32:33], v[56:59]\n"
"ds_read_b64_tr_b16 a[14:15], v207 offset:18656\n"
"s_waitcnt lgkmcnt(8)\n"
"v_mfma_f32_16x16x16_bf16 v[60:63], v[220:221], a[34:35], v[60:63]\n"
"ds_read_b64_tr_b16 a[16:17], v207 offset:18688\n"
"s_waitcnt lgkmcnt(8)\n"
"v_mfma_f32_16x16x16_bf16 v[28:31], v[222:223], a[0:1], v[28:31]\n"
"ds_read_b64_tr_b16 a[18:19], v207 offset:23040\n"
"s_waitcnt lgkmcnt(8)\n"
"v_mfma_f32_16x16x16_bf16 v[32:35], v[222:223], a[2:3], v[32:35]\n"
"ds_read_b64_tr_b16 a[20:21], v207 offset:23072\n"
"s_waitcnt lgkmcnt(8)\n"
"v_mfma_f32_16x16x16_bf16 v[36:39], v[222:223], a[4:5], v[36:39]\n"
"ds_read_b64_tr_b16 a[22:23], v207 offset:23104\n"
"s_waitcnt lgkmcnt(8)\n"
"v_mfma_f32_16x16x16_bf16 v[40:43], v[222:223], a[6:7], v[40:43]\n"
"ds_read_b64_tr_b16 a[24:25], v207 offset:23136\n"
"s_waitcnt lgkmcnt(8)\n"
"v_mfma_f32_16x16x16_bf16 v[44:47], v[222:223], a[8:9], v[44:47]\n"
"ds_read_b64_tr_b16 a[26:27], v207 offset:23168\n"
"s_waitcnt lgkmcnt(8)\n"
"v_mfma_f32_16x16x16_bf16 v[48:51], v[222:223], a[10:11], v[48:51]\n"
"ds_read_b64_tr_b16 a[28:29], v207 offset:23200\n"
"s_waitcnt lgkmcnt(8)\n"
"v_mfma_f32_16x16x16_bf16 v[52:55], v[222:223], a[12:13], v[52:55]\n"
"ds_read_b64_tr_b16 a[30:31], v207 offset:23232\n"
"s_waitcnt lgkmcnt(8)\n"
"v_mfma_f32_16x16x16_bf16 v[56:59], v[222:223], a[14:15], v[56:59]\n"
"ds_read_b64_tr_b16 a[32:33], v207 offset:23264\n"
"s_waitcnt lgkmcnt(8)\n"
"v_mfma_f32_16x16x16_bf16 v[60:63], v[222:223], a[16:17], v[60:63]\n"
"ds_read_b64_tr_b16 a[34:35], v207 offset:23296\n"
"s_waitcnt lgkmcnt(8)\n"
"v_mfma_f32_16x16x16_bf16 v[28:31], v[224:225], a[18:19], v[28:31]\n"
"s_waitcnt vmcnt(9)\n"
"ds_read_b64_tr_b16 a[0:1], v207 offset:27648\n"
"s_waitcnt lgkmcnt(8)\n"
"v_mfma_f32_16x16x16_bf16 v[32:35], v[224:225], a[20:21], v[32:35]\n"
"ds_read_b64_tr_b16 a[2:3], v207 offset:27680\n"
"s_waitcnt lgkmcnt(8)\n"
"v_mfma_f32_16x16x16_bf16 v[36:39], v[224:225], a[22:23], v[36:39]\n"
"ds_read_b64_tr_b16 a[4:5], v207 offset:27712\n"
"s_waitcnt lgkmcnt(8)\n"
"v_mfma_f32_16x16x16_bf16 v[40:43], v[224:225], a[24:25], v[40:43]\n"
"ds_read_b64_tr_b16 a[6:7], v207 offset:27744\n"
"s_waitcnt lgkmcnt(8)\n"
"v_mfma_f32_16x16x16_bf16 v[44:47], v[224:225], a[26:27], v[44:47]\n"
"ds_read_b64_tr_b16 a[8:9], v207 offset:27776\n"
"s_waitcnt lgkmcnt(8)\n"
"v_mfma_f32_16x16x16_bf16 v[48:51], v[224:225], a[28:29], v[48:51]\n"
"ds_read_b64_tr_b16 a[10:11], v207 offset:27808\n"
"s_waitcnt lgkmcnt(8)\n"
"v_mfma_f32_16x16x16_bf16 v[52:55], v[224:225], a[30:31], v[52:55]\n"
"ds_read_b64_tr_b16 a[12:13], v207 offset:27840\n"
"s_waitcnt lgkmcnt(8)\n"
"v_mfma_f32_16x16x16_bf16 v[56:59], v[224:225], a[32:33], v[56:59]\n"
"ds_read_b64_tr_b16 a[14:15], v207 offset:27872\n"
"s_waitcnt lgkmcnt(8)\n"
"v_mfma_f32_16x16x16_bf16 v[60:63], v[224:225], a[34:35], v[60:63]\n"
"ds_read_b64_tr_b16 a[16:17], v207 offset:27904\n"
"s_waitcnt lgkmcnt(8)\n"
"v_mfma_f32_16x16x16_bf16 v[28:31], v[226:227], a[0:1], v[28:31]\n"
"s_waitcnt vmcnt(5)\n"
"ds_read_b64_tr_b16 a[18:19], v207 offset:32256\n"
"s_waitcnt lgkmcnt(8)\n"
"v_mfma_f32_16x16x16_bf16 v[32:35], v[226:227], a[2:3], v[32:35]\n"
"ds_read_b64_tr_b16 a[20:21], v207 offset:32288\n"
"s_waitcnt lgkmcnt(8)\n"
"v_mfma_f32_16x16x16_bf16 v[36:39], v[226:227], a[4:5], v[36:39]\n"
"ds_read_b64_tr_b16 a[22:23], v207 offset:32320\n"
"s_waitcnt lgkmcnt(8)\n"
"v_mfma_f32_16x16x16_bf16 v[40:43], v[226:227], a[6:7], v[40:43]\n"
"ds_read_b64_tr_b16 a[24:25], v207 offset:32352\n"
"s_waitcnt lgkmcnt(8)\n"
"v_mfma_f32_16x16x16_bf16 v[44:47], v[226:227], a[8:9], v[44:47]\n"
"ds_read_b64_tr_b16 a[26:27], v207 offset:32384\n"
"s_waitcnt lgkmcnt(8)\n"
"v_mfma_f32_16x16x16_bf16 v[48:51], v[226:227], a[10:11], v[48:51]\n"
"ds_read_b64_tr_b16 a[28:29], v207 offset:32416\n"
"s_waitcnt lgkmcnt(8)\n"
"v_mfma_f32_16x16x16_bf16 v[52:55], v[226:227], a[12:13], v[52:55]\n"
"ds_read_b64_tr_b16 a[30:31], v207 offset:32448\n"
"s_waitcnt lgkmcnt(8)\n"
"v_mfma_f32_16x16x16_bf16 v[56:59], v[226:227], a[14:15], v[56:59]\n"
"ds_read_b64_tr_b16 a[32:33], v207 offset:32480\n"
"s_waitcnt lgkmcnt(8)\n"
"v_mfma_f32_16x16x16_bf16 v[60:63], v[226:227], a[16:17], v[60:63]\n"
"ds_read_b64_tr_b16 a[34:35], v207 offset:32512\n"
"s_waitcnt vmcnt(4)\n"
"ds_write_b128 v209, v[140:143]\n"
"s_waitcnt vmcnt(3)\n"
"ds_write_b128 v209, v[144:147] offset:1024\n"
"s_waitcnt vmcnt(2)\n"
"ds_write_b128 v209, v[148:151] offset:2048\n"
"s_waitcnt vmcnt(1)\n"
"ds_write_b128 v209, v[152:155] offset:3072\n"
"s_waitcnt vmcnt(0)\n"
"ds_write_b128 v209, v[156:159] offset:4096\n"
"s_waitcnt lgkmcnt(13)\n"
"v_mfma_f32_16x16x16_bf16 v[28:31], v[228:229], a[18:19], v[28:31]\n"
"ds_read_b64_tr_b16 a[0:1], v207\n"
"s_waitcnt lgkmcnt(13)\n"
"v_mfma_f32_16x16x16_bf16 v[32:35], v[228:229], a[20:21], v[32:35]\n"
"ds_read_b64_tr_b16 a[2:3], v207 offset:32\n"
"s_waitcnt lgkmcnt(13)\n"
"v_mfma_f32_16x16x16_bf16 v[36:39], v[228:229], a[22:23], v[36:39]\n"
"ds_read_b64_tr_b16 a[4:5], v207 offset:64\n"
"s_waitcnt lgkmcnt(13)\n"
"v_mfma_f32_16x16x16_bf16 v[40:43], v[228:229], a[24:25], v[40:43]\n"
"ds_read_b64_tr_b16 a[6:7], v207 offset:96\n"
"s_waitcnt lgkmcnt(13)\n"
"v_mfma_f32_16x16x16_bf16 v[44:47], v[228:229], a[26:27], v[44:47]\n"
"ds_read_b64_tr_b16 a[8:9], v207 offset:128\n"
"s_waitcnt lgkmcnt(13)\n"
"v_mfma_f32_16x16x16_bf16 v[48:51], v[228:229], a[28:29], v[48:51]\n"
"ds_read_b64_tr_b16 a[10:11], v207 offset:160\n"
"s_waitcnt lgkmcnt(13)\n"
"v_mfma_f32_16x16x16_bf16 v[52:55], v[228:229], a[30:31], v[52:55]\n"
"ds_read_b64_tr_b16 a[12:13], v207 offset:192\n"
"s_waitcnt lgkmcnt(13)\n"
"v_mfma_f32_16x16x16_bf16 v[56:59], v[228:229], a[32:33], v[56:59]\n"
"ds_read_b64_tr_b16 a[14:15], v207 offset:224\n"
"s_waitcnt lgkmcnt(13)\n"
"v_mfma_f32_16x16x16_bf16 v[60:63], v[228:229], a[34:35], v[60:63]\n"
"ds_read_b64_tr_b16 a[16:17], v207 offset:256\n"
"s_waitcnt lgkmcnt(8)\n"
"v_mfma_f32_16x16x16_bf16 v[28:31], v[230:231], a[0:1], v[28:31]\n"
"s_waitcnt lgkmcnt(7)\n"
"v_mfma_f32_16x16x16_bf16 v[32:35], v[230:231], a[2:3], v[32:35]\n"
"s_waitcnt lgkmcnt(6)\n"
"v_mfma_f32_16x16x16_bf16 v[36:39], v[230:231], a[4:5], v[36:39]\n"
"s_waitcnt lgkmcnt(5)\n"
"v_mfma_f32_16x16x16_bf16 v[40:43], v[230:231], a[6:7], v[40:43]\n"
"s_waitcnt lgkmcnt(4)\n"
"v_mfma_f32_16x16x16_bf16 v[44:47], v[230:231], a[8:9], v[44:47]\n"
"s_waitcnt lgkmcnt(3)\n"
"v_mfma_f32_16x16x16_bf16 v[48:51], v[230:231], a[10:11], v[48:51]\n"
"s_waitcnt lgkmcnt(2)\n"
"v_mfma_f32_16x16x16_bf16 v[52:55], v[230:231], a[12:13], v[52:55]\n"
"s_waitcnt lgkmcnt(1)\n"
"v_mfma_f32_16x16x16_bf16 v[56:59], v[230:231], a[14:15], v[56:59]\n"
"s_waitcnt lgkmcnt(0)\n"
"v_mfma_f32_16x16x16_bf16 v[60:63], v[230:231], a[16:17], v[60:63]\n"
"s_mov_b64 exec, 0xffff\n"
"ds_write_b64 v210, v[28:29]\n"
"ds_write_b64 v210, v[32:33] offset:128\n"
"ds_write_b64 v210, v[36:37] offset:256\n"
"ds_write_b64 v210, v[40:41] offset:384\n"
"ds_write_b64 v210, v[44:45] offset:512\n"
"ds_write_b64 v210, v[48:49] offset:640\n"
"ds_write_b64 v210, v[52:53] offset:768\n"
"ds_write_b64 v210, v[56:57] offset:896\n"
"ds_write_b64 v210, v[60:61] offset:1024\n"
"s_mov_b64 exec, -1\n"
"s_waitcnt lgkmcnt(0)\n"
"s_barrier\n"
"ds_read_b64 v[64:65], v211\n"
"ds_read_b64 v[66:67], v211 offset:1152\n"
"ds_read_b64 v[68:69], v211 offset:2304\n"
"ds_read_b64 v[70:71], v211 offset:3456\n"
"s_waitcnt lgkmcnt(2)\n"
"v_add_f32 v64, v64, v66\n"
"v_add_f32 v65, v65, v67\n"
"s_waitcnt lgkmcnt(0)\n"
"v_add_f32 v68, v68, v70\n"
"v_add_f32 v69, v69, v71\n"
"v_add_f32 v64, v64, v68\n"
"v_add_f32 v65, v65, v69\n"
"v_cvt_pk_bf16_f32 v66, v64, v65\n"
"v_lshlrev_b32 v67, 16, v66\n"
"v_and_b32 v68, 0xffff0000, v66\n"
"v_sub_f32 v64, v64, v67\n"
"v_sub_f32 v65, v65, v68\n"
"v_cvt_pk_bf16_f32 v70, v67, v64\n"
"v_cvt_pk_bf16_f32 v71, v68, v65\n"
"s_cmp_eq_u32 s33, 3\n"
"s_cbranch_scc1 FFIN_%=\n"
"global_store_dwordx2 v212, v[70:71], s[96:97] sc1\n"
"FFIN_%=:\n"
"v_readfirstlane_b32 s86, v13\n"
"s_nop 7\n"
"v_and_b32 v65, 63, %[tid]\n"
"s_and_b32 s50, %[blk], 3\n"
"s_bfe_u32 s51, %[blk], 0x20003\n"
"s_lshl_b32 s84, s50, 2\n"
"s_add_u32 s84, s84, s51\n"
"s_bfe_u32 s50, %[blk], 0x10002\n"
"s_lshr_b32 s51, %[blk], 5\n"
"s_lshl_b32 s85, s50, 3\n"
"s_add_u32 s85, s85, s51\n"
"v_lshrrev_b32 v66, 3, v65\n"
"v_and_b32 v67, 6, v66\n"
"v_xor_b32 v67, v67, v65\n"
"v_and_b32 v67, 7, v67\n"
"v_lshlrev_b32 v67, 4, v67\n"
"s_and_b32 s50, s33, 1\n"
"s_lshr_b32 s51, s33, 1\n"
"s_lshl_b32 s34, s33, 12\n"
"s_lshl_b32 s52, s50, 5\n"
"v_add_u32 v64, s52, v66\n"
"s_cmp_eq_u32 s51, 0\n"
"s_cselect_b32 s20, s90, s92\n"
"s_cselect_b32 s21, s91, s93\n"
"s_and_b32 s21, s21, 0xffff\n"
"s_mov_b32 s22, 5308416\n"
"s_mov_b32 s23, 0x20000\n"
"v_mul_u32_u24 v20, 9216, v64\n"
"v_add_u32 v20, v20, v67\n"
"v_add_u32 v21, 72704, v20\n"
"v_add_u32 v22, 145408, v20\n"
"v_add_u32 v23, 218112, v20\n"
"s_mul_i32 s53, s84, 576\n"
"s_and_b32 s54, s84, 1\n"
"s_lshl_b32 s55, s54, 6\n"
"s_add_u32 s40, s53, s55\n"
"s_mov_b32 s36, s96\n"
"s_and_b32 s37, s97, 0xffff\n"
"s_mov_b32 s38, 36701440\n"
"s_mov_b32 s39, 0x20000\n"
"v_mul_u32_u24 v70, 128, v65\n"
"v_add_u32 v70, 36586752, v70\n"
"s_mov_b32 s76, 8192\n"
"s_mov_b32 s16, 16384\n"
"v_mov_b32 v19, s86\n"
"s_add_u32 s24, s96, 2424832\n"
"s_addc_u32 s25, s97, 0\n"
"s_and_b32 s25, s25, 0xffff\n"
"s_mov_b32 s26, 2359296\n"
"s_mov_b32 s27, 0x20000\n"
"v_lshl_add_u32 v24, v64, 8, v67\n"
"v_add_u32 v25, 1024, v24\n"
"v_add_u32 v26, 2048, v24\n"
"v_add_u32 v27, 3072, v24\n"
"s_lshl_b32 s52, s51, 7\n"
"s_mul_i32 s42, s85, 147456\n"
"s_add_u32 s42, s42, s52\n"
"v_lshlrev_b32 v8, 4, v65\n"
"s_lshl_b32 s78, s33, 10\n"
"s_lshl_b32 s77, s33, 11\n"
"s_lshr_b32 s61, s84, 2\n"
"s_lshl_b32 s55, s85, 2\n"
"s_add_u32 s61, s61, s55\n"
"s_mul_i32 s81, s61, 36864\n"
"s_add_u32 s81, s81, 65536\n"
"s_and_b32 s52, s84, 3\n"
"s_mul_i32 s55, s52, 9216\n"
"s_add_u32 s79, s81, s55\n"
"s_lshl_b32 s55, s52, 10\n"
"s_add_u32 s80, s81, s55\n"
"s_add_u32 s80, s80, 32768\n"
"s_lshl_b32 s55, s85, 2\n"
"s_add_u32 s55, s55, s33\n"
"s_mul_i32 s81, s55, 36864\n"
"s_lshl_b32 s55, s52, 13\n"
"s_add_u32 s81, s81, s55\n"
"s_lshr_b32 s55, s84, 2\n"
"s_lshl_b32 s55, s55, 11\n"
"s_add_u32 s81, s81, s55\n"
"s_add_u32 s81, s81, 65536\n"
"v_min_u32 v72, 191, %[tid]\n"
"s_lshl_b32 s55, s52, 7\n"
"v_add_u32 v73, s55, v72\n"
"v_add_u32 v74, 384, v72\n"
"v_cmp_gt_u32 vcc, 128, v72\n"
"s_nop 1\n"
"v_cndmask_b32 v73, v74, v73, vcc\n"
"v_lshlrev_b32 v73, 3, v73\n"
"v_add_u32 v73, 36395520, v73\n"
"v_lshlrev_b32 v72, 2, v72\n"
"v_add_u32 v72, 161792, v72\n"
"s_mov_b32 s62, 27648\n"
"s_mov_b32 s63, 55296\n"
"v_and_b32 v68, 15, v65\n"
"v_lshrrev_b32 v69, 4, v65\n"
"s_lshl_b32 s55, s61, 2\n"
"s_add_u32 s55, s55, s52\n"
"s_lshl_b32 s55, s55, 9\n"
"s_lshl_b32 s58, s50, 8\n"
"s_add_u32 s55, s55, s58\n"
"s_add_u32 s55, s55, 36455680\n"
"s_cmp_eq_u32 s51, 0\n"
"s_cselect_b32 s55, s55, 2147479536\n"
"v_lshl_add_u32 v180, v68, 4, s55\n"
"v_lshrrev_b32 v66, 2, v68\n"
"v_cmp_eq_u32 vcc, v66, v69\n"
"v_mov_b32 v66, 2147483632\n"
"s_nop 0\n"
"v_cndmask_b32 v180, v66, v180, vcc\n"
"s_lshl_b32 s55, s85, 1\n"
"s_add_u32 s55, s55, s51\n"
"s_lshl_b32 s55, s55, 12\n"
"s_add_u32 s55, s55, 36455680\n"
"v_lshlrev_b32 v66, 9, v69\n"
"v_lshl_add_u32 v66, v68, 4, v66\n"
"v_add_u32 v181, s55, v66\n"
"s_lshl_b32 s55, s50, 5\n"
"s_add_u32 s55, s55, 152576\n"
"v_lshrrev_b32 v66, 2, v65\n"
"v_lshlrev_b32 v66, 6, v66\n"
"v_and_b32 v182, 3, v65\n"
"v_lshl_add_u32 v182, v182, 3, v66\n"
"v_add_u32 v182, s55, v182\n"
"s_lshl_b32 s55, s51, 8\n"
"s_add_u32 s55, s55, 162560\n"
"v_lshl_add_u32 v183, v65, 2, s55\n"
"s_lshl_b32 s55, s50, 8\n"
"s_add_u32 s55, s55, 147456\n"
"v_lshl_add_u32 v47, v68, 3, s55\n"
"s_lshr_b32 s55, s84, 2\n"
"s_lshl_b32 s55, s55, 3\n"
"s_lshl_b32 s58, s52, 1\n"
"s_add_u32 s55, s55, s58\n"
"s_lshl_b32 s55, s55, 4\n"
"s_add_u32 s55, s55, 13312\n"
"v_and_b32 v66, 1, v65\n"
"v_bfe_u32 v184, v65, 1, 1\n"
"v_lshlrev_b32 v184, 9, v184\n"
"v_lshl_add_u32 v184, v66, 4, v184\n"
"v_add_u32 v184, s55, v184\n"
"s_mul_i32 s55, s84, 2304\n"
"s_add_u32 s55, s55, 36241408\n"
"s_lshl_b32 s58, s54, 8\n"
"s_add_u32 s58, s58, s55\n"
"s_mul_i32 s59, s51, 36864\n"
"s_add_u32 s58, s58, s59\n"
"v_and_b32 v66, 127, %[tid]\n"
"v_lshl_add_u32 v178, v66, 4, s58\n"
"s_xor_b32 s58, s54, 1\n"
"s_lshl_b32 s58, s58, 11\n"
"s_add_u32 s55, s55, s58\n"
"v_bfe_u32 v66, v65, 3, 1\n"
"v_mul_u32_u24 v179, 36864, v66\n"
"v_bfe_u32 v66, v65, 2, 1\n"
"v_lshl_add_u32 v179, v66, 7, v179\n"
"v_bfe_u32 v66, v65, 4, 1\n"
"v_lshl_add_u32 v179, v66, 6, v179\n"
"v_and_b32 v66, 3, v65\n"
"v_lshl_add_u32 v179, v66, 4, v179\n"
"v_add_u32 v179, s55, v179\n"
"v_mov_b32 v148, 0x3f803f80\n"
"v_mov_b32 v149, 0x3f803f80\n"
"v_mov_b32 v150, 0x3f803f80\n"
"v_mov_b32 v151, 0x3f803f80\n"
"s_lshr_b32 s55, %[blk], 3\n"
"s_lshl_b32 s55, s55, 2\n"
"s_add_u32 s55, s55, s33\n"
"s_lshl_b32 s55, s55, 1\n"
"s_bfe_u32 s58, %[blk], 0x10002\n"
"s_add_u32 s55, s55, s58\n"
"s_mul_i32 s55, s55, 6912\n"
"s_cmp_ge_u32 %[blk], 192\n"
"s_cselect_b32 s55, 0x70000000, s55\n"
"s_lshr_b32 s58, s84, 2\n"
"s_mul_i32 s58, s58, 1327104\n"
"s_add_u32 s55, s55, s58\n"
"v_min_u32 v66, 47, v65\n"
"v_lshrrev_b32 v59, 3, v66\n"
"v_add_u32 v66, v66, v59\n"
"v_lshl_add_u32 v59, v66, 7, s55\n"
"s_xor_b32 s55, s54, 1\n"
"s_lshl_b32 s55, s55, 9\n"
"s_add_u32 s41, s53, s55\n"
"s_lshl_b32 s55, s84, 1\n"
"s_add_u32 s55, s55, s51\n"
"s_mul_i32 s55, s55, 36864\n"
"s_add_u32 s41, s55, 34144256\n"
"s_mul_i32 s55, s50, 2048\n"
"s_sub_u32 s48, 32768, s55\n"
"s_add_u32 s48, s48, s41\n"
"s_lshl_b32 s47, s51, 13\n"
"s_lshl_b32 s55, s50, 11\n"
"s_add_u32 s47, s47, s55\n"
"s_mov_b32 s28, s94\n"
"s_and_b32 s29, s95, 0xffff\n"
"s_mov_b32 s30, 5308416\n"
"s_mov_b32 s31, 0x20000\n"
"s_and_b32 s52, s84, 12\n"
"s_and_b32 s53, s84, 3\n"
"s_add_u32 s54, s53, s33\n"
"s_and_b32 s54, s54, 3\n"
"s_add_u32 s55, s52, s54\n"
"s_mul_i32 s61, s55, 9216\n"
"s_mul_i32 s58, s53, 147456\n"
"s_add_u32 s61, s61, s58\n"
"s_add_u32 s61, s61, 33095680\n"
"s_and_b32 s58, s54, 1\n"
"s_xor_b32 s17, s58, 1\n"
"s_lshl_b32 s17, s17, 13\n"
"s_add_u32 s17, s17, s61\n"
"s_lshl_b32 s58, s58, 10\n"
"s_add_u32 s61, s61, s58\n"
"s_mul_i32 s55, s55, 331776\n"
"s_lshl_b32 s58, s53, 8\n"
"s_add_u32 s55, s55, s58\n"
"s_and_b32 s54, s54, 1\n"
"s_mul_i32 s58, s54, 36864\n"
"s_add_u32 s43, s55, s58\n"
"s_xor_b32 s54, s54, 1\n"
"s_mul_i32 s54, s54, 294912\n"
"s_add_u32 s44, s55, s54\n"
"s_mul_i32 s46, s33, 8448\n"
"s_mul_i32 s18, s33, 1056\n"
"v_lshrrev_b32 v64, 4, v65\n"
"v_mul_u32_u24 v5, 9216, v64\n"
"v_and_b32 v66, 15, v65\n"
"s_xor_b32 s55, s52, 12\n"
"v_xor_b32 v66, s55, v66\n"
"v_lshl_add_u32 v1, v66, 4, v5\n"
"v_add_u32 v2, 96, v1\n"
"v_add_u32 v3, 192, v1\n"
"v_add_u32 v4, 288, v1\n"
"v_lshrrev_b32 v64, 2, v65\n"
"s_lshl_b32 s55, s50, 6\n"
"v_add_u32 v64, s55, v64\n"
"v_mul_u32_u24 v28, 64, v64\n"
"v_bfe_u32 v64, v65, 4, 1\n"
"v_xor_b32 v64, v64, v65\n"
"v_and_b32 v64, 3, v64\n"
"v_lshl_add_u32 v28, v64, 4, v28\n"
"v_lshrrev_b32 v64, 2, v65\n"
"v_and_b32 v66, 3, v65\n"
"v_lshlrev_b32 v66, 3, v66\n"
"v_bfe_u32 v67, v65, 3, 2\n"
"v_lshl_add_u32 v46, v64, 7, v66\n"
"v_and_b32 v68, 7, v64\n"
"v_mul_u32_u24 v68, 1056, v68\n"
"s_waitcnt vmcnt(0)\n"
"s_barrier\n"
"buffer_store_dword v19, v253, s[36:39], 0 offen sc1\n"
"s_cmp_eq_u32 s33, 3\n"
"s_cbranch_scc1 PA_%=\n"
"s_sleep 4\n"
"buffer_load_dwordx2 v[76:77], v73, s[36:39], 0 offen sc1\n"
"buffer_load_dwordx2 v[78:79], v73, s[36:39], s62 offen sc1\n"
"buffer_load_dwordx2 v[80:81], v73, s[36:39], s63 offen sc1\n"
"s_add_u32 m0, s34, 81920\n"
"s_add_u32 s75, s40, 0\n"
"buffer_load_dwordx4 v20, s[20:23], s75 offen lds\n"
"buffer_load_dwordx4 v21, s[20:23], s75 offen offset:1024 lds\n"
"buffer_load_dwordx4 v22, s[20:23], s75 offen offset:2048 lds\n"
"buffer_load_dwordx4 v23, s[20:23], s75 offen offset:3072 lds\n"
"s_add_u32 m0, s34, 114688\n"
"s_add_u32 s75, s40, 589824\n"
"buffer_load_dwordx4 v20, s[20:23], s75 offen lds\n"
"buffer_load_dwordx4 v21, s[20:23], s75 offen offset:1024 lds\n"
"buffer_load_dwordx4 v22, s[20:23], s75 offen offset:2048 lds\n"
"buffer_load_dwordx4 v23, s[20:23], s75 offen offset:3072 lds\n"
"s_add_u32 m0, s34, 0\n"
"s_add_u32 s75, s40, 1179648\n"
"buffer_load_dwordx4 v20, s[20:23], s75 offen lds\n"
"buffer_load_dwordx4 v21, s[20:23], s75 offen offset:1024 lds\n"
"buffer_load_dwordx4 v22, s[20:23], s75 offen offset:2048 lds\n"
"buffer_load_dwordx4 v23, s[20:23], s75 offen offset:3072 lds\n"
"s_add_u32 m0, s34, 32768\n"
"s_add_u32 s75, s40, 1769472\n"
"buffer_load_dwordx4 v20, s[20:23], s75 offen lds\n"
"buffer_load_dwordx4 v21, s[20:23], s75 offen offset:1024 lds\n"
"buffer_load_dwordx4 v22, s[20:23], s75 offen offset:2048 lds\n"
"buffer_load_dwordx4 v23, s[20:23], s75 offen offset:3072 lds\n"
"s_cmp_eq_u32 s33, 2\n"
"s_cbranch_scc0 PB_%=\n"
"s_add_u32 m0, s34, 86016\n"
"s_nop 0\n"
"s_add_u32 s75, s40, 294912\n"
"buffer_load_dwordx4 v20, s[20:23], s75 offen lds\n"
"buffer_load_dwordx4 v21, s[20:23], s75 offen offset:1024 lds\n"
"buffer_load_dwordx4 v22, s[20:23], s75 offen offset:2048 lds\n"
"buffer_load_dwordx4 v23, s[20:23], s75 offen offset:3072 lds\n"
"s_add_u32 m0, s34, 118784\n"
"s_nop 0\n"
"s_add_u32 s75, s40, 884736\n"
"buffer_load_dwordx4 v20, s[20:23], s75 offen lds\n"
"buffer_load_dwordx4 v21, s[20:23], s75 offen offset:1024 lds\n"
"buffer_load_dwordx4 v22, s[20:23], s75 offen offset:2048 lds\n"
"buffer_load_dwordx4 v23, s[20:23], s75 offen offset:3072 lds\n"
"s_add_u32 m0, s34, 4096\n"
"s_nop 0\n"
"s_add_u32 s75, s40, 1474560\n"
"buffer_load_dwordx4 v20, s[20:23], s75 offen lds\n"
"buffer_load_dwordx4 v21, s[20:23], s75 offen offset:1024 lds\n"
"buffer_load_dwordx4 v22, s[20:23], s75 offen offset:2048 lds\n"
"buffer_load_dwordx4 v23, s[20:23], s75 offen offset:3072 lds\n"
"s_add_u32 m0, s34, 36864\n"
"s_nop 0\n"
"s_add_u32 s75, s40, 2064384\n"
"buffer_load_dwordx4 v20, s[20:23], s75 offen lds\n"
"buffer_load_dwordx4 v21, s[20:23], s75 offen offset:1024 lds\n"
"buffer_load_dwordx4 v22, s[20:23], s75 offen offset:2048 lds\n"
"buffer_load_dwordx4 v23, s[20:23], s75 offen offset:3072 lds\n"
"s_branch PB_%=\n"
"PA_%=:\n"
"buffer_load_dword v82, v70, s[36:39], 0 offen sc1\n"
"PB_%=:\n"
"v_lshrrev_b32 v69, 3, v64\n"
"v_lshl_add_u32 v69, v69, 8, v66\n"
"v_add_u32 v10, v68, v69\n"
"v_lshlrev_b32 v68, 3, v65\n"
"v_add_u32 v6, 33792, v68\n"
"v_add_u32 v9, 109824, v68\n"
"s_lshl_b32 s55, s51, 13\n"
"v_xor_b32 v32, 0, v67\n"
"v_lshl_add_u32 v32, v32, 5, v46\n"
"v_add_u32 v32, s55, v32\n"
"v_xor_b32 v35, 1, v67\n"
"v_lshl_add_u32 v35, v35, 5, v46\n"
"v_add_u32 v35, s55, v35\n"
"v_xor_b32 v38, 2, v67\n"
"v_lshl_add_u32 v38, v38, 5, v46\n"
"v_add_u32 v38, s55, v38\n"
"v_xor_b32 v41, 3, v67\n"
"v_lshl_add_u32 v41, v41, 5, v46\n"
"v_add_u32 v41, s55, v41\n"
"s_lshl_b32 s54, s50, 1\n"
"v_xor_b32 v248, s54, v67\n"
"v_xor_b32 v251, 1, v248\n"
"v_lshl_add_u32 v248, v248, 5, v46\n"
"v_lshl_add_u32 v251, v251, 5, v46\n"
"v_and_b32 v68, 1, v65\n"
"v_lshlrev_b32 v68, 3, v68\n"
"v_lshl_add_u32 v44, v64, 6, v68\n"
"v_bfe_u32 v67, v65, 4, 1\n"
"v_xor_b32 v67, s50, v67\n"
"v_and_b32 v68, 2, v65\n"
"v_or_b32 v67, v68, v67\n"
"v_lshl_add_u32 v44, v67, 4, v44\n"
"v_add_u32 v33, 0x10000, v32\n"
"v_add_u32 v34, 0x20000, v32\n"
"v_add_u32 v36, 0x10000, v35\n"
"v_add_u32 v37, 0x20000, v35\n"
"v_add_u32 v39, 0x10000, v38\n"
"v_add_u32 v40, 0x20000, v38\n"
"v_add_u32 v42, 0x10000, v41\n"
"v_add_u32 v43, 0x20000, v41\n"
"v_add_u32 v249, 0x10000, v248\n"
"v_add_u32 v250, 0x20000, v248\n"
"v_add_u32 v252, 0x10000, v251\n"
"v_add_u32 v253, 0x20000, v251\n"
"v_add_u32 v45, 0x10000, v44\n"
"v_add_u32 v46, 0x20000, v44\n"
"v_add_u32 v11, 0x10000, v10\n"
"v_add_u32 v12, 0x20000, v10\n"
"s_add_u32 s54, s53, 0\n"
"s_and_b32 s54, s54, 3\n"
"s_mul_i32 s54, s54, 73728\n"
"v_add_u32 v13, s54, v8\n"
"s_add_u32 s54, s53, 1\n"
"s_and_b32 s54, s54, 3\n"
"s_mul_i32 s54, s54, 73728\n"
"v_add_u32 v14, s54, v8\n"
"s_add_u32 s54, s53, 2\n"
"s_and_b32 s54, s54, 3\n"
"s_mul_i32 s54, s54, 73728\n"
"v_add_u32 v15, s54, v8\n"
"s_add_u32 s54, s53, 3\n"
"s_and_b32 s54, s54, 3\n"
"s_mul_i32 s54, s54, 73728\n"
"v_add_u32 v16, s54, v8\n"
"v_lshrrev_b32 v64, 5, v65\n"
"v_lshlrev_b32 v64, 9, v64\n"
"v_add_u32 v254, v64, v8\n"
"s_lshl_b32 s54, s85, 4\n"
"s_add_u32 s55, s54, s84\n"
"s_mul_i32 s55, s55, 73728\n"
"s_add_u32 s55, s55, 4784128\n"
"s_lshl_b32 s58, s51, 12\n"
"s_add_u32 s55, s55, s58\n"
"s_lshl_b32 s58, s50, 13\n"
"s_add_u32 s49, s55, s58\n"
"s_lshl_b32 s58, s50, 9\n"
"s_add_u32 s56, s55, s58\n"
"s_add_u32 s56, s56, 65536\n"
"s_add_u32 s54, s54, s52\n"
"s_mul_i32 s55, s54, 73728\n"
"s_lshl_b32 s58, s33, 11\n"
"s_add_u32 s55, s55, s58\n"
"s_add_u32 s57, s55, 4784128\n"
"s_lshl_b32 s59, s54, 5\n"
"s_add_u32 s59, s59, 32768\n"
"s_lshl_b32 s55, s53, 2\n"
"s_add_u32 s55, s55, s59\n"
"v_mov_b32 v64, s55\n"
"v_mov_b32 v18, 2147483632\n"
"v_cmp_eq_u32 vcc, 0, %[tid]\n"
"s_nop 1\n"
"v_cndmask_b32 v18, v18, v64, vcc\n"
"s_lshl_b32 s54, s85, 2\n"
"s_add_u32 s54, s54, s53\n"
"s_lshl_b32 s54, s54, 17\n"
"s_lshl_b32 s55, s33, 13\n"
"s_add_u32 s54, s54, s55\n"
"s_add_u32 s54, s54, 23658496\n"
"s_xor_b32 s55, s52, 12\n"
"s_lshl_b32 s55, s55, 13\n"
"s_add_u32 s45, s54, s55\n"
"s_cmp_eq_u32 s33, 3\n"
"s_cbranch_scc0 PX_%=\n"
"s_waitcnt vmcnt(0)\n"
"v_cmp_ne_u32 vcc, s86, v82\n"
"s_nop 1\n"
"s_cbranch_vccnz SLOWX_%=\n"
"BACKX_%=:\n"
"buffer_load_dword v83, v70, s[36:39], s76 offen sc1\n"
"buffer_load_dword v84, v70, s[36:39], s16 offen sc1\n"
"PX_%=:\n"
"s_barrier\n"
"s_cmp_eq_u32 s33, 3\n"
"s_cbranch_scc1 PE_%=\n"
"s_cmp_eq_u32 s33, 2\n"
"s_cbranch_scc0 PC0_%=\n"
"s_add_u32 m0, s34, 69632\n"
"s_nop 0\n"
"s_add_u32 s35, s42, 8192\n"
"buffer_load_dwordx4 v24, s[24:27], s35 offen sc1 lds\n"
"buffer_load_dwordx4 v25, s[24:27], s35 offen offset:1024 sc1 lds\n"
"buffer_load_dwordx4 v26, s[24:27], s35 offen offset:2048 sc1 lds\n"
"buffer_load_dwordx4 v27, s[24:27], s35 offen offset:3072 sc1 lds\n"
"PC0_%=:\n"
"s_add_u32 m0, s34, 65536\n"
"s_add_u32 s35, s42, 0\n"
"buffer_load_dwordx4 v24, s[24:27], s35 offen sc1 lds\n"
"buffer_load_dwordx4 v25, s[24:27], s35 offen offset:1024 sc1 lds\n"
"buffer_load_dwordx4 v26, s[24:27], s35 offen offset:2048 sc1 lds\n"
"buffer_load_dwordx4 v27, s[24:27], s35 offen offset:3072 sc1 lds\n"
"s_cmp_eq_u32 s33, 2\n"
"s_cbranch_scc0 PC1_%=\n"
"s_add_u32 m0, s34, 102400\n"
"s_nop 0\n"
"s_add_u32 s35, s42, 24576\n"
"buffer_load_dwordx4 v24, s[24:27], s35 offen sc1 lds\n"
"buffer_load_dwordx4 v25, s[24:27], s35 offen offset:1024 sc1 lds\n"
"buffer_load_dwordx4 v26, s[24:27], s35 offen offset:2048 sc1 lds\n"
"buffer_load_dwordx4 v27, s[24:27], s35 offen offset:3072 sc1 lds\n"
"PC1_%=:\n"
"s_add_u32 m0, s34, 98304\n"
"s_add_u32 s35, s42, 16384\n"
"buffer_load_dwordx4 v24, s[24:27], s35 offen sc1 lds\n"
"buffer_load_dwordx4 v25, s[24:27], s35 offen offset:1024 sc1 lds\n"
"buffer_load_dwordx4 v26, s[24:27], s35 offen offset:2048 sc1 lds\n"
"buffer_load_dwordx4 v27, s[24:27], s35 offen offset:3072 sc1 lds\n"
"s_cmp_eq_u32 s33, 2\n"
"s_cbranch_scc0 PC2_%=\n"
"s_add_u32 m0, s34, 135168\n"
"s_nop 0\n"
"s_add_u32 s35, s42, 40960\n"
"buffer_load_dwordx4 v24, s[24:27], s35 offen sc1 lds\n"
"buffer_load_dwordx4 v25, s[24:27], s35 offen offset:1024 sc1 lds\n"
"buffer_load_dwordx4 v26, s[24:27], s35 offen offset:2048 sc1 lds\n"
"buffer_load_dwordx4 v27, s[24:27], s35 offen offset:3072 sc1 lds\n"
"PC2_%=:\n"
"s_add_u32 m0, s34, 131072\n"
"s_add_u32 s35, s42, 32768\n"
"buffer_load_dwordx4 v24, s[24:27], s35 offen sc1 lds\n"
"buffer_load_dwordx4 v25, s[24:27], s35 offen offset:1024 sc1 lds\n"
"buffer_load_dwordx4 v26, s[24:27], s35 offen offset:2048 sc1 lds\n"
"buffer_load_dwordx4 v27, s[24:27], s35 offen offset:3072 sc1 lds\n"
"s_waitcnt vmcnt(30)\n"
"v_xor_b32 v75, s86, v77\n"
"s_waitcnt vmcnt(29)\n"
"v_bitop3_b32 v75, v75, v79, s86 bitop3:0xf6\n"
"s_waitcnt vmcnt(28)\n"
"v_bitop3_b32 v75, v75, v81, s86 bitop3:0xf6\n"
"v_cmp_ne_u32 vcc, 0, v75\n"
"s_nop 1\n"
"s_cbranch_vccnz SLOWG_%=\n"
"BACKG_%=:\n"
"v_add_f32 v76, v76, v78\n"
"v_mov_b32 v75, 0\n"
"v_add_f32 v76, v76, v80\n"
"v_cvt_pk_bf16_f32 v76, v75, v76\n"
"ds_write_b32 v72, v76\n"
"PE_%=:\n"
"s_mov_b32 s62, 0x22222222\n"
"s_mov_b32 s63, 0x22222222\n"
"s_mov_b32 s64, 0x44444444\n"
"s_mov_b32 s65, 0x44444444\n"
"s_mov_b32 s66, 0x88888888\n"
"s_mov_b32 s67, 0x88888888\n"
"s_xor_b32 s55, s52, 8\n"
"s_lshl_b32 s55, s55, 13\n"
"s_add_u32 s87, s54, s55\n"
"s_add_u32 s87, s87, 2048\n"
"s_xor_b32 s55, s52, 4\n"
"s_lshl_b32 s55, s55, 13\n"
"s_add_u32 s82, s54, s55\n"
"s_add_u32 s82, s82, 4096\n"
"s_xor_b32 s55, s52, 0\n"
"s_lshl_b32 s55, s55, 13\n"
"s_add_u32 s68, s54, s55\n"
"s_add_u32 s69, s68, 4096\n"
"s_lshl_b32 s55, s33, 13\n"
"s_sub_u32 s54, s54, s55\n"
"s_sub_u32 s58, s68, s55\n"
"s_lshl_b32 s55, s33, 15\n"
"s_add_u32 s54, s54, s55\n"
"s_lshr_b32 s55, s52, 2\n"
"s_xor_b32 s55, s55, s33\n"
"s_lshl_b32 s55, s55, 13\n"
"s_add_u32 s54, s54, s55\n"
"s_sub_u32 s54, s54, 2048\n"
"s_add_u32 s70, s58, 6144\n"
"s_add_u32 s71, s58, 14336\n"
"s_add_u32 s74, s58, 22528\n"
"s_cmp_eq_u32 s55, 0\n"
"s_cselect_b32 s19, 2147483632, s54\n"
"s_cselect_b32 s70, s70, 2147483632\n"
"s_cselect_b32 s71, s71, 2147483632\n"
"s_cselect_b32 s74, s74, 2147483632\n"
"s_lshl_b32 s54, s85, 9\n"
"s_add_u32 s54, s54, 32800\n"
"v_bfe_u32 v64, v65, 2, 2\n"
"v_lshl_add_u32 v64, v64, 7, s54\n"
"v_and_b32 v66, 3, v65\n"
"v_lshl_add_u32 v17, v66, 2, v64\n"
"s_cmp_eq_u32 s33, 3\n"
"s_cbranch_scc0 PF_%=\n"
"s_waitcnt vmcnt(0)\n"
"v_xor_b32 v71, s86, v83\n"
"v_bitop3_b32 v71, v71, v84, s86 bitop3:0xf6\n"
"v_cmp_ne_u32 vcc, 0, v71\n"
"s_nop 1\n"
"s_cbranch_vccnz SLOWY_%=\n"
"BACKY_%=:\n"
"s_branch PG_%=\n"
"PF_%=:\n"
"s_cmp_eq_u32 s33, 2\n"
"s_cbranch_scc0 PH_%=\n"
"s_waitcnt vmcnt(16)\n"
"s_branch PG_%=\n"
"PH_%=:\n"
"s_waitcnt vmcnt(8)\n"
"s_waitcnt vmcnt(8)\n"
"PG_%=:\n"
"s_barrier\n"
"ds_read_b64_tr_b16 v[224:225], v249 offset:16384\n"
"ds_read_b64_tr_b16 v[226:227], v249 offset:18432\n"
"ds_read_b64_tr_b16 v[228:229], v249 offset:20480\n"
"ds_read_b64_tr_b16 v[230:231], v249 offset:22528\n"
"ds_read_b64_tr_b16 a[0:1], v33 offset:0\n"
"ds_read_b64_tr_b16 a[2:3], v33 offset:2048\n"
"ds_read_b64_tr_b16 a[16:17], v33 offset:4096\n"
"ds_read_b64_tr_b16 a[18:19], v33 offset:6144\n"
"ds_read_b64_tr_b16 a[4:5], v36 offset:0\n"
"ds_read_b64_tr_b16 a[6:7], v36 offset:2048\n"
"ds_read_b64_tr_b16 a[20:21], v36 offset:4096\n"
"ds_read_b64_tr_b16 a[22:23], v36 offset:6144\n"
"ds_read_b64_tr_b16 a[8:9], v39 offset:0\n"
"ds_read_b64_tr_b16 a[10:11], v39 offset:2048\n"
"ds_read_b64_tr_b16 a[24:25], v39 offset:4096\n"
"ds_read_b64_tr_b16 a[26:27], v39 offset:6144\n"
"ds_read_b64_tr_b16 a[12:13], v42 offset:0\n"
"ds_read_b64_tr_b16 a[14:15], v42 offset:2048\n"
"ds_read_b64_tr_b16 a[28:29], v42 offset:4096\n"
"ds_read_b64_tr_b16 a[30:31], v42 offset:6144\n"
"ds_read_b64_tr_b16 v[232:233], v249 offset:24576\n"
"ds_read_b64_tr_b16 v[234:235], v249 offset:26624\n"
"ds_read_b64_tr_b16 v[236:237], v249 offset:28672\n"
"ds_read_b64_tr_b16 v[238:239], v249 offset:30720\n"
"s_add_u32 m0, s77, 152576\n"
"s_add_u32 s35, s77, s79\n"
"buffer_load_dwordx4 v8, s[36:39], s35 offen sc1 lds\n"
"buffer_load_dwordx4 v8, s[36:39], s35 offen offset:1024 sc1 lds\n"
"s_mov_b32 m0, 160768\n"
"s_add_u32 s35, s79, 8192\n"
"buffer_load_dwordx4 v8, s[36:39], s35 offen sc1 lds\n"
"s_add_u32 s35, s42, 49152\n"
"s_waitcnt lgkmcnt(14)\n"
"v_mfma_f32_16x16x32_bf16 v[64:67], v[224:227], a[0:3], 0\n"
"ds_read_b64_tr_b16 v[240:241], v252 offset:16384\n"
"s_add_u32 m0, s34, 16384\n"
"v_mfma_f32_16x16x32_bf16 v[64:67], v[228:231], a[16:19], v[64:67]\n"
"buffer_load_dwordx4 v24, s[24:27], s35 offen sc1 lds\n"
"s_waitcnt lgkmcnt(13)\n"
"v_mfma_f32_16x16x32_bf16 v[68:71], v[224:227], a[4:7], 0\n"
"ds_read_b64_tr_b16 v[242:243], v252 offset:18432\n"
"v_mfma_f32_16x16x32_bf16 v[68:71], v[228:231], a[20:23], v[68:71]\n"
"ds_read_b64_tr_b16 v[244:245], v252 offset:20480\n"
"s_waitcnt lgkmcnt(11)\n"
"v_mfma_f32_16x16x32_bf16 v[72:75], v[224:227], a[8:11], 0\n"
"ds_read_b64_tr_b16 v[246:247], v252 offset:22528\n"
"v_mfma_f32_16x16x32_bf16 v[72:75], v[228:231], a[24:27], v[72:75]\n"
"buffer_load_dwordx4 v25, s[24:27], s35 offen offset:1024 sc1 lds\n"
"s_waitcnt lgkmcnt(8)\n"
"v_mfma_f32_16x16x32_bf16 v[76:79], v[224:227], a[12:15], 0\n"
"v_mfma_f32_16x16x32_bf16 v[76:79], v[228:231], a[28:31], v[76:79]\n"
"s_waitcnt lgkmcnt(4)\n"
"v_mfma_f32_16x16x32_bf16 v[80:83], v[232:235], a[0:3], 0\n"
"ds_read_b64_tr_b16 v[224:225], v252 offset:24576\n"
"v_mfma_f32_16x16x32_bf16 v[80:83], v[236:239], a[16:19], v[80:83]\n"
"buffer_load_dwordx4 v26, s[24:27], s35 offen offset:2048 sc1 lds\n"
"v_mfma_f32_16x16x32_bf16 v[84:87], v[232:235], a[4:7], 0\n"
"ds_read_b64_tr_b16 v[226:227], v252 offset:26624\n"
"v_mfma_f32_16x16x32_bf16 v[84:87], v[236:239], a[20:23], v[84:87]\n"
"ds_read_b64_tr_b16 v[228:229], v252 offset:28672\n"
"v_mfma_f32_16x16x32_bf16 v[88:91], v[232:235], a[8:11], 0\n"
"ds_read_b64_tr_b16 v[230:231], v252 offset:30720\n"
"v_mfma_f32_16x16x32_bf16 v[88:91], v[236:239], a[24:27], v[88:91]\n"
"buffer_load_dwordx4 v27, s[24:27], s35 offen offset:3072 sc1 lds\n"
"v_mfma_f32_16x16x32_bf16 v[92:95], v[232:235], a[12:15], 0\n"
"s_add_u32 m0, s34, 49152\n"
"s_add_u32 s35, s42, 65536\n"
"v_mfma_f32_16x16x32_bf16 v[92:95], v[236:239], a[28:31], v[92:95]\n"
"s_waitcnt vmcnt(11) lgkmcnt(4)\n"
"s_barrier\n"
"v_mfma_f32_16x16x32_bf16 v[96:99], v[240:243], a[0:3], 0\n"
"ds_read_b64_tr_b16 v[232:233], v249 offset:49152\n"
"ds_read_b64_tr_b16 v[234:235], v249 offset:51200\n"
"ds_read_b64_tr_b16 v[236:237], v249 offset:53248\n"
"v_mfma_f32_16x16x32_bf16 v[96:99], v[244:247], a[16:19], v[96:99]\n"
"buffer_load_dwordx4 v24, s[24:27], s35 offen sc1 lds\n"
"v_mfma_f32_16x16x32_bf16 v[100:103], v[240:243], a[4:7], 0\n"
"ds_read_b64_tr_b16 v[238:239], v249 offset:55296\n"
"ds_read_b64_tr_b16 a[32:33], v33 offset:32768\n"
"ds_read_b64_tr_b16 a[34:35], v33 offset:34816\n"
"v_mfma_f32_16x16x32_bf16 v[100:103], v[244:247], a[20:23], v[100:103]\n"
"ds_read_b64_tr_b16 a[48:49], v33 offset:36864\n"
"ds_read_b64_tr_b16 a[50:51], v33 offset:38912\n"
"v_mfma_f32_16x16x32_bf16 v[104:107], v[240:243], a[8:11], 0\n"
"ds_read_b64_tr_b16 a[36:37], v36 offset:32768\n"
"ds_read_b64_tr_b16 a[38:39], v36 offset:34816\n"
"v_mfma_f32_16x16x32_bf16 v[104:107], v[244:247], a[24:27], v[104:107]\n"
"buffer_load_dwordx4 v25, s[24:27], s35 offen offset:1024 sc1 lds\n"
"v_mfma_f32_16x16x32_bf16 v[108:111], v[240:243], a[12:15], 0\n"
"ds_read_b64_tr_b16 a[52:53], v36 offset:36864\n"
"ds_read_b64_tr_b16 a[54:55], v36 offset:38912\n"
"v_mfma_f32_16x16x32_bf16 v[108:111], v[244:247], a[28:31], v[108:111]\n"
"ds_read_b64_tr_b16 a[40:41], v39 offset:32768\n"
"s_waitcnt lgkmcnt(13)\n"
"v_mfma_f32_16x16x32_bf16 v[112:115], v[224:227], a[0:3], 0\n"
"ds_read_b64_tr_b16 a[42:43], v39 offset:34816\n"
"ds_read_b64_tr_b16 a[56:57], v39 offset:36864\n"
"ds_read_b64_tr_b16 a[58:59], v39 offset:38912\n"
"v_mfma_f32_16x16x32_bf16 v[112:115], v[228:231], a[16:19], v[112:115]\n"
"buffer_load_dwordx4 v26, s[24:27], s35 offen offset:2048 sc1 lds\n"
"v_mfma_f32_16x16x32_bf16 v[116:119], v[224:227], a[4:7], 0\n"
"ds_read_b64_tr_b16 a[44:45], v42 offset:32768\n"
"ds_read_b64_tr_b16 a[46:47], v42 offset:34816\n"
"v_mfma_f32_16x16x32_bf16 v[116:119], v[228:231], a[20:23], v[116:119]\n"
"ds_read_b64_tr_b16 a[60:61], v42 offset:36864\n"
"ds_read_b64_tr_b16 a[62:63], v42 offset:38912\n"
"v_mfma_f32_16x16x32_bf16 v[120:123], v[224:227], a[8:11], 0\n"
"ds_read_b64_tr_b16 v[240:241], v249 offset:57344\n"
"ds_read_b64_tr_b16 v[242:243], v249 offset:59392\n"
"v_mfma_f32_16x16x32_bf16 v[120:123], v[228:231], a[24:27], v[120:123]\n"
"buffer_load_dwordx4 v27, s[24:27], s35 offen offset:3072 sc1 lds\n"
"v_mfma_f32_16x16x32_bf16 v[124:127], v[224:227], a[12:15], 0\n"
"ds_read_b64_tr_b16 v[244:245], v249 offset:61440\n"
"ds_read_b64_tr_b16 v[246:247], v249 offset:63488\n"
"v_mfma_f32_16x16x32_bf16 v[124:127], v[228:231], a[28:31], v[124:127]\n"
"s_waitcnt lgkmcnt(14)\n"
"v_mfma_f32_16x16x32_bf16 v[64:67], v[232:235], a[32:35], v[64:67]\n"
"ds_read_b64_tr_b16 v[224:225], v252 offset:49152\n"
"v_mfma_f32_16x16x32_bf16 v[64:67], v[236:239], a[48:51], v[64:67]\n"
"buffer_load_dword v176, v184, s[36:39], 0 offen sc1\n"
"s_waitcnt lgkmcnt(13)\n"
"v_mfma_f32_16x16x32_bf16 v[68:71], v[232:235], a[36:39], v[68:71]\n"
"ds_read_b64_tr_b16 v[226:227], v252 offset:51200\n"
"v_mfma_f32_16x16x32_bf16 v[68:71], v[236:239], a[52:55], v[68:71]\n"
"ds_read_b64_tr_b16 v[228:229], v252 offset:53248\n"
"s_add_u32 s75, s40, 2359296\n"
"s_waitcnt lgkmcnt(11)\n"
"v_mfma_f32_16x16x32_bf16 v[72:75], v[232:235], a[40:43], v[72:75]\n"
"ds_read_b64_tr_b16 v[230:231], v252 offset:55296\n"
"s_add_u32 m0, s34, 65536\n"
"v_mfma_f32_16x16x32_bf16 v[72:75], v[236:239], a[56:59], v[72:75]\n"
"buffer_load_dwordx4 v20, s[20:23], s75 offen lds\n"
"s_waitcnt lgkmcnt(8)\n"
"v_mfma_f32_16x16x32_bf16 v[76:79], v[232:235], a[44:47], v[76:79]\n"
"v_mfma_f32_16x16x32_bf16 v[76:79], v[236:239], a[60:63], v[76:79]\n"
"s_waitcnt lgkmcnt(4)\n"
"v_mfma_f32_16x16x32_bf16 v[80:83], v[240:243], a[32:35], v[80:83]\n"
"ds_read_b64_tr_b16 v[232:233], v252 offset:57344\n"
"v_mfma_f32_16x16x32_bf16 v[80:83], v[244:247], a[48:51], v[80:83]\n"
"buffer_load_dwordx4 v21, s[20:23], s75 offen offset:1024 lds\n"
"v_mfma_f32_16x16x32_bf16 v[84:87], v[240:243], a[36:39], v[84:87]\n"
"ds_read_b64_tr_b16 v[234:235], v252 offset:59392\n"
"v_mfma_f32_16x16x32_bf16 v[84:87], v[244:247], a[52:55], v[84:87]\n"
"ds_read_b64_tr_b16 v[236:237], v252 offset:61440\n"
"v_mfma_f32_16x16x32_bf16 v[88:91], v[240:243], a[40:43], v[88:91]\n"
"ds_read_b64_tr_b16 v[238:239], v252 offset:63488\n"
"v_mfma_f32_16x16x32_bf16 v[88:91], v[244:247], a[56:59], v[88:91]\n"
"buffer_load_dwordx4 v22, s[20:23], s75 offen offset:2048 lds\n"
"v_mfma_f32_16x16x32_bf16 v[92:95], v[240:243], a[44:47], v[92:95]\n"
"s_add_u32 s35, s42, 81920\n"
"v_mfma_f32_16x16x32_bf16 v[92:95], v[244:247], a[60:63], v[92:95]\n"
"s_waitcnt vmcnt(12) lgkmcnt(4)\n"
"s_barrier\n"
"v_mfma_f32_16x16x32_bf16 v[96:99], v[224:227], a[32:35], v[96:99]\n"
"ds_read_b64_tr_b16 v[240:241], v248 offset:0\n"
"ds_read_b64_tr_b16 v[242:243], v248 offset:2048\n"
"ds_read_b64_tr_b16 v[244:245], v248 offset:4096\n"
"v_mfma_f32_16x16x32_bf16 v[96:99], v[228:231], a[48:51], v[96:99]\n"
"buffer_load_dwordx4 v23, s[20:23], s75 offen offset:3072 lds\n"
"v_mfma_f32_16x16x32_bf16 v[100:103], v[224:227], a[36:39], v[100:103]\n"
"ds_read_b64_tr_b16 v[246:247], v248 offset:6144\n"
"ds_read_b64_tr_b16 a[64:65], v34 offset:0\n"
"ds_read_b64_tr_b16 a[66:67], v34 offset:2048\n"
"v_mfma_f32_16x16x32_bf16 v[100:103], v[228:231], a[52:55], v[100:103]\n"
"ds_read_b64_tr_b16 a[80:81], v34 offset:4096\n"
"ds_read_b64_tr_b16 a[82:83], v34 offset:6144\n"
"v_mfma_f32_16x16x32_bf16 v[104:107], v[224:227], a[40:43], v[104:107]\n"
"ds_read_b64_tr_b16 a[68:69], v37 offset:0\n"
"ds_read_b64_tr_b16 a[70:71], v37 offset:2048\n"
"s_add_u32 m0, s34, 81920\n"
"v_mfma_f32_16x16x32_bf16 v[104:107], v[228:231], a[56:59], v[104:107]\n"
"buffer_load_dwordx4 v24, s[24:27], s35 offen sc1 lds\n"
"v_mfma_f32_16x16x32_bf16 v[108:111], v[224:227], a[44:47], v[108:111]\n"
"ds_read_b64_tr_b16 a[84:85], v37 offset:4096\n"
"ds_read_b64_tr_b16 a[86:87], v37 offset:6144\n"
"v_mfma_f32_16x16x32_bf16 v[108:111], v[228:231], a[60:63], v[108:111]\n"
"ds_read_b64_tr_b16 a[72:73], v40 offset:0\n"
"s_waitcnt lgkmcnt(13)\n"
"v_mfma_f32_16x16x32_bf16 v[112:115], v[232:235], a[32:35], v[112:115]\n"
"ds_read_b64_tr_b16 a[74:75], v40 offset:2048\n"
"ds_read_b64_tr_b16 a[88:89], v40 offset:4096\n"
"ds_read_b64_tr_b16 a[90:91], v40 offset:6144\n"
"v_mfma_f32_16x16x32_bf16 v[112:115], v[236:239], a[48:51], v[112:115]\n"
"buffer_load_dwordx4 v25, s[24:27], s35 offen offset:1024 sc1 lds\n"
"v_mfma_f32_16x16x32_bf16 v[116:119], v[232:235], a[36:39], v[116:119]\n"
"ds_read_b64_tr_b16 a[76:77], v43 offset:0\n"
"ds_read_b64_tr_b16 a[78:79], v43 offset:2048\n"
"v_mfma_f32_16x16x32_bf16 v[116:119], v[236:239], a[52:55], v[116:119]\n"
"ds_read_b64_tr_b16 a[92:93], v43 offset:4096\n"
"ds_read_b64_tr_b16 a[94:95], v43 offset:6144\n"
"v_mfma_f32_16x16x32_bf16 v[120:123], v[232:235], a[40:43], v[120:123]\n"
"ds_read_b64_tr_b16 v[224:225], v248 offset:8192\n"
"ds_read_b64_tr_b16 v[226:227], v248 offset:10240\n"
"v_mfma_f32_16x16x32_bf16 v[120:123], v[236:239], a[56:59], v[120:123]\n"
"buffer_load_dwordx4 v26, s[24:27], s35 offen offset:2048 sc1 lds\n"
"v_mfma_f32_16x16x32_bf16 v[124:127], v[232:235], a[44:47], v[124:127]\n"
"ds_read_b64_tr_b16 v[228:229], v248 offset:12288\n"
"ds_read_b64_tr_b16 v[230:231], v248 offset:14336\n"
"v_mfma_f32_16x16x32_bf16 v[124:127], v[236:239], a[60:63], v[124:127]\n"
"s_waitcnt lgkmcnt(14)\n"
"v_mfma_f32_16x16x32_bf16 v[64:67], v[240:243], a[64:67], v[64:67]\n"
"ds_read_b64_tr_b16 v[232:233], v251 offset:0\n"
"v_mfma_f32_16x16x32_bf16 v[64:67], v[244:247], a[80:83], v[64:67]\n"
"buffer_load_dwordx4 v27, s[24:27], s35 offen offset:3072 sc1 lds\n"
"s_waitcnt lgkmcnt(13)\n"
"v_mfma_f32_16x16x32_bf16 v[68:71], v[240:243], a[68:71], v[68:71]\n"
"ds_read_b64_tr_b16 v[234:235], v251 offset:2048\n"
"v_mfma_f32_16x16x32_bf16 v[68:71], v[244:247], a[84:87], v[68:71]\n"
"ds_read_b64_tr_b16 v[236:237], v251 offset:4096\n"
"s_add_u32 s75, s40, 2949120\n"
"s_waitcnt lgkmcnt(11)\n"
"v_mfma_f32_16x16x32_bf16 v[72:75], v[240:243], a[72:75], v[72:75]\n"
"ds_read_b64_tr_b16 v[238:239], v251 offset:6144\n"
"s_add_u32 m0, s34, 98304\n"
"v_mfma_f32_16x16x32_bf16 v[72:75], v[244:247], a[88:91], v[72:75]\n"
"buffer_load_dwordx4 v20, s[20:23], s75 offen lds\n"
"s_waitcnt lgkmcnt(8)\n"
"v_mfma_f32_16x16x32_bf16 v[76:79], v[240:243], a[76:79], v[76:79]\n"
"v_mfma_f32_16x16x32_bf16 v[76:79], v[244:247], a[92:95], v[76:79]\n"
"s_waitcnt lgkmcnt(4)\n"
"v_mfma_f32_16x16x32_bf16 v[80:83], v[224:227], a[64:67], v[80:83]\n"
"ds_read_b64_tr_b16 v[240:241], v251 offset:8192\n"
"v_mfma_f32_16x16x32_bf16 v[80:83], v[228:231], a[80:83], v[80:83]\n"
"buffer_load_dwordx4 v21, s[20:23], s75 offen offset:1024 lds\n"
"v_mfma_f32_16x16x32_bf16 v[84:87], v[224:227], a[68:71], v[84:87]\n"
"ds_read_b64_tr_b16 v[242:243], v251 offset:10240\n"
"s_add_u32 s35, s42, 98304\n"
"v_mfma_f32_16x16x32_bf16 v[84:87], v[228:231], a[84:87], v[84:87]\n"
"ds_read_b64_tr_b16 v[244:245], v251 offset:12288\n"
"ds_read_b64_tr_b16 v[128:129], v182 offset:0\n"
"ds_read_b64_tr_b16 v[130:131], v182 offset:1024\n"
"v_mfma_f32_16x16x32_bf16 v[88:91], v[224:227], a[72:75], v[88:91]\n"
"ds_read_b64_tr_b16 v[246:247], v251 offset:14336\n"
"ds_read_b64_tr_b16 v[132:133], v182 offset:2048\n"
"ds_read_b64_tr_b16 v[134:135], v182 offset:3072\n"
"v_mfma_f32_16x16x32_bf16 v[88:91], v[228:231], a[88:91], v[88:91]\n"
"buffer_load_dwordx4 v22, s[20:23], s75 offen offset:2048 lds\n"
"v_mfma_f32_16x16x32_bf16 v[92:95], v[224:227], a[76:79], v[92:95]\n"
"ds_read_b64_tr_b16 v[136:137], v182 offset:4096\n"
"ds_read_b64_tr_b16 v[138:139], v182 offset:5120\n"
"ds_read_b64_tr_b16 v[140:141], v182 offset:6144\n"
"v_mfma_f32_16x16x32_bf16 v[92:95], v[228:231], a[92:95], v[92:95]\n"
"s_waitcnt vmcnt(16) lgkmcnt(11)\n"
"s_barrier\n"
"v_mfma_f32_16x16x32_bf16 v[96:99], v[232:235], a[64:67], v[96:99]\n"
"ds_read_b64_tr_b16 v[224:225], v248 offset:32768\n"
"ds_read_b64_tr_b16 v[226:227], v248 offset:34816\n"
"ds_read_b64_tr_b16 v[228:229], v248 offset:36864\n"
"v_mfma_f32_16x16x32_bf16 v[96:99], v[236:239], a[80:83], v[96:99]\n"
"buffer_load_dwordx4 v23, s[20:23], s75 offen offset:3072 lds\n"
"v_mfma_f32_16x16x32_bf16 v[100:103], v[232:235], a[68:71], v[100:103]\n"
"ds_read_b64_tr_b16 v[230:231], v248 offset:38912\n"
"ds_read_b64_tr_b16 a[96:97], v32 offset:16384\n"
"ds_read_b64_tr_b16 a[98:99], v32 offset:18432\n"
"v_mfma_f32_16x16x32_bf16 v[100:103], v[236:239], a[84:87], v[100:103]\n"
"ds_read_b64_tr_b16 a[112:113], v32 offset:20480\n"
"ds_read_b64_tr_b16 a[114:115], v32 offset:22528\n"
"ds_read_b64_tr_b16 v[142:143], v182 offset:7168\n"
"v_mfma_f32_16x16x32_bf16 v[104:107], v[232:235], a[72:75], v[104:107]\n"
"ds_read_b64_tr_b16 a[100:101], v35 offset:16384\n"
"ds_read_b64_tr_b16 a[102:103], v35 offset:18432\n"
"ds_read_b64_tr_b16 v[144:145], v182 offset:8192\n"
"s_add_u32 m0, s34, 114688\n"
"v_mfma_f32_16x16x32_bf16 v[104:107], v[236:239], a[88:91], v[104:107]\n"
"buffer_load_dwordx4 v24, s[24:27], s35 offen sc1 lds\n"
"v_mfma_f32_16x16x32_bf16 v[108:111], v[232:235], a[76:79], v[108:111]\n"
"ds_read_b64_tr_b16 a[116:117], v35 offset:20480\n"
"ds_read_b64_tr_b16 a[118:119], v35 offset:22528\n"
"v_mfma_f32_16x16x32_bf16 v[108:111], v[236:239], a[92:95], v[108:111]\n"
"ds_read_b64_tr_b16 a[104:105], v38 offset:16384\n"
"s_waitcnt lgkmcnt(14)\n"
"v_mfma_f32_16x16x32_bf16 v[112:115], v[240:243], a[64:67], v[112:115]\n"
"ds_read_b64_tr_b16 a[106:107], v38 offset:18432\n"
"ds_read_b64_tr_b16 a[120:121], v38 offset:20480\n"
"ds_read_b64_tr_b16 a[122:123], v38 offset:22528\n"
"v_mfma_f32_16x16x32_bf16 v[112:115], v[244:247], a[80:83], v[112:115]\n"
"buffer_load_dwordx4 v25, s[24:27], s35 offen offset:1024 sc1 lds\n"
"v_mfma_f32_16x16x32_bf16 v[116:119], v[240:243], a[68:71], v[116:119]\n"
"ds_read_b64_tr_b16 a[108:109], v41 offset:16384\n"
"ds_read_b64_tr_b16 a[110:111], v41 offset:18432\n"
"v_mfma_f32_16x16x32_bf16 v[116:119], v[244:247], a[84:87], v[116:119]\n"
"ds_read_b64_tr_b16 a[124:125], v41 offset:20480\n"
"ds_read_b64_tr_b16 a[126:127], v41 offset:22528\n"
"v_mfma_f32_16x16x32_bf16 v[120:123], v[240:243], a[72:75], v[120:123]\n"
"ds_read_b64_tr_b16 v[232:233], v248 offset:40960\n"
"ds_read_b64_tr_b16 v[234:235], v248 offset:43008\n"
"v_mfma_f32_16x16x32_bf16 v[120:123], v[244:247], a[88:91], v[120:123]\n"
"buffer_load_dwordx4 v26, s[24:27], s35 offen offset:2048 sc1 lds\n"
"v_mfma_f32_16x16x32_bf16 v[124:127], v[240:243], a[76:79], v[124:127]\n"
"ds_read_b64_tr_b16 v[236:237], v248 offset:45056\n"
"ds_read_b64_tr_b16 v[238:239], v248 offset:47104\n"
"v_mfma_f32_16x16x32_bf16 v[124:127], v[244:247], a[92:95], v[124:127]\n"
"s_waitcnt lgkmcnt(14)\n"
"v_mfma_f32_16x16x32_bf16 v[152:155], v[148:151], v[128:131], 0\n"
"v_mfma_f32_16x16x32_bf16 v[156:159], v[128:131], v[128:131], 0\n"
"buffer_load_dwordx4 v27, s[24:27], s35 offen offset:3072 sc1 lds\n"
"v_mfma_f32_16x16x32_bf16 v[152:155], v[148:151], v[132:135], v[152:155]\n"
"v_mfma_f32_16x16x32_bf16 v[156:159], v[132:135], v[132:135], v[156:159]\n"
"v_mfma_f32_16x16x32_bf16 v[152:155], v[148:151], v[136:139], v[152:155]\n"
"s_add_u32 m0, s34, 131072\n"
"s_add_u32 s75, s40, 3538944\n"
"v_mfma_f32_16x16x32_bf16 v[156:159], v[136:139], v[136:139], v[156:159]\n"
"buffer_load_dwordx4 v20, s[20:23], s75 offen lds\n"
"v_mfma_f32_16x16x32_bf16 v[152:155], v[148:151], v[140:143], v[152:155]\n"
"v_mfma_f32_16x16x32_bf16 v[156:159], v[140:143], v[140:143], v[156:159]\n"
"v_mfma_f32_16x16x16_bf16 v[152:155], v[148:149], v[144:145], v[152:155]\n"
"v_mfma_f32_16x16x16_bf16 v[156:159], v[144:145], v[144:145], v[156:159]\n"
"v_mfma_f32_16x16x32_bf16 v[64:67], v[224:227], a[96:99], v[64:67]\n"
"ds_read_b64_tr_b16 v[240:241], v251 offset:32768\n"
"v_mfma_f32_16x16x32_bf16 v[64:67], v[228:231], a[112:115], v[64:67]\n"
"buffer_load_dwordx4 v21, s[20:23], s75 offen offset:1024 lds\n"
"s_waitcnt lgkmcnt(13)\n"
"v_mfma_f32_16x16x32_bf16 v[68:71], v[224:227], a[100:103], v[68:71]\n"
"ds_read_b64_tr_b16 v[242:243], v251 offset:34816\n"
"v_mfma_f32_16x16x32_bf16 v[68:71], v[228:231], a[116:119], v[68:71]\n"
"ds_read_b64_tr_b16 v[244:245], v251 offset:36864\n"
"v_cndmask_b32 v154, v156, v157, s[62:63]\n"
"s_waitcnt lgkmcnt(11)\n"
"v_mfma_f32_16x16x32_bf16 v[72:75], v[224:227], a[104:107], v[72:75]\n"
"ds_read_b64_tr_b16 v[246:247], v251 offset:38912\n"
"v_mov_b32 v153, s86\n"
"v_mfma_f32_16x16x32_bf16 v[72:75], v[228:231], a[120:123], v[72:75]\n"
"buffer_load_dwordx4 v22, s[20:23], s75 offen offset:2048 lds\n"
"s_waitcnt lgkmcnt(8)\n"
"v_mfma_f32_16x16x32_bf16 v[76:79], v[224:227], a[108:111], v[76:79]\n"
"v_cndmask_b32 v154, v154, v158, s[64:65]\n"
"v_mov_b32 v155, s86\n"
"v_mfma_f32_16x16x32_bf16 v[76:79], v[228:231], a[124:127], v[76:79]\n"
"v_cndmask_b32 v154, v154, v159, s[66:67]\n"
"s_waitcnt lgkmcnt(4)\n"
"v_mfma_f32_16x16x32_bf16 v[80:83], v[232:235], a[96:99], v[80:83]\n"
"ds_read_b64_tr_b16 v[224:225], v251 offset:40960\n"
"s_waitcnt vmcnt(19)\n"
"v_cmp_ne_u32 vcc, s86, v176\n"
"v_mfma_f32_16x16x32_bf16 v[80:83], v[236:239], a[112:115], v[80:83]\n"
"buffer_store_dwordx4 v[152:155], v180, s[36:39], 0 offen sc1\n"
"v_mfma_f32_16x16x32_bf16 v[84:87], v[232:235], a[100:103], v[84:87]\n"
"ds_read_b64_tr_b16 v[226:227], v251 offset:43008\n"
"s_cbranch_vccnz SLOWU_%=\n"
"BACKU_%=:\n"
"v_mfma_f32_16x16x32_bf16 v[84:87], v[236:239], a[116:119], v[84:87]\n"
"ds_read_b64_tr_b16 v[228:229], v251 offset:45056\n"
"v_mfma_f32_16x16x32_bf16 v[88:91], v[232:235], a[104:107], v[88:91]\n"
"ds_read_b64_tr_b16 v[230:231], v251 offset:47104\n"
"v_mfma_f32_16x16x32_bf16 v[88:91], v[236:239], a[120:123], v[88:91]\n"
"buffer_load_dwordx4 v23, s[20:23], s75 offen offset:3072 lds\n"
"v_mfma_f32_16x16x32_bf16 v[92:95], v[232:235], a[108:111], v[92:95]\n"
"s_add_u32 m0, s34, 0\n"
"s_add_u32 s35, s42, 114688\n"
"v_mfma_f32_16x16x32_bf16 v[92:95], v[236:239], a[124:127], v[92:95]\n"
"s_waitcnt vmcnt(17) lgkmcnt(4)\n"
"s_barrier\n"
"v_mfma_f32_16x16x32_bf16 v[96:99], v[240:243], a[96:99], v[96:99]\n"
"ds_read_b64_tr_b16 v[232:233], v249 offset:0\n"
"ds_read_b64_tr_b16 v[234:235], v249 offset:2048\n"
"ds_read_b64_tr_b16 v[236:237], v249 offset:4096\n"
"v_mfma_f32_16x16x32_bf16 v[96:99], v[244:247], a[112:115], v[96:99]\n"
"buffer_load_dwordx4 v24, s[24:27], s35 offen sc1 lds\n"
"v_mfma_f32_16x16x32_bf16 v[100:103], v[240:243], a[100:103], v[100:103]\n"
"ds_read_b64_tr_b16 v[238:239], v249 offset:6144\n"
"ds_read_b64_tr_b16 a[128:129], v32 offset:49152\n"
"ds_read_b64_tr_b16 a[130:131], v32 offset:51200\n"
"v_mfma_f32_16x16x32_bf16 v[100:103], v[244:247], a[116:119], v[100:103]\n"
"ds_read_b64_tr_b16 a[144:145], v32 offset:53248\n"
"ds_read_b64_tr_b16 a[146:147], v32 offset:55296\n"
"v_mfma_f32_16x16x32_bf16 v[104:107], v[240:243], a[104:107], v[104:107]\n"
"ds_read_b64_tr_b16 a[132:133], v35 offset:49152\n"
"ds_read_b64_tr_b16 a[134:135], v35 offset:51200\n"
"v_mfma_f32_16x16x32_bf16 v[104:107], v[244:247], a[120:123], v[104:107]\n"
"buffer_load_dwordx4 v25, s[24:27], s35 offen offset:1024 sc1 lds\n"
"v_mfma_f32_16x16x32_bf16 v[108:111], v[240:243], a[108:111], v[108:111]\n"
"ds_read_b64_tr_b16 a[148:149], v35 offset:53248\n"
"ds_read_b64_tr_b16 a[150:151], v35 offset:55296\n"
"v_mfma_f32_16x16x32_bf16 v[108:111], v[244:247], a[124:127], v[108:111]\n"
"ds_read_b64_tr_b16 a[136:137], v38 offset:49152\n"
"s_waitcnt lgkmcnt(13)\n"
"v_mfma_f32_16x16x32_bf16 v[112:115], v[224:227], a[96:99], v[112:115]\n"
"ds_read_b64_tr_b16 a[138:139], v38 offset:51200\n"
"ds_read_b64_tr_b16 a[152:153], v38 offset:53248\n"
"ds_read_b64_tr_b16 a[154:155], v38 offset:55296\n"
"v_mfma_f32_16x16x32_bf16 v[112:115], v[228:231], a[112:115], v[112:115]\n"
"buffer_load_dwordx4 v26, s[24:27], s35 offen offset:2048 sc1 lds\n"
"v_mfma_f32_16x16x32_bf16 v[116:119], v[224:227], a[100:103], v[116:119]\n"
"ds_read_b64_tr_b16 a[140:141], v41 offset:49152\n"
"ds_read_b64_tr_b16 a[142:143], v41 offset:51200\n"
"v_mfma_f32_16x16x32_bf16 v[116:119], v[228:231], a[116:119], v[116:119]\n"
"ds_read_b64_tr_b16 a[156:157], v41 offset:53248\n"
"ds_read_b64_tr_b16 a[158:159], v41 offset:55296\n"
"v_mfma_f32_16x16x32_bf16 v[120:123], v[224:227], a[104:107], v[120:123]\n"
"ds_read_b64_tr_b16 v[240:241], v249 offset:8192\n"
"ds_read_b64_tr_b16 v[242:243], v249 offset:10240\n"
"v_mfma_f32_16x16x32_bf16 v[120:123], v[228:231], a[120:123], v[120:123]\n"
"buffer_load_dwordx4 v27, s[24:27], s35 offen offset:3072 sc1 lds\n"
"v_mfma_f32_16x16x32_bf16 v[124:127], v[224:227], a[108:111], v[124:127]\n"
"ds_read_b64_tr_b16 v[244:245], v249 offset:12288\n"
"ds_read_b64_tr_b16 v[246:247], v249 offset:14336\n"
"v_mfma_f32_16x16x32_bf16 v[124:127], v[228:231], a[124:127], v[124:127]\n"
"s_add_u32 s75, s40, 4128768\n"
"s_waitcnt lgkmcnt(14)\n"
"v_mfma_f32_16x16x32_bf16 v[64:67], v[232:235], a[128:131], v[64:67]\n"
"ds_read_b64_tr_b16 v[224:225], v252 offset:0\n"
"s_add_u32 m0, s34, 16384\n"
"v_mfma_f32_16x16x32_bf16 v[64:67], v[236:239], a[144:147], v[64:67]\n"
"buffer_load_dwordx4 v20, s[20:23], s75 offen lds\n"
"s_waitcnt lgkmcnt(13)\n"
"v_mfma_f32_16x16x32_bf16 v[68:71], v[232:235], a[132:135], v[68:71]\n"
"ds_read_b64_tr_b16 v[226:227], v252 offset:2048\n"
"v_mfma_f32_16x16x32_bf16 v[68:71], v[236:239], a[148:151], v[68:71]\n"
"ds_read_b64_tr_b16 v[228:229], v252 offset:4096\n"
"s_waitcnt lgkmcnt(11)\n"
"v_mfma_f32_16x16x32_bf16 v[72:75], v[232:235], a[136:139], v[72:75]\n"
"ds_read_b64_tr_b16 v[230:231], v252 offset:6144\n"
"v_mfma_f32_16x16x32_bf16 v[72:75], v[236:239], a[152:155], v[72:75]\n"
"buffer_load_dwordx4 v21, s[20:23], s75 offen offset:1024 lds\n"
"s_waitcnt lgkmcnt(8)\n"
"v_mfma_f32_16x16x32_bf16 v[76:79], v[232:235], a[140:143], v[76:79]\n"
"v_mfma_f32_16x16x32_bf16 v[76:79], v[236:239], a[156:159], v[76:79]\n"
"s_waitcnt lgkmcnt(4)\n"
"v_mfma_f32_16x16x32_bf16 v[80:83], v[240:243], a[128:131], v[80:83]\n"
"ds_read_b64_tr_b16 v[232:233], v252 offset:8192\n"
"v_mfma_f32_16x16x32_bf16 v[80:83], v[244:247], a[144:147], v[80:83]\n"
"buffer_load_dwordx4 v22, s[20:23], s75 offen offset:2048 lds\n"
"v_mfma_f32_16x16x32_bf16 v[84:87], v[240:243], a[132:135], v[84:87]\n"
"ds_read_b64_tr_b16 v[234:235], v252 offset:10240\n"
"v_mfma_f32_16x16x32_bf16 v[84:87], v[244:247], a[148:151], v[84:87]\n"
"ds_read_b64_tr_b16 v[236:237], v252 offset:12288\n"
"v_mfma_f32_16x16x32_bf16 v[88:91], v[240:243], a[136:139], v[88:91]\n"
"ds_read_b64_tr_b16 v[238:239], v252 offset:14336\n"
"v_mfma_f32_16x16x32_bf16 v[88:91], v[244:247], a[152:155], v[88:91]\n"
"buffer_load_dwordx4 v23, s[20:23], s75 offen offset:3072 lds\n"
"v_mfma_f32_16x16x32_bf16 v[92:95], v[240:243], a[140:143], v[92:95]\n"
"s_add_u32 m0, s78, 147456\n"
"s_add_u32 s35, s42, 131072\n"
"v_mfma_f32_16x16x32_bf16 v[92:95], v[244:247], a[156:159], v[92:95]\n"
"s_waitcnt vmcnt(17) lgkmcnt(4)\n"
"s_barrier\n"
"v_mfma_f32_16x16x32_bf16 v[96:99], v[224:227], a[128:131], v[96:99]\n"
"ds_read_b64_tr_b16 v[240:241], v249 offset:32768\n"
"ds_read_b64_tr_b16 v[242:243], v249 offset:34816\n"
"ds_read_b64_tr_b16 v[244:245], v249 offset:36864\n"
"v_mfma_f32_16x16x32_bf16 v[96:99], v[228:231], a[144:147], v[96:99]\n"
"buffer_load_dwordx4 v178, s[36:39], 0 offen sc1 lds\n"
"v_mfma_f32_16x16x32_bf16 v[100:103], v[224:227], a[132:135], v[100:103]\n"
"ds_read_b64_tr_b16 v[246:247], v249 offset:38912\n"
"ds_read_b64_tr_b16 a[160:161], v33 offset:16384\n"
"ds_read_b64_tr_b16 a[162:163], v33 offset:18432\n"
"v_mfma_f32_16x16x32_bf16 v[100:103], v[228:231], a[148:151], v[100:103]\n"
"ds_read_b64_tr_b16 a[176:177], v33 offset:20480\n"
"ds_read_b64_tr_b16 a[178:179], v33 offset:22528\n"
"v_mfma_f32_16x16x32_bf16 v[104:107], v[224:227], a[136:139], v[104:107]\n"
"ds_read_b64_tr_b16 a[164:165], v36 offset:16384\n"
"ds_read_b64_tr_b16 a[166:167], v36 offset:18432\n"
"s_mov_b32 m0, 151552\n"
"v_mfma_f32_16x16x32_bf16 v[104:107], v[228:231], a[152:155], v[104:107]\n"
"buffer_load_dwordx4 v179, s[36:39], 0 offen sc1 lds\n"
"v_mfma_f32_16x16x32_bf16 v[108:111], v[224:227], a[140:143], v[108:111]\n"
"ds_read_b64_tr_b16 a[180:181], v36 offset:20480\n"
"ds_read_b64_tr_b16 a[182:183], v36 offset:22528\n"
"v_mfma_f32_16x16x32_bf16 v[108:111], v[228:231], a[156:159], v[108:111]\n"
"ds_read_b64_tr_b16 a[168:169], v39 offset:16384\n"
"s_add_u32 m0, s34, 32768\n"
"s_waitcnt lgkmcnt(13)\n"
"v_mfma_f32_16x16x32_bf16 v[112:115], v[232:235], a[128:131], v[112:115]\n"
"ds_read_b64_tr_b16 a[170:171], v39 offset:18432\n"
"ds_read_b64_tr_b16 a[184:185], v39 offset:20480\n"
"ds_read_b64_tr_b16 a[186:187], v39 offset:22528\n"
"v_mfma_f32_16x16x32_bf16 v[112:115], v[236:239], a[144:147], v[112:115]\n"
"buffer_load_dwordx4 v24, s[24:27], s35 offen sc1 lds\n"
"v_mfma_f32_16x16x32_bf16 v[116:119], v[232:235], a[132:135], v[116:119]\n"
"ds_read_b64_tr_b16 a[172:173], v42 offset:16384\n"
"ds_read_b64_tr_b16 a[174:175], v42 offset:18432\n"
"v_mfma_f32_16x16x32_bf16 v[116:119], v[236:239], a[148:151], v[116:119]\n"
"ds_read_b64_tr_b16 a[188:189], v42 offset:20480\n"
"ds_read_b64_tr_b16 a[190:191], v42 offset:22528\n"
"v_mfma_f32_16x16x32_bf16 v[120:123], v[232:235], a[136:139], v[120:123]\n"
"ds_read_b64_tr_b16 v[224:225], v249 offset:40960\n"
"ds_read_b64_tr_b16 v[226:227], v249 offset:43008\n"
"v_mfma_f32_16x16x32_bf16 v[120:123], v[236:239], a[152:155], v[120:123]\n"
"buffer_load_dwordx4 v25, s[24:27], s35 offen offset:1024 sc1 lds\n"
"v_mfma_f32_16x16x32_bf16 v[124:127], v[232:235], a[140:143], v[124:127]\n"
"ds_read_b64_tr_b16 v[228:229], v249 offset:45056\n"
"ds_read_b64_tr_b16 v[230:231], v249 offset:47104\n"
"v_mfma_f32_16x16x32_bf16 v[124:127], v[236:239], a[156:159], v[124:127]\n"
"s_waitcnt lgkmcnt(14)\n"
"v_mfma_f32_16x16x32_bf16 v[64:67], v[240:243], a[160:163], v[64:67]\n"
"ds_read_b64_tr_b16 v[232:233], v252 offset:32768\n"
"v_mfma_f32_16x16x32_bf16 v[64:67], v[244:247], a[176:179], v[64:67]\n"
"buffer_load_dwordx4 v26, s[24:27], s35 offen offset:2048 sc1 lds\n"
"s_waitcnt lgkmcnt(13)\n"
"v_mfma_f32_16x16x32_bf16 v[68:71], v[240:243], a[164:167], v[68:71]\n"
"ds_read_b64_tr_b16 v[234:235], v252 offset:34816\n"
"v_mfma_f32_16x16x32_bf16 v[68:71], v[244:247], a[180:183], v[68:71]\n"
"ds_read_b64_tr_b16 v[236:237], v252 offset:36864\n"
"s_waitcnt lgkmcnt(11)\n"
"v_mfma_f32_16x16x32_bf16 v[72:75], v[240:243], a[168:171], v[72:75]\n"
"ds_read_b64_tr_b16 v[238:239], v252 offset:38912\n"
"v_mfma_f32_16x16x32_bf16 v[72:75], v[244:247], a[184:187], v[72:75]\n"
"buffer_load_dwordx4 v27, s[24:27], s35 offen offset:3072 sc1 lds\n"
"s_waitcnt lgkmcnt(8)\n"
"v_mfma_f32_16x16x32_bf16 v[76:79], v[240:243], a[172:175], v[76:79]\n"
"v_mfma_f32_16x16x32_bf16 v[76:79], v[244:247], a[188:191], v[76:79]\n"
"s_add_u32 s75, s40, 4718592\n"
"s_waitcnt lgkmcnt(4)\n"
"v_mfma_f32_16x16x32_bf16 v[80:83], v[224:227], a[160:163], v[80:83]\n"
"ds_read_b64_tr_b16 v[240:241], v252 offset:40960\n"
"s_add_u32 m0, s34, 49152\n"
"v_mfma_f32_16x16x32_bf16 v[80:83], v[228:231], a[176:179], v[80:83]\n"
"buffer_load_dwordx4 v20, s[20:23], s75 offen lds\n"
"v_mfma_f32_16x16x32_bf16 v[84:87], v[224:227], a[164:167], v[84:87]\n"
"ds_read_b64_tr_b16 v[242:243], v252 offset:43008\n"
"v_mfma_f32_16x16x32_bf16 v[84:87], v[228:231], a[180:183], v[84:87]\n"
"ds_read_b64_tr_b16 v[244:245], v252 offset:45056\n"
"v_mfma_f32_16x16x32_bf16 v[88:91], v[224:227], a[168:171], v[88:91]\n"
"ds_read_b64_tr_b16 v[246:247], v252 offset:47104\n"
"v_mfma_f32_16x16x32_bf16 v[88:91], v[228:231], a[184:187], v[88:91]\n"
"buffer_load_dwordx4 v21, s[20:23], s75 offen offset:1024 lds\n"
"v_mfma_f32_16x16x32_bf16 v[92:95], v[224:227], a[172:175], v[92:95]\n"
"s_add_u32 s35, s41, 0\n"
"v_mfma_f32_16x16x32_bf16 v[92:95], v[228:231], a[188:191], v[92:95]\n"
"s_waitcnt vmcnt(16) lgkmcnt(4)\n"
"s_barrier\n"
"v_mfma_f32_16x16x32_bf16 v[96:99], v[232:235], a[160:163], v[96:99]\n"
"ds_read_b64_tr_b16 v[224:225], v250 offset:0\n"
"ds_read_b64_tr_b16 v[226:227], v250 offset:2048\n"
"ds_read_b64_tr_b16 v[228:229], v250 offset:4096\n"
"v_mfma_f32_16x16x32_bf16 v[96:99], v[236:239], a[176:179], v[96:99]\n"
"buffer_load_dwordx4 v22, s[20:23], s75 offen offset:2048 lds\n"
"v_mfma_f32_16x16x32_bf16 v[100:103], v[232:235], a[164:167], v[100:103]\n"
"ds_read_b64_tr_b16 v[230:231], v250 offset:6144\n"
"ds_read_b64_tr_b16 a[192:193], v33 offset:49152\n"
"ds_read_b64_tr_b16 a[194:195], v33 offset:51200\n"
"v_mfma_f32_16x16x32_bf16 v[100:103], v[236:239], a[180:183], v[100:103]\n"
"ds_read_b64_tr_b16 a[208:209], v33 offset:53248\n"
"ds_read_b64_tr_b16 a[210:211], v33 offset:55296\n"
"v_mfma_f32_16x16x32_bf16 v[104:107], v[232:235], a[168:171], v[104:107]\n"
"ds_read_b64_tr_b16 a[196:197], v36 offset:49152\n"
"ds_read_b64_tr_b16 a[198:199], v36 offset:51200\n"
"v_mfma_f32_16x16x32_bf16 v[104:107], v[236:239], a[184:187], v[104:107]\n"
"buffer_load_dwordx4 v23, s[20:23], s75 offen offset:3072 lds\n"
"v_mfma_f32_16x16x32_bf16 v[108:111], v[232:235], a[172:175], v[108:111]\n"
"ds_read_b64_tr_b16 a[212:213], v36 offset:53248\n"
"ds_read_b64_tr_b16 a[214:215], v36 offset:55296\n"
"v_mfma_f32_16x16x32_bf16 v[108:111], v[236:239], a[188:191], v[108:111]\n"
"ds_read_b64_tr_b16 a[200:201], v39 offset:49152\n"
"s_add_u32 m0, s34, 65536\n"
"s_waitcnt lgkmcnt(13)\n"
"v_mfma_f32_16x16x32_bf16 v[112:115], v[240:243], a[160:163], v[112:115]\n"
"ds_read_b64_tr_b16 a[202:203], v39 offset:51200\n"
"ds_read_b64_tr_b16 a[216:217], v39 offset:53248\n"
"ds_read_b64_tr_b16 a[218:219], v39 offset:55296\n"
"v_mfma_f32_16x16x32_bf16 v[112:115], v[244:247], a[176:179], v[112:115]\n"
"buffer_load_dwordx4 v28, s[36:39], s35 offen sc1 lds\n"
"v_mfma_f32_16x16x32_bf16 v[116:119], v[240:243], a[164:167], v[116:119]\n"
"ds_read_b64_tr_b16 a[204:205], v42 offset:49152\n"
"ds_read_b64_tr_b16 a[206:207], v42 offset:51200\n"
"v_mfma_f32_16x16x32_bf16 v[116:119], v[244:247], a[180:183], v[116:119]\n"
"ds_read_b64_tr_b16 a[220:221], v42 offset:53248\n"
"ds_read_b64_tr_b16 a[222:223], v42 offset:55296\n"
"v_mfma_f32_16x16x32_bf16 v[120:123], v[240:243], a[168:171], v[120:123]\n"
"ds_read_b64_tr_b16 v[232:233], v250 offset:8192\n"
"ds_read_b64_tr_b16 v[234:235], v250 offset:10240\n"
"v_mfma_f32_16x16x32_bf16 v[120:123], v[244:247], a[184:187], v[120:123]\n"
"buffer_load_dwordx4 v28, s[36:39], s35 offen offset:1024 sc1 lds\n"
"v_mfma_f32_16x16x32_bf16 v[124:127], v[240:243], a[172:175], v[124:127]\n"
"ds_read_b64_tr_b16 v[236:237], v250 offset:12288\n"
"ds_read_b64_tr_b16 v[238:239], v250 offset:14336\n"
"v_mfma_f32_16x16x32_bf16 v[124:127], v[244:247], a[188:191], v[124:127]\n"
"s_waitcnt lgkmcnt(14)\n"
"v_mfma_f32_16x16x32_bf16 v[64:67], v[224:227], a[192:195], v[64:67]\n"
"ds_read_b64_tr_b16 v[240:241], v253 offset:0\n"
"v_mfma_f32_16x16x32_bf16 v[64:67], v[228:231], a[208:211], v[64:67]\n"
"buffer_load_dwordx4 v28, s[36:39], s35 offen offset:2048 sc1 lds\n"
"s_waitcnt lgkmcnt(13)\n"
"v_mfma_f32_16x16x32_bf16 v[68:71], v[224:227], a[196:199], v[68:71]\n"
"ds_read_b64_tr_b16 v[242:243], v253 offset:2048\n"
"v_mfma_f32_16x16x32_bf16 v[68:71], v[228:231], a[212:215], v[68:71]\n"
"ds_read_b64_tr_b16 v[244:245], v253 offset:4096\n"
"s_waitcnt lgkmcnt(11)\n"
"v_mfma_f32_16x16x32_bf16 v[72:75], v[224:227], a[200:203], v[72:75]\n"
"ds_read_b64_tr_b16 v[246:247], v253 offset:6144\n"
"v_mfma_f32_16x16x32_bf16 v[72:75], v[228:231], a[216:219], v[72:75]\n"
"buffer_load_dwordx4 v28, s[36:39], s35 offen offset:3072 sc1 lds\n"
"s_waitcnt lgkmcnt(8)\n"
"v_mfma_f32_16x16x32_bf16 v[76:79], v[224:227], a[204:207], v[76:79]\n"
"v_mfma_f32_16x16x32_bf16 v[76:79], v[228:231], a[220:223], v[76:79]\n"
"s_waitcnt lgkmcnt(4)\n"
"v_mfma_f32_16x16x32_bf16 v[80:83], v[232:235], a[192:195], v[80:83]\n"
"ds_read_b64_tr_b16 v[224:225], v253 offset:8192\n"
"v_mfma_f32_16x16x32_bf16 v[80:83], v[236:239], a[208:211], v[80:83]\n"
"buffer_load_dwordx4 v[160:163], v181, s[36:39], 0 offen sc1\n"
"v_mfma_f32_16x16x32_bf16 v[84:87], v[232:235], a[196:199], v[84:87]\n"
"ds_read_b64_tr_b16 v[226:227], v253 offset:10240\n"
"v_mfma_f32_16x16x32_bf16 v[84:87], v[236:239], a[212:215], v[84:87]\n"
"ds_read_b64_tr_b16 v[228:229], v253 offset:12288\n"
"v_mfma_f32_16x16x32_bf16 v[88:91], v[232:235], a[200:203], v[88:91]\n"
"ds_read_b64_tr_b16 v[230:231], v253 offset:14336\n"
"v_mfma_f32_16x16x32_bf16 v[88:91], v[236:239], a[216:219], v[88:91]\n"
"buffer_load_dwordx4 v[164:167], v181, s[36:39], 0 offen offset:256 sc1\n"
"v_mfma_f32_16x16x32_bf16 v[92:95], v[232:235], a[204:207], v[92:95]\n"
"s_add_u32 m0, s34, 81920\n"
"s_add_u32 s75, s41, 8192\n"
"v_mfma_f32_16x16x32_bf16 v[92:95], v[236:239], a[220:223], v[92:95]\n"
"s_waitcnt vmcnt(16) lgkmcnt(4)\n"
"s_barrier\n"
"v_mfma_f32_16x16x32_bf16 v[96:99], v[240:243], a[192:195], v[96:99]\n"
"ds_read_b64_tr_b16 v[232:233], v248 offset:16384\n"
"ds_read_b64_tr_b16 v[234:235], v248 offset:18432\n"
"ds_read_b64_tr_b16 v[236:237], v248 offset:20480\n"
"v_mfma_f32_16x16x32_bf16 v[96:99], v[244:247], a[208:211], v[96:99]\n"
"buffer_load_dwordx4 v[168:171], v181, s[36:39], 0 offen offset:2048 sc1\n"
"v_mfma_f32_16x16x32_bf16 v[100:103], v[240:243], a[196:199], v[100:103]\n"
"ds_read_b64_tr_b16 v[238:239], v248 offset:22528\n"
"ds_read_b64_tr_b16 a[224:225], v32 offset:0\n"
"ds_read_b64_tr_b16 a[226:227], v32 offset:2048\n"
"v_mfma_f32_16x16x32_bf16 v[100:103], v[244:247], a[212:215], v[100:103]\n"
"ds_read_b64_tr_b16 a[240:241], v32 offset:4096\n"
"ds_read_b64_tr_b16 a[242:243], v32 offset:6144\n"
"v_mfma_f32_16x16x32_bf16 v[104:107], v[240:243], a[200:203], v[104:107]\n"
"ds_read_b64_tr_b16 a[228:229], v35 offset:0\n"
"ds_read_b64_tr_b16 a[230:231], v35 offset:2048\n"
"v_mfma_f32_16x16x32_bf16 v[104:107], v[244:247], a[216:219], v[104:107]\n"
"buffer_load_dwordx4 v[172:175], v181, s[36:39], 0 offen offset:2304 sc1\n"
"v_mfma_f32_16x16x32_bf16 v[108:111], v[240:243], a[204:207], v[108:111]\n"
"ds_read_b64_tr_b16 a[244:245], v35 offset:4096\n"
"ds_read_b64_tr_b16 a[246:247], v35 offset:6144\n"
"v_mfma_f32_16x16x32_bf16 v[108:111], v[244:247], a[220:223], v[108:111]\n"
"ds_read_b64_tr_b16 a[232:233], v38 offset:0\n"
"s_waitcnt lgkmcnt(13)\n"
"v_mfma_f32_16x16x32_bf16 v[112:115], v[224:227], a[192:195], v[112:115]\n"
"ds_read_b64_tr_b16 a[234:235], v38 offset:2048\n"
"ds_read_b64_tr_b16 a[248:249], v38 offset:4096\n"
"ds_read_b64_tr_b16 a[250:251], v38 offset:6144\n"
"v_mfma_f32_16x16x32_bf16 v[112:115], v[228:231], a[208:211], v[112:115]\n"
"buffer_load_dwordx4 v28, s[36:39], s75 offen sc1 lds\n"
"v_mfma_f32_16x16x32_bf16 v[116:119], v[224:227], a[196:199], v[116:119]\n"
"ds_read_b64_tr_b16 a[236:237], v41 offset:0\n"
"ds_read_b64_tr_b16 a[238:239], v41 offset:2048\n"
"v_mfma_f32_16x16x32_bf16 v[116:119], v[228:231], a[212:215], v[116:119]\n"
"ds_read_b64_tr_b16 a[252:253], v41 offset:4096\n"
"ds_read_b64_tr_b16 a[254:255], v41 offset:6144\n"
"v_mfma_f32_16x16x32_bf16 v[120:123], v[224:227], a[200:203], v[120:123]\n"
"ds_read_b64_tr_b16 v[240:241], v248 offset:24576\n"
"ds_read_b64_tr_b16 v[242:243], v248 offset:26624\n"
"v_mfma_f32_16x16x32_bf16 v[120:123], v[228:231], a[216:219], v[120:123]\n"
"buffer_load_dwordx4 v28, s[36:39], s75 offen offset:1024 sc1 lds\n"
"v_mfma_f32_16x16x32_bf16 v[124:127], v[224:227], a[204:207], v[124:127]\n"
"ds_read_b64_tr_b16 v[244:245], v248 offset:28672\n"
"ds_read_b64_tr_b16 v[246:247], v248 offset:30720\n"
"v_mfma_f32_16x16x32_bf16 v[124:127], v[228:231], a[220:223], v[124:127]\n"
"s_waitcnt lgkmcnt(14)\n"
"v_mfma_f32_16x16x32_bf16 v[64:67], v[232:235], a[224:227], v[64:67]\n"
"ds_read_b64_tr_b16 v[224:225], v251 offset:16384\n"
"v_mfma_f32_16x16x32_bf16 v[64:67], v[236:239], a[240:243], v[64:67]\n"
"buffer_load_dwordx4 v28, s[36:39], s75 offen offset:2048 sc1 lds\n"
"s_waitcnt lgkmcnt(13)\n"
"v_mfma_f32_16x16x32_bf16 v[68:71], v[232:235], a[228:231], v[68:71]\n"
"ds_read_b64_tr_b16 v[226:227], v251 offset:18432\n"
"v_mfma_f32_16x16x32_bf16 v[68:71], v[236:239], a[244:247], v[68:71]\n"
"ds_read_b64_tr_b16 v[228:229], v251 offset:20480\n"
"s_waitcnt lgkmcnt(11)\n"
"v_mfma_f32_16x16x32_bf16 v[72:75], v[232:235], a[232:235], v[72:75]\n"
"ds_read_b64_tr_b16 v[230:231], v251 offset:22528\n"
"v_mfma_f32_16x16x32_bf16 v[72:75], v[236:239], a[248:251], v[72:75]\n"
"buffer_load_dwordx4 v28, s[36:39], s75 offen offset:3072 sc1 lds\n"
"s_waitcnt lgkmcnt(8)\n"
"v_mfma_f32_16x16x32_bf16 v[76:79], v[232:235], a[236:239], v[76:79]\n"
"v_mfma_f32_16x16x32_bf16 v[76:79], v[236:239], a[252:255], v[76:79]\n"
"s_add_u32 s35, s41, 16384\n"
"s_waitcnt lgkmcnt(4)\n"
"v_mfma_f32_16x16x32_bf16 v[80:83], v[240:243], a[224:227], v[80:83]\n"
"ds_read_b64_tr_b16 v[232:233], v251 offset:24576\n"
"s_add_u32 m0, s34, 98304\n"
"v_mfma_f32_16x16x32_bf16 v[80:83], v[244:247], a[240:243], v[80:83]\n"
"buffer_load_dwordx4 v28, s[36:39], s35 offen sc1 lds\n"
"v_mfma_f32_16x16x32_bf16 v[84:87], v[240:243], a[228:231], v[84:87]\n"
"ds_read_b64_tr_b16 v[234:235], v251 offset:26624\n"
"v_mfma_f32_16x16x32_bf16 v[84:87], v[244:247], a[244:247], v[84:87]\n"
"ds_read_b64_tr_b16 v[236:237], v251 offset:28672\n"
"v_mfma_f32_16x16x32_bf16 v[88:91], v[240:243], a[232:235], v[88:91]\n"
"ds_read_b64_tr_b16 v[238:239], v251 offset:30720\n"
"v_mfma_f32_16x16x32_bf16 v[88:91], v[244:247], a[248:251], v[88:91]\n"
"buffer_load_dwordx4 v28, s[36:39], s35 offen offset:1024 sc1 lds\n"
"v_mfma_f32_16x16x32_bf16 v[92:95], v[240:243], a[236:239], v[92:95]\n"
"s_add_u32 s75, s41, 24576\n"
"v_mfma_f32_16x16x32_bf16 v[92:95], v[244:247], a[252:255], v[92:95]\n"
"s_waitcnt vmcnt(14) lgkmcnt(4)\n"
"s_barrier\n"
"v_mfma_f32_16x16x32_bf16 v[96:99], v[224:227], a[224:227], v[96:99]\n"
"ds_read_b64_tr_b16 v[240:241], v248 offset:49152\n"
"ds_read_b64_tr_b16 v[242:243], v248 offset:51200\n"
"ds_read_b64_tr_b16 v[244:245], v248 offset:53248\n"
"v_mfma_f32_16x16x32_bf16 v[96:99], v[228:231], a[240:243], v[96:99]\n"
"buffer_load_dwordx4 v28, s[36:39], s35 offen offset:2048 sc1 lds\n"
"v_mfma_f32_16x16x32_bf16 v[100:103], v[224:227], a[228:231], v[100:103]\n"
"ds_read_b64_tr_b16 v[246:247], v248 offset:55296\n"
"ds_read_b64_tr_b16 v[192:193], v32 offset:32768\n"
"ds_read_b64_tr_b16 v[194:195], v32 offset:34816\n"
"v_mfma_f32_16x16x32_bf16 v[100:103], v[228:231], a[244:247], v[100:103]\n"
"ds_read_b64_tr_b16 v[208:209], v32 offset:36864\n"
"ds_read_b64_tr_b16 v[210:211], v32 offset:38912\n"
"v_mfma_f32_16x16x32_bf16 v[104:107], v[224:227], a[232:235], v[104:107]\n"
"ds_read_b64_tr_b16 v[196:197], v35 offset:32768\n"
"ds_read_b64_tr_b16 v[198:199], v35 offset:34816\n"
"v_mfma_f32_16x16x32_bf16 v[104:107], v[228:231], a[248:251], v[104:107]\n"
"buffer_load_dwordx4 v28, s[36:39], s35 offen offset:3072 sc1 lds\n"
"v_mfma_f32_16x16x32_bf16 v[108:111], v[224:227], a[236:239], v[108:111]\n"
"ds_read_b64_tr_b16 v[212:213], v35 offset:36864\n"
"ds_read_b64_tr_b16 v[214:215], v35 offset:38912\n"
"v_mfma_f32_16x16x32_bf16 v[108:111], v[228:231], a[252:255], v[108:111]\n"
"ds_read_b64_tr_b16 v[200:201], v38 offset:32768\n"
"s_add_u32 m0, s34, 114688\n"
"s_waitcnt lgkmcnt(13)\n"
"v_mfma_f32_16x16x32_bf16 v[112:115], v[232:235], a[224:227], v[112:115]\n"
"ds_read_b64_tr_b16 v[202:203], v38 offset:34816\n"
"ds_read_b64_tr_b16 v[216:217], v38 offset:36864\n"
"ds_read_b64_tr_b16 v[218:219], v38 offset:38912\n"
"v_mfma_f32_16x16x32_bf16 v[112:115], v[236:239], a[240:243], v[112:115]\n"
"buffer_load_dwordx4 v28, s[36:39], s75 offen sc1 lds\n"
"v_mfma_f32_16x16x32_bf16 v[116:119], v[232:235], a[228:231], v[116:119]\n"
"ds_read_b64_tr_b16 v[204:205], v41 offset:32768\n"
"ds_read_b64_tr_b16 v[206:207], v41 offset:34816\n"
"v_mfma_f32_16x16x32_bf16 v[116:119], v[236:239], a[244:247], v[116:119]\n"
"ds_read_b64_tr_b16 v[220:221], v41 offset:36864\n"
"ds_read_b64_tr_b16 v[222:223], v41 offset:38912\n"
"v_mfma_f32_16x16x32_bf16 v[120:123], v[232:235], a[232:235], v[120:123]\n"
"ds_read_b64_tr_b16 v[224:225], v248 offset:57344\n"
"ds_read_b64_tr_b16 v[226:227], v248 offset:59392\n"
"v_mfma_f32_16x16x32_bf16 v[120:123], v[236:239], a[248:251], v[120:123]\n"
"buffer_load_dwordx4 v28, s[36:39], s75 offen offset:1024 sc1 lds\n"
"v_mfma_f32_16x16x32_bf16 v[124:127], v[232:235], a[236:239], v[124:127]\n"
"ds_read_b64_tr_b16 v[228:229], v248 offset:61440\n"
"ds_read_b64_tr_b16 v[230:231], v248 offset:63488\n"
"v_mfma_f32_16x16x32_bf16 v[124:127], v[236:239], a[252:255], v[124:127]\n"
"s_waitcnt lgkmcnt(14)\n"
"v_mfma_f32_16x16x32_bf16 v[64:67], v[240:243], v[192:195], v[64:67]\n"
"ds_read_b64_tr_b16 v[232:233], v251 offset:49152\n"
"v_mfma_f32_16x16x32_bf16 v[64:67], v[244:247], v[208:211], v[64:67]\n"
"buffer_load_dwordx4 v28, s[36:39], s75 offen offset:2048 sc1 lds\n"
"s_waitcnt lgkmcnt(13)\n"
"v_mfma_f32_16x16x32_bf16 v[68:71], v[240:243], v[196:199], v[68:71]\n"
"ds_read_b64_tr_b16 v[234:235], v251 offset:51200\n"
"s_waitcnt vmcnt(14)\n"
"v_xor_b32 v177, s86, v161\n"
"v_bitop3_b32 v177, v177, v163, s86 bitop3:0xf6\n"
"v_mfma_f32_16x16x32_bf16 v[68:71], v[244:247], v[212:215], v[68:71]\n"
"ds_read_b64_tr_b16 v[236:237], v251 offset:53248\n"
"s_waitcnt vmcnt(13)\n"
"v_bitop3_b32 v177, v177, v165, s86 bitop3:0xf6\n"
"s_waitcnt lgkmcnt(11)\n"
"v_mfma_f32_16x16x32_bf16 v[72:75], v[240:243], v[200:203], v[72:75]\n"
"ds_read_b64_tr_b16 v[238:239], v251 offset:55296\n"
"v_bitop3_b32 v177, v177, v167, s86 bitop3:0xf6\n"
"v_mfma_f32_16x16x32_bf16 v[72:75], v[244:247], v[216:219], v[72:75]\n"
"buffer_load_dwordx4 v28, s[36:39], s75 offen offset:3072 sc1 lds\n"
"s_waitcnt lgkmcnt(8)\n"
"v_mfma_f32_16x16x32_bf16 v[76:79], v[240:243], v[204:207], v[76:79]\n"
"s_waitcnt vmcnt(13)\n"
"v_bitop3_b32 v177, v177, v169, s86 bitop3:0xf6\n"
"v_mfma_f32_16x16x32_bf16 v[76:79], v[244:247], v[220:223], v[76:79]\n"
"s_add_u32 s35, s48, 0\n"
"v_bitop3_b32 v177, v177, v171, s86 bitop3:0xf6\n"
"s_waitcnt lgkmcnt(4)\n"
"v_mfma_f32_16x16x32_bf16 v[80:83], v[224:227], v[192:195], v[80:83]\n"
"ds_read_b64_tr_b16 v[240:241], v251 offset:57344\n"
"s_add_u32 m0, s47, 131072\n"
"s_waitcnt vmcnt(12)\n"
"v_bitop3_b32 v177, v177, v173, s86 bitop3:0xf6\n"
"v_mfma_f32_16x16x32_bf16 v[80:83], v[228:231], v[208:211], v[80:83]\n"
"buffer_load_dwordx4 v28, s[36:39], s35 offen sc1 lds\n"
"v_mfma_f32_16x16x32_bf16 v[84:87], v[224:227], v[196:199], v[84:87]\n"
"ds_read_b64_tr_b16 v[242:243], v251 offset:59392\n"
"v_bitop3_b32 v177, v177, v175, s86 bitop3:0xf6\n"
"v_mfma_f32_16x16x32_bf16 v[84:87], v[228:231], v[212:215], v[84:87]\n"
"ds_read_b64_tr_b16 v[244:245], v251 offset:61440\n"
"v_cmp_ne_u32 vcc, 0, v177\n"
"v_mfma_f32_16x16x32_bf16 v[88:91], v[224:227], v[200:203], v[88:91]\n"
"ds_read_b64_tr_b16 v[246:247], v251 offset:63488\n"
"s_cbranch_vccnz SLOWS_%=\n"
"BACKS_%=:\n"
"v_mfma_f32_16x16x32_bf16 v[88:91], v[228:231], v[216:219], v[88:91]\n"
"buffer_load_dwordx4 v28, s[36:39], s35 offen offset:1024 sc1 lds\n"
"v_mfma_f32_16x16x32_bf16 v[92:95], v[224:227], v[204:207], v[92:95]\n"
"s_add_u32 s75, s40, 128\n"
"v_permlane32_swap_b32 v160, v162\n"
"v_permlane32_swap_b32 v164, v166\n"
"v_mfma_f32_16x16x32_bf16 v[92:95], v[228:231], v[220:223], v[92:95]\n"
"s_waitcnt lgkmcnt(4)\n"
"s_barrier\n"
"v_permlane32_swap_b32 v168, v170\n"
"v_mfma_f32_16x16x32_bf16 v[96:99], v[232:235], v[192:195], v[96:99]\n"
"ds_read_b64_tr_b16 v[224:225], v45 offset:0\n"
"s_add_u32 m0, s34, 0\n"
"v_permlane32_swap_b32 v172, v174\n"
"v_mfma_f32_16x16x32_bf16 v[96:99], v[236:239], v[208:211], v[96:99]\n"
"buffer_load_dwordx4 v20, s[20:23], s75 offen lds\n"
"v_mfma_f32_16x16x32_bf16 v[100:103], v[232:235], v[196:199], v[100:103]\n"
"ds_read_b64_tr_b16 v[226:227], v45 offset:1024\n"
"v_add_f32 v160, v160, v162\n"
"v_add_f32 v164, v164, v166\n"
"v_mfma_f32_16x16x32_bf16 v[100:103], v[236:239], v[212:215], v[100:103]\n"
"ds_read_b64_tr_b16 v[228:229], v45 offset:2048\n"
"v_add_f32 v168, v168, v170\n"
"v_add_f32 v172, v172, v174\n"
"v_mfma_f32_16x16x32_bf16 v[104:107], v[232:235], v[200:203], v[104:107]\n"
"ds_read_b64_tr_b16 v[230:231], v45 offset:3072\n"
"v_permlane16_swap_b32 v160, v164\n"
"v_permlane16_swap_b32 v168, v172\n"
"v_mfma_f32_16x16x32_bf16 v[104:107], v[236:239], v[216:219], v[104:107]\n"
"buffer_load_dwordx4 v21, s[20:23], s75 offen offset:1024 lds\n"
"v_mfma_f32_16x16x32_bf16 v[108:111], v[232:235], v[204:207], v[108:111]\n"
"v_add_f32 v160, v160, v164\n"
"v_add_f32 v168, v168, v172\n"
"v_mov_b32 v161, 0\n"
"v_mfma_f32_16x16x32_bf16 v[108:111], v[236:239], v[220:223], v[108:111]\n"
"v_permlane32_swap_b32 v160, v168\n"
"v_mul_f32 v160, 0x3ae38e39, v160\n"
"s_waitcnt lgkmcnt(4)\n"
"v_mfma_f32_16x16x32_bf16 v[112:115], v[240:243], v[192:195], v[112:115]\n"
"ds_read_b64_tr_b16 v[232:233], v45 offset:4096\n"
"v_mul_f32 v168, 0x3ae38e39, v168\n"
"v_fma_f32 v168, -v160, v160, v168\n"
"v_mfma_f32_16x16x32_bf16 v[112:115], v[244:247], v[208:211], v[112:115]\n"
"buffer_load_dwordx4 v22, s[20:23], s75 offen offset:2048 lds\n"
"v_mfma_f32_16x16x32_bf16 v[116:119], v[240:243], v[196:199], v[116:119]\n"
"ds_read_b64_tr_b16 v[234:235], v45 offset:5120\n"
"v_max_f32 v168, 0, v168\n"
"v_cvt_pk_bf16_f32 v168, v161, v168\n"
"v_mfma_f32_16x16x32_bf16 v[116:119], v[244:247], v[212:215], v[116:119]\n"
"ds_read_b64_tr_b16 v[236:237], v45 offset:6144\n"
"v_add_f32 v168, 0x3727c5ac, v168\n"
"v_cvt_pk_bf16_f32 v168, v161, v168\n"
"v_mfma_f32_16x16x32_bf16 v[120:123], v[240:243], v[200:203], v[120:123]\n"
"ds_read_b64_tr_b16 v[238:239], v45 offset:7168\n"
"s_add_u32 s35, s40, 589952\n"
"v_sqrt_f32 v168, v168\n"
"v_mfma_f32_16x16x32_bf16 v[120:123], v[244:247], v[216:219], v[120:123]\n"
"buffer_load_dwordx4 v23, s[20:23], s75 offen offset:3072 lds\n"
"v_mfma_f32_16x16x32_bf16 v[124:127], v[240:243], v[204:207], v[124:127]\n"
"ds_read_b64 v[48:49], v47 offset:0\n"
"ds_read_b64 v[50:51], v47 offset:2048\n"
"v_cvt_pk_bf16_f32 v56, -v160, -v160\n"
"v_mfma_f32_16x16x32_bf16 v[124:127], v[244:247], v[220:223], v[124:127]\n"
"ds_read_b64 v[52:53], v47 offset:128\n"
"v_cvt_pk_bf16_f32 v57, v168, v168\n"
"s_waitcnt lgkmcnt(7)\n"
"v_mfma_f32_16x16x32_bf16 v[128:131], v[224:227], a[0:3], 0\n"
"ds_read_b64_tr_b16 v[240:241], v45 offset:8192\n"
"s_add_u32 m0, s34, 16384\n"
"v_lshlrev_b32 v168, 16, v57\n"
"v_mfma_f32_16x16x32_bf16 v[128:131], v[228:231], a[16:19], v[128:131]\n"
"buffer_load_dwordx4 v20, s[20:23], s35 offen lds\n"
"v_mfma_f32_16x16x32_bf16 v[132:135], v[224:227], a[4:7], 0\n"
"ds_read_b64_tr_b16 v[242:243], v45 offset:9216\n"
"ds_read_b64 v[54:55], v47 offset:2176\n"
"v_rcp_f32 v168, v168\n"
"v_mfma_f32_16x16x32_bf16 v[132:135], v[228:231], a[20:23], v[132:135]\n"
"ds_read_b64_tr_b16 v[244:245], v45 offset:10240\n"
"v_mul_f32 v168, v168, v168\n"
"ds_write_b32 v183, v168\n"
"v_mfma_f32_16x16x32_bf16 v[136:139], v[224:227], a[8:11], 0\n"
"ds_read_b64_tr_b16 v[246:247], v45 offset:11264\n"
"v_mfma_f32_16x16x32_bf16 v[136:139], v[228:231], a[24:27], v[136:139]\n"
"buffer_load_dwordx4 v21, s[20:23], s35 offen offset:1024 lds\n"
"v_mfma_f32_16x16x32_bf16 v[140:143], v[224:227], a[12:15], 0\n"
"v_mfma_f32_16x16x32_bf16 v[140:143], v[228:231], a[28:31], v[140:143]\n"
"s_waitcnt lgkmcnt(3)\n"
"v_mfma_f32_16x16x4_4b_bf16 v[64:79], v[48:49], v[56:57], v[64:79]\n"
"buffer_load_dwordx4 v22, s[20:23], s35 offen offset:2048 lds\n"
"v_mfma_f32_16x16x4_4b_bf16 v[80:95], v[50:51], v[56:57], v[80:95]\n"
"v_mfma_f32_16x16x4_4b_bf16 v[96:111], v[52:53], v[56:57], v[96:111]\n"
"buffer_load_dwordx4 v23, s[20:23], s35 offen offset:3072 lds\n"
"v_mfma_f32_16x16x4_4b_bf16 v[112:127], v[54:55], v[56:57], v[112:127]\n"
"s_add_u32 s75, s40, 1179776\n"
"v_mfma_f32_16x16x32_bf16 v[128:131], v[232:235], a[32:35], v[128:131]\n"
"ds_read_b64_tr_b16 v[224:225], v45 offset:12288\n"
"s_add_u32 m0, s34, 32768\n"
"v_mfma_f32_16x16x32_bf16 v[128:131], v[236:239], a[48:51], v[128:131]\n"
"buffer_load_dwordx4 v20, s[20:23], s75 offen lds\n"
"v_mfma_f32_16x16x32_bf16 v[132:135], v[232:235], a[36:39], v[132:135]\n"
"ds_read_b64_tr_b16 v[226:227], v45 offset:13312\n"
"v_mfma_f32_16x16x32_bf16 v[132:135], v[236:239], a[52:55], v[132:135]\n"
"ds_read_b64_tr_b16 v[228:229], v45 offset:14336\n"
"v_mfma_f32_16x16x32_bf16 v[136:139], v[232:235], a[40:43], v[136:139]\n"
"ds_read_b64_tr_b16 v[230:231], v45 offset:15360\n"
"v_max_f32 v64, 0, v64\n"
"v_mfma_f32_16x16x32_bf16 v[136:139], v[236:239], a[56:59], v[136:139]\n"
"buffer_load_dwordx4 v21, s[20:23], s75 offen offset:1024 lds\n"
"v_mfma_f32_16x16x32_bf16 v[140:143], v[232:235], a[44:47], v[140:143]\n"
"v_max_f32 v65, 0, v65\n"
"v_max_f32 v66, 0, v66\n"
"v_mfma_f32_16x16x32_bf16 v[140:143], v[236:239], a[60:63], v[140:143]\n"
"s_waitcnt vmcnt(20) lgkmcnt(4)\n"
"s_barrier\n"
"v_max_f32 v67, 0, v67\n"
"v_mfma_f32_16x16x32_bf16 v[144:147], v[240:243], a[0:3], 0\n"
"ds_read_b64_tr_b16 v[232:233], v45 offset:16384\n"
"v_mul_f32 v64, v64, v80\n"
"v_mfma_f32_16x16x32_bf16 v[144:147], v[244:247], a[16:19], v[144:147]\n"
"buffer_load_dwordx4 v22, s[20:23], s75 offen offset:2048 lds\n"
"v_mfma_f32_16x16x32_bf16 v[148:151], v[240:243], a[4:7], 0\n"
"ds_read_b64_tr_b16 v[234:235], v45 offset:17408\n"
"v_mul_f32 v65, v65, v81\n"
"v_mul_f32 v66, v66, v82\n"
"v_mfma_f32_16x16x32_bf16 v[148:151], v[244:247], a[20:23], v[148:151]\n"
"ds_read_b64_tr_b16 v[236:237], v45 offset:18432\n"
"v_mul_f32 v67, v67, v83\n"
"v_mfma_f32_16x16x32_bf16 v[152:155], v[240:243], a[8:11], 0\n"
"ds_read_b64_tr_b16 v[238:239], v45 offset:19456\n"
"v_cvt_pk_bf16_f32 v64, v64, v65\n"
"v_mfma_f32_16x16x32_bf16 v[152:155], v[244:247], a[24:27], v[152:155]\n"
"buffer_load_dwordx4 v23, s[20:23], s75 offen offset:3072 lds\n"
"v_mfma_f32_16x16x32_bf16 v[156:159], v[240:243], a[12:15], 0\n"
"v_cvt_pk_bf16_f32 v65, v66, v67\n"
"v_max_f32 v96, 0, v96\n"
"v_mfma_f32_16x16x32_bf16 v[156:159], v[244:247], a[28:31], v[156:159]\n"
"s_add_u32 s35, s40, 1769600\n"
"v_max_f32 v97, 0, v97\n"
"s_waitcnt lgkmcnt(4)\n"
"v_mfma_f32_16x16x32_bf16 v[144:147], v[224:227], a[32:35], v[144:147]\n"
"ds_read_b64_tr_b16 v[240:241], v45 offset:20480\n"
"s_add_u32 m0, s34, 49152\n"
"v_max_f32 v98, 0, v98\n"
"v_mfma_f32_16x16x32_bf16 v[144:147], v[228:231], a[48:51], v[144:147]\n"
"buffer_load_dwordx4 v20, s[20:23], s35 offen lds\n"
"v_mfma_f32_16x16x32_bf16 v[148:151], v[224:227], a[36:39], v[148:151]\n"
"ds_read_b64_tr_b16 v[242:243], v45 offset:21504\n"
"v_max_f32 v99, 0, v99\n"
"v_mul_f32 v96, v96, v112\n"
"v_mfma_f32_16x16x32_bf16 v[148:151], v[228:231], a[52:55], v[148:151]\n"
"ds_read_b64_tr_b16 v[244:245], v45 offset:22528\n"
"v_mul_f32 v97, v97, v113\n"
"v_mfma_f32_16x16x32_bf16 v[152:155], v[224:227], a[40:43], v[152:155]\n"
"ds_read_b64_tr_b16 v[246:247], v45 offset:23552\n"
"v_mul_f32 v98, v98, v114\n"
"v_mfma_f32_16x16x32_bf16 v[152:155], v[228:231], a[56:59], v[152:155]\n"
"buffer_load_dwordx4 v21, s[20:23], s35 offen offset:1024 lds\n"
"v_mfma_f32_16x16x32_bf16 v[156:159], v[224:227], a[44:47], v[156:159]\n"
"v_mul_f32 v99, v99, v115\n"
"v_cvt_pk_bf16_f32 v66, v96, v97\n"
"v_mfma_f32_16x16x32_bf16 v[156:159], v[228:231], a[60:63], v[156:159]\n"
"v_cvt_pk_bf16_f32 v67, v98, v99\n"
"s_waitcnt lgkmcnt(4)\n"
"v_mfma_f32_16x16x32_bf16 v[128:131], v[232:235], a[64:67], v[128:131]\n"
"ds_read_b64_tr_b16 v[224:225], v45 offset:24576\n"
"v_mfma_f32_16x16x32_bf16 v[128:131], v[236:239], a[80:83], v[128:131]\n"
"buffer_store_dwordx4 v[64:67], v8, s[36:39], s49 offen sc1\n"
"v_mfma_f32_16x16x32_bf16 v[132:135], v[232:235], a[68:71], v[132:135]\n"
"ds_read_b64_tr_b16 v[226:227], v45 offset:25600\n"
"v_max_f32 v68, 0, v68\n"
"v_mfma_f32_16x16x32_bf16 v[132:135], v[236:239], a[84:87], v[132:135]\n"
"ds_read_b64_tr_b16 v[228:229], v45 offset:26624\n"
"v_max_f32 v69, 0, v69\n"
"v_mfma_f32_16x16x32_bf16 v[136:139], v[232:235], a[72:75], v[136:139]\n"
"ds_read_b64_tr_b16 v[230:231], v45 offset:27648\n"
"v_max_f32 v70, 0, v70\n"
"v_mfma_f32_16x16x32_bf16 v[136:139], v[236:239], a[88:91], v[136:139]\n"
"buffer_load_dwordx4 v22, s[20:23], s35 offen offset:2048 lds\n"
"v_mfma_f32_16x16x32_bf16 v[140:143], v[232:235], a[76:79], v[140:143]\n"
"v_max_f32 v71, 0, v71\n"
"v_mul_f32 v68, v68, v84\n"
"v_mfma_f32_16x16x32_bf16 v[140:143], v[236:239], a[92:95], v[140:143]\n"
"v_mul_f32 v69, v69, v85\n"
"s_waitcnt lgkmcnt(4)\n"
"v_mfma_f32_16x16x32_bf16 v[128:131], v[240:243], a[96:99], v[128:131]\n"
"ds_read_b64_tr_b16 v[232:233], v45 offset:28672\n"
"v_mul_f32 v70, v70, v86\n"
"v_mfma_f32_16x16x32_bf16 v[128:131], v[244:247], a[112:115], v[128:131]\n"
"buffer_load_dwordx4 v23, s[20:23], s35 offen offset:3072 lds\n"
"v_mfma_f32_16x16x32_bf16 v[132:135], v[240:243], a[100:103], v[132:135]\n"
"ds_read_b64_tr_b16 v[234:235], v45 offset:29696\n"
"v_mul_f32 v71, v71, v87\n"
"v_mfma_f32_16x16x32_bf16 v[132:135], v[244:247], a[116:119], v[132:135]\n"
"ds_read_b64_tr_b16 v[236:237], v45 offset:30720\n"
"v_cvt_pk_bf16_f32 v68, v68, v69\n"
"v_mfma_f32_16x16x32_bf16 v[136:139], v[240:243], a[104:107], v[136:139]\n"
"ds_read_b64_tr_b16 v[238:239], v45 offset:31744\n"
"v_cvt_pk_bf16_f32 v69, v70, v71\n"
"v_mfma_f32_16x16x32_bf16 v[136:139], v[244:247], a[120:123], v[136:139]\n"
"v_max_f32 v100, 0, v100\n"
"v_max_f32 v101, 0, v101\n"
"v_mfma_f32_16x16x32_bf16 v[140:143], v[240:243], a[108:111], v[140:143]\n"
"s_add_u32 s75, s40, 2359424\n"
"v_max_f32 v102, 0, v102\n"
"v_mfma_f32_16x16x32_bf16 v[140:143], v[244:247], a[124:127], v[140:143]\n"
"s_waitcnt vmcnt(23) lgkmcnt(4)\n"
"s_barrier\n"
"v_mfma_f32_16x16x32_bf16 v[144:147], v[224:227], a[64:67], v[144:147]\n"
"ds_read_b64_tr_b16 v[240:241], v45 offset:32768\n"
"s_add_u32 m0, s34, 65536\n"
"v_mfma_f32_16x16x32_bf16 v[144:147], v[228:231], a[80:83], v[144:147]\n"
"buffer_load_dwordx4 v20, s[20:23], s75 offen lds\n"
"v_mfma_f32_16x16x32_bf16 v[148:151], v[224:227], a[68:71], v[148:151]\n"
"ds_read_b64_tr_b16 v[242:243], v45 offset:33792\n"
"v_max_f32 v103, 0, v103\n"
"v_mfma_f32_16x16x32_bf16 v[148:151], v[228:231], a[84:87], v[148:151]\n"
"ds_read_b64_tr_b16 v[244:245], v45 offset:34816\n"
"v_mul_f32 v100, v100, v116\n"
"v_mfma_f32_16x16x32_bf16 v[152:155], v[224:227], a[72:75], v[152:155]\n"
"ds_read_b64_tr_b16 v[246:247], v45 offset:35840\n"
"v_mul_f32 v101, v101, v117\n"
"v_mfma_f32_16x16x32_bf16 v[152:155], v[228:231], a[88:91], v[152:155]\n"
"buffer_load_dwordx4 v21, s[20:23], s75 offen offset:1024 lds\n"
"v_mfma_f32_16x16x32_bf16 v[156:159], v[224:227], a[76:79], v[156:159]\n"
"v_mul_f32 v102, v102, v118\n"
"v_mul_f32 v103, v103, v119\n"
"v_mfma_f32_16x16x32_bf16 v[156:159], v[228:231], a[92:95], v[156:159]\n"
"v_cvt_pk_bf16_f32 v70, v100, v101\n"
"s_waitcnt lgkmcnt(4)\n"
"v_mfma_f32_16x16x32_bf16 v[144:147], v[232:235], a[96:99], v[144:147]\n"
"ds_read_b64_tr_b16 v[224:225], v45 offset:36864\n"
"v_cvt_pk_bf16_f32 v71, v102, v103\n"
"v_mfma_f32_16x16x32_bf16 v[144:147], v[236:239], a[112:115], v[144:147]\n"
"buffer_store_dwordx4 v[68:71], v8, s[36:39], s49 offen offset:1024 sc1\n"
"v_mfma_f32_16x16x32_bf16 v[148:151], v[232:235], a[100:103], v[148:151]\n"
"ds_read_b64_tr_b16 v[226:227], v45 offset:37888\n"
"v_max_f32 v72, 0, v72\n"
"v_mfma_f32_16x16x32_bf16 v[148:151], v[236:239], a[116:119], v[148:151]\n"
"ds_read_b64_tr_b16 v[228:229], v45 offset:38912\n"
"v_max_f32 v73, 0, v73\n"
"v_mfma_f32_16x16x32_bf16 v[152:155], v[232:235], a[104:107], v[152:155]\n"
"ds_read_b64_tr_b16 v[230:231], v45 offset:39936\n"
"v_max_f32 v74, 0, v74\n"
"v_mfma_f32_16x16x32_bf16 v[152:155], v[236:239], a[120:123], v[152:155]\n"
"v_max_f32 v75, 0, v75\n"
"v_mul_f32 v72, v72, v88\n"
"v_mfma_f32_16x16x32_bf16 v[156:159], v[232:235], a[108:111], v[156:159]\n"
"v_mul_f32 v73, v73, v89\n"
"v_mfma_f32_16x16x32_bf16 v[156:159], v[236:239], a[124:127], v[156:159]\n"
"v_mul_f32 v74, v74, v90\n"
"s_waitcnt lgkmcnt(4)\n"
"v_mfma_f32_16x16x32_bf16 v[128:131], v[240:243], a[128:131], v[128:131]\n"
"ds_read_b64_tr_b16 v[232:233], v45 offset:40960\n"
"v_mul_f32 v75, v75, v91\n"
"v_mfma_f32_16x16x32_bf16 v[128:131], v[244:247], a[144:147], v[128:131]\n"
"buffer_load_dwordx4 v22, s[20:23], s75 offen offset:2048 lds\n"
"v_mfma_f32_16x16x32_bf16 v[132:135], v[240:243], a[132:135], v[132:135]\n"
"ds_read_b64_tr_b16 v[234:235], v45 offset:41984\n"
"v_cvt_pk_bf16_f32 v72, v72, v73\n"
"v_mfma_f32_16x16x32_bf16 v[132:135], v[244:247], a[148:151], v[132:135]\n"
"ds_read_b64_tr_b16 v[236:237], v45 offset:43008\n"
"v_cvt_pk_bf16_f32 v73, v74, v75\n"
"v_mfma_f32_16x16x32_bf16 v[136:139], v[240:243], a[136:139], v[136:139]\n"
"ds_read_b64_tr_b16 v[238:239], v45 offset:44032\n"
"v_max_f32 v104, 0, v104\n"
"v_mfma_f32_16x16x32_bf16 v[136:139], v[244:247], a[152:155], v[136:139]\n"
"buffer_load_dwordx4 v23, s[20:23], s75 offen offset:3072 lds\n"
"v_mfma_f32_16x16x32_bf16 v[140:143], v[240:243], a[140:143], v[140:143]\n"
"v_max_f32 v105, 0, v105\n"
"v_max_f32 v106, 0, v106\n"
"v_mfma_f32_16x16x32_bf16 v[140:143], v[244:247], a[156:159], v[140:143]\n"
"v_max_f32 v107, 0, v107\n"
"s_waitcnt lgkmcnt(4)\n"
"v_mfma_f32_16x16x32_bf16 v[128:131], v[224:227], a[160:163], v[128:131]\n"
"ds_read_b64_tr_b16 v[240:241], v45 offset:45056\n"
"v_mul_f32 v104, v104, v120\n"
"v_mfma_f32_16x16x32_bf16 v[128:131], v[228:231], a[176:179], v[128:131]\n"
"ds_read_b64_tr_b16 v[242:243], v45 offset:46080\n"
"v_mul_f32 v105, v105, v121\n"
"v_mfma_f32_16x16x32_bf16 v[132:135], v[224:227], a[164:167], v[132:135]\n"
"ds_read_b64_tr_b16 v[244:245], v45 offset:47104\n"
"v_mul_f32 v106, v106, v122\n"
"v_mfma_f32_16x16x32_bf16 v[132:135], v[228:231], a[180:183], v[132:135]\n"
"ds_read_b64_tr_b16 v[246:247], v45 offset:48128\n"
"v_mul_f32 v107, v107, v123\n"
"v_mfma_f32_16x16x32_bf16 v[136:139], v[224:227], a[168:171], v[136:139]\n"
"v_cvt_pk_bf16_f32 v74, v104, v105\n"
"v_cvt_pk_bf16_f32 v75, v106, v107\n"
"v_mfma_f32_16x16x32_bf16 v[136:139], v[228:231], a[184:187], v[136:139]\n"
"buffer_store_dwordx4 v[72:75], v8, s[36:39], s49 offen offset:2048 sc1\n"
"v_mfma_f32_16x16x32_bf16 v[140:143], v[224:227], a[172:175], v[140:143]\n"
"s_add_u32 s35, s40, 2949248\n"
"v_max_f32 v76, 0, v76\n"
"v_mfma_f32_16x16x32_bf16 v[140:143], v[228:231], a[188:191], v[140:143]\n"
"s_waitcnt vmcnt(25) lgkmcnt(4)\n"
"s_barrier\n"
"v_mfma_f32_16x16x32_bf16 v[144:147], v[232:235], a[128:131], v[144:147]\n"
"ds_read_b64_tr_b16 v[224:225], v45 offset:49152\n"
"s_add_u32 m0, s34, 81920\n"
"v_mfma_f32_16x16x32_bf16 v[144:147], v[236:239], a[144:147], v[144:147]\n"
"buffer_load_dwordx4 v20, s[20:23], s35 offen lds\n"
"v_mfma_f32_16x16x32_bf16 v[148:151], v[232:235], a[132:135], v[148:151]\n"
"ds_read_b64_tr_b16 v[226:227], v45 offset:50176\n"
"v_max_f32 v77, 0, v77\n"
"v_mfma_f32_16x16x32_bf16 v[148:151], v[236:239], a[148:151], v[148:151]\n"
"ds_read_b64_tr_b16 v[228:229], v45 offset:51200\n"
"v_max_f32 v78, 0, v78\n"
"v_mfma_f32_16x16x32_bf16 v[152:155], v[232:235], a[136:139], v[152:155]\n"
"ds_read_b64_tr_b16 v[230:231], v45 offset:52224\n"
"v_max_f32 v79, 0, v79\n"
"v_mfma_f32_16x16x32_bf16 v[152:155], v[236:239], a[152:155], v[152:155]\n"
"buffer_load_dwordx4 v21, s[20:23], s35 offen offset:1024 lds\n"
"v_mfma_f32_16x16x32_bf16 v[156:159], v[232:235], a[140:143], v[156:159]\n"
"v_mul_f32 v76, v76, v92\n"
"v_mul_f32 v77, v77, v93\n"
"v_mfma_f32_16x16x32_bf16 v[156:159], v[236:239], a[156:159], v[156:159]\n"
"v_mul_f32 v78, v78, v94\n"
"s_waitcnt lgkmcnt(4)\n"
"v_mfma_f32_16x16x32_bf16 v[144:147], v[240:243], a[160:163], v[144:147]\n"
"ds_read_b64_tr_b16 v[232:233], v45 offset:53248\n"
"v_mul_f32 v79, v79, v95\n"
"v_mfma_f32_16x16x32_bf16 v[144:147], v[244:247], a[176:179], v[144:147]\n"
"ds_read_b64_tr_b16 v[234:235], v45 offset:54272\n"
"v_cvt_pk_bf16_f32 v76, v76, v77\n"
"v_mfma_f32_16x16x32_bf16 v[148:151], v[240:243], a[164:167], v[148:151]\n"
"ds_read_b64_tr_b16 v[236:237], v45 offset:55296\n"
"v_cvt_pk_bf16_f32 v77, v78, v79\n"
"v_mfma_f32_16x16x32_bf16 v[148:151], v[244:247], a[180:183], v[148:151]\n"
"ds_read_b64_tr_b16 v[238:239], v45 offset:56320\n"
"v_max_f32 v108, 0, v108\n"
"v_mfma_f32_16x16x32_bf16 v[152:155], v[240:243], a[168:171], v[152:155]\n"
"v_max_f32 v109, 0, v109\n"
"v_max_f32 v110, 0, v110\n"
"v_mfma_f32_16x16x32_bf16 v[152:155], v[244:247], a[184:187], v[152:155]\n"
"buffer_load_dwordx4 v22, s[20:23], s35 offen offset:2048 lds\n"
"v_mfma_f32_16x16x32_bf16 v[156:159], v[240:243], a[172:175], v[156:159]\n"
"v_max_f32 v111, 0, v111\n"
"v_mul_f32 v108, v108, v124\n"
"v_mfma_f32_16x16x32_bf16 v[156:159], v[244:247], a[188:191], v[156:159]\n"
"v_mul_f32 v109, v109, v125\n"
"s_waitcnt lgkmcnt(4)\n"
"v_mfma_f32_16x16x32_bf16 v[128:131], v[224:227], a[192:195], v[128:131]\n"
"ds_read_b64_tr_b16 v[240:241], v45 offset:57344\n"
"v_mul_f32 v110, v110, v126\n"
"v_mfma_f32_16x16x32_bf16 v[128:131], v[228:231], a[208:211], v[128:131]\n"
"ds_read_b64_tr_b16 v[242:243], v45 offset:58368\n"
"v_mul_f32 v111, v111, v127\n"
"v_mfma_f32_16x16x32_bf16 v[132:135], v[224:227], a[196:199], v[132:135]\n"
"ds_read_b64_tr_b16 v[244:245], v45 offset:59392\n"
"v_cvt_pk_bf16_f32 v78, v108, v109\n"
"v_mfma_f32_16x16x32_bf16 v[132:135], v[228:231], a[212:215], v[132:135]\n"
"ds_read_b64_tr_b16 v[246:247], v45 offset:60416\n"
"v_cvt_pk_bf16_f32 v79, v110, v111\n"
"v_mfma_f32_16x16x32_bf16 v[136:139], v[224:227], a[200:203], v[136:139]\n"
"v_mfma_f32_16x16x32_bf16 v[136:139], v[228:231], a[216:219], v[136:139]\n"
"buffer_store_dwordx4 v[76:79], v8, s[36:39], s49 offen offset:3072 sc1\n"
"v_mfma_f32_16x16x32_bf16 v[140:143], v[224:227], a[204:207], v[140:143]\n"
"s_add_u32 s49, s49, 16384\n"
"v_mfma_f32_16x16x32_bf16 v[140:143], v[228:231], a[220:223], v[140:143]\n"
"s_waitcnt lgkmcnt(4)\n"
"v_mfma_f32_16x16x32_bf16 v[128:131], v[232:235], a[224:227], v[128:131]\n"
"ds_read_b64_tr_b16 v[224:225], v45 offset:61440\n"
"v_mfma_f32_16x16x32_bf16 v[128:131], v[236:239], a[240:243], v[128:131]\n"
"buffer_load_dwordx4 v23, s[20:23], s35 offen offset:3072 lds\n"
"v_mfma_f32_16x16x32_bf16 v[132:135], v[232:235], a[228:231], v[132:135]\n"
"ds_read_b64_tr_b16 v[226:227], v45 offset:62464\n"
"v_mfma_f32_16x16x32_bf16 v[132:135], v[236:239], a[244:247], v[132:135]\n"
"ds_read_b64_tr_b16 v[228:229], v45 offset:63488\n"
"v_mfma_f32_16x16x32_bf16 v[136:139], v[232:235], a[232:235], v[136:139]\n"
"ds_read_b64_tr_b16 v[230:231], v45 offset:64512\n"
"v_mfma_f32_16x16x32_bf16 v[136:139], v[236:239], a[248:251], v[136:139]\n"
"v_mfma_f32_16x16x32_bf16 v[140:143], v[232:235], a[236:239], v[140:143]\n"
"s_add_u32 s75, s40, 3539072\n"
"v_mfma_f32_16x16x32_bf16 v[140:143], v[236:239], a[252:255], v[140:143]\n"
"s_waitcnt vmcnt(28) lgkmcnt(4)\n"
"s_barrier\n"
"v_mfma_f32_16x16x32_bf16 v[144:147], v[240:243], a[192:195], v[144:147]\n"
"ds_read_b64_tr_b16 v[232:233], v46 offset:0\n"
"s_add_u32 m0, s34, 98304\n"
"v_mfma_f32_16x16x32_bf16 v[144:147], v[244:247], a[208:211], v[144:147]\n"
"buffer_load_dwordx4 v20, s[20:23], s75 offen lds\n"
"v_mfma_f32_16x16x32_bf16 v[148:151], v[240:243], a[196:199], v[148:151]\n"
"ds_read_b64_tr_b16 v[234:235], v46 offset:1024\n"
"v_mfma_f32_16x16x32_bf16 v[148:151], v[244:247], a[212:215], v[148:151]\n"
"ds_read_b64_tr_b16 v[236:237], v46 offset:2048\n"
"v_mfma_f32_16x16x32_bf16 v[152:155], v[240:243], a[200:203], v[152:155]\n"
"ds_read_b64_tr_b16 v[238:239], v46 offset:3072\n"
"ds_read_b64 v[48:49], v47 offset:512\n"
"v_mfma_f32_16x16x32_bf16 v[152:155], v[244:247], a[216:219], v[152:155]\n"
"buffer_load_dwordx4 v21, s[20:23], s75 offen offset:1024 lds\n"
"v_mfma_f32_16x16x32_bf16 v[156:159], v[240:243], a[204:207], v[156:159]\n"
"ds_read_b64 v[50:51], v47 offset:2560\n"
"ds_read_b64 v[52:53], v47 offset:640\n"
"v_mfma_f32_16x16x32_bf16 v[156:159], v[244:247], a[220:223], v[156:159]\n"
"ds_read_b64 v[54:55], v47 offset:2688\n"
"s_waitcnt lgkmcnt(8)\n"
"v_mfma_f32_16x16x32_bf16 v[144:147], v[224:227], a[224:227], v[144:147]\n"
"ds_read_b64_tr_b16 v[240:241], v46 offset:8192\n"
"v_mfma_f32_16x16x32_bf16 v[144:147], v[228:231], a[240:243], v[144:147]\n"
"ds_read_b64_tr_b16 v[242:243], v46 offset:9216\n"
"v_mfma_f32_16x16x32_bf16 v[148:151], v[224:227], a[228:231], v[148:151]\n"
"ds_read_b64_tr_b16 v[244:245], v46 offset:10240\n"
"v_mfma_f32_16x16x32_bf16 v[148:151], v[228:231], a[244:247], v[148:151]\n"
"ds_read_b64_tr_b16 v[246:247], v46 offset:11264\n"
"v_mfma_f32_16x16x32_bf16 v[152:155], v[224:227], a[232:235], v[152:155]\n"
"v_mfma_f32_16x16x32_bf16 v[152:155], v[228:231], a[248:251], v[152:155]\n"
"buffer_load_dwordx4 v22, s[20:23], s75 offen offset:2048 lds\n"
"v_mfma_f32_16x16x32_bf16 v[156:159], v[224:227], a[236:239], v[156:159]\n"
"v_mfma_f32_16x16x32_bf16 v[156:159], v[228:231], a[252:255], v[156:159]\n"
"s_waitcnt lgkmcnt(4)\n"
"v_mfma_f32_16x16x4_4b_bf16 v[64:79], v[48:49], v[56:57], 0\n"
"buffer_load_dwordx4 v23, s[20:23], s75 offen offset:3072 lds\n"
"v_mfma_f32_16x16x4_4b_bf16 v[80:95], v[50:51], v[56:57], 0\n"
"v_mfma_f32_16x16x4_4b_bf16 v[96:111], v[52:53], v[56:57], 0\n"
"v_mfma_f32_16x16x4_4b_bf16 v[112:127], v[54:55], v[56:57], 0\n"
"s_waitcnt vmcnt(28)\n"
"s_barrier\n"
"s_add_u32 s35, s40, 4128896\n"
"v_mfma_f32_16x16x32_bf16 v[128:131], v[232:235], v[192:195], v[128:131]\n"
"ds_read_b64_tr_b16 v[224:225], v248 offset:0\n"
"s_add_u32 m0, s34, 114688\n"
"v_mfma_f32_16x16x32_bf16 v[128:131], v[236:239], v[208:211], v[128:131]\n"
"buffer_load_dwordx4 v20, s[20:23], s35 offen lds\n"
"v_mfma_f32_16x16x32_bf16 v[132:135], v[232:235], v[196:199], v[132:135]\n"
"ds_read_b64_tr_b16 v[226:227], v248 offset:2048\n"
"v_mfma_f32_16x16x32_bf16 v[132:135], v[236:239], v[212:215], v[132:135]\n"
"ds_read_b64_tr_b16 v[228:229], v248 offset:4096\n"
"v_lshrrev_b32 v28, 2, v8\n"
"v_mfma_f32_16x16x32_bf16 v[136:139], v[232:235], v[200:203], v[136:139]\n"
"ds_read_b64_tr_b16 v[230:231], v248 offset:6144\n"
"v_mfma_f32_16x16x32_bf16 v[136:139], v[236:239], v[216:219], v[136:139]\n"
"v_mfma_f32_16x16x32_bf16 v[140:143], v[232:235], v[204:207], v[140:143]\n"
"v_mfma_f32_16x16x32_bf16 v[140:143], v[236:239], v[220:223], v[140:143]\n"
"s_waitcnt lgkmcnt(4)\n"
"v_mfma_f32_16x16x32_bf16 v[144:147], v[240:243], v[192:195], v[144:147]\n"
"ds_read_b64_tr_b16 v[232:233], v248 offset:8192\n"
"v_mfma_f32_16x16x32_bf16 v[144:147], v[244:247], v[208:211], v[144:147]\n"
"buffer_load_dwordx4 v21, s[20:23], s35 offen offset:1024 lds\n"
"v_mfma_f32_16x16x32_bf16 v[148:151], v[240:243], v[196:199], v[148:151]\n"
"ds_read_b64_tr_b16 v[234:235], v248 offset:10240\n"
"v_mov_b32 v30, -1\n"
"v_mfma_f32_16x16x32_bf16 v[148:151], v[244:247], v[212:215], v[148:151]\n"
"ds_read_b64_tr_b16 v[236:237], v248 offset:12288\n"
"v_mfma_f32_16x16x32_bf16 v[152:155], v[240:243], v[200:203], v[152:155]\n"
"ds_read_b64_tr_b16 v[238:239], v248 offset:14336\n"
"v_mfma_f32_16x16x32_bf16 v[152:155], v[244:247], v[216:219], v[152:155]\n"
"ds_read_b64 v[48:49], v47 offset:4096\n"
"v_mfma_f32_16x16x32_bf16 v[156:159], v[240:243], v[204:207], v[156:159]\n"
"ds_read_b64 v[50:51], v47 offset:4224\n"
"v_mfma_f32_16x16x32_bf16 v[156:159], v[244:247], v[220:223], v[156:159]\n"
"s_waitcnt lgkmcnt(6)\n"
"v_mfma_f32_16x16x32_bf16 v[64:67], v[224:227], a[0:3], v[64:67]\n"
"ds_read_b64_tr_b16 v[240:241], v251 offset:0\n"
"v_mfma_f32_16x16x32_bf16 v[64:67], v[228:231], a[16:19], v[64:67]\n"
"buffer_load_dwordx4 v22, s[20:23], s35 offen offset:2048 lds\n"
"v_mfma_f32_16x16x32_bf16 v[68:71], v[224:227], a[4:7], v[68:71]\n"
"ds_read_b64_tr_b16 v[242:243], v251 offset:2048\n"
"v_mfma_f32_16x16x32_bf16 v[68:71], v[228:231], a[20:23], v[68:71]\n"
"ds_read_b64_tr_b16 v[244:245], v251 offset:4096\n"
"v_mfma_f32_16x16x32_bf16 v[72:75], v[224:227], a[8:11], v[72:75]\n"
"ds_read_b64_tr_b16 v[246:247], v251 offset:6144\n"
"v_mfma_f32_16x16x32_bf16 v[72:75], v[228:231], a[24:27], v[72:75]\n"
"buffer_store_dword v30, v28, s[36:39], s68 offen sc1\n"
"v_mfma_f32_16x16x32_bf16 v[76:79], v[224:227], a[12:15], v[76:79]\n"
"buffer_store_dword v30, v28, s[36:39], s68 offen offset:256 sc1\n"
"v_mfma_f32_16x16x32_bf16 v[76:79], v[228:231], a[28:31], v[76:79]\n"
"s_waitcnt lgkmcnt(4)\n"
"v_mfma_f32_16x16x4_4b_bf16 v[128:143], v[48:49], v[56:57], v[128:143]\n"
"buffer_load_dwordx4 v23, s[20:23], s35 offen offset:3072 lds\n"
"v_mfma_f32_16x16x4_4b_bf16 v[144:159], v[50:51], v[56:57], v[144:159]\n"
"v_mfma_f32_16x16x32_bf16 v[80:83], v[232:235], a[0:3], v[80:83]\n"
"ds_read_b64_tr_b16 v[224:225], v251 offset:8192\n"
"v_mfma_f32_16x16x32_bf16 v[80:83], v[236:239], a[16:19], v[80:83]\n"
"ds_read_b64_tr_b16 v[226:227], v251 offset:10240\n"
"v_mfma_f32_16x16x32_bf16 v[84:87], v[232:235], a[4:7], v[84:87]\n"
"ds_read_b64_tr_b16 v[228:229], v251 offset:12288\n"
"v_mfma_f32_16x16x32_bf16 v[84:87], v[236:239], a[20:23], v[84:87]\n"
"ds_read_b64_tr_b16 v[230:231], v251 offset:14336\n"
"v_mfma_f32_16x16x32_bf16 v[88:91], v[232:235], a[8:11], v[88:91]\n"
"s_nop 0\n"
"v_max_f32 v128, 0, v128\n"
"v_mfma_f32_16x16x32_bf16 v[88:91], v[236:239], a[24:27], v[88:91]\n"
"buffer_store_dword v30, v28, s[36:39], s68 offen offset:512 sc1\n"
"v_mfma_f32_16x16x32_bf16 v[92:95], v[232:235], a[12:15], v[92:95]\n"
"s_add_u32 s75, s40, 4718720\n"
"v_max_f32 v129, 0, v129\n"
"v_max_f32 v130, 0, v130\n"
"v_mfma_f32_16x16x32_bf16 v[92:95], v[236:239], a[28:31], v[92:95]\n"
"s_waitcnt vmcnt(31) lgkmcnt(4)\n"
"s_barrier\n"
"v_max_f32 v131, 0, v131\n"
"v_mfma_f32_16x16x32_bf16 v[96:99], v[240:243], a[0:3], v[96:99]\n"
"ds_read_b64_tr_b16 v[232:233], v248 offset:16384\n"
"s_add_u32 m0, s34, 131072\n"
"v_mul_f32 v128, v128, v144\n"
"v_mfma_f32_16x16x32_bf16 v[96:99], v[244:247], a[16:19], v[96:99]\n"
"buffer_load_dwordx4 v20, s[20:23], s75 offen lds\n"
"v_mfma_f32_16x16x32_bf16 v[100:103], v[240:243], a[4:7], v[100:103]\n"
"ds_read_b64_tr_b16 v[234:235], v248 offset:18432\n"
"v_mul_f32 v129, v129, v145\n"
"v_mul_f32 v130, v130, v146\n"
"v_mfma_f32_16x16x32_bf16 v[100:103], v[244:247], a[20:23], v[100:103]\n"
"ds_read_b64_tr_b16 v[236:237], v248 offset:20480\n"
"v_mul_f32 v131, v131, v147\n"
"v_mfma_f32_16x16x32_bf16 v[104:107], v[240:243], a[8:11], v[104:107]\n"
"ds_read_b64_tr_b16 v[238:239], v248 offset:22528\n"
"v_mfma_f32_16x16x32_bf16 v[104:107], v[244:247], a[24:27], v[104:107]\n"
"buffer_load_dwordx4 v21, s[20:23], s75 offen offset:1024 lds\n"
"v_mfma_f32_16x16x32_bf16 v[108:111], v[240:243], a[12:15], v[108:111]\n"
"buffer_store_dword v30, v28, s[36:39], s68 offen offset:768 sc1\n"
"v_mfma_f32_16x16x32_bf16 v[108:111], v[244:247], a[28:31], v[108:111]\n"
"v_cvt_pk_bf16_f32 v128, v128, v129\n"
"v_cvt_pk_bf16_f32 v129, v130, v131\n"
"s_waitcnt lgkmcnt(4)\n"
"v_mfma_f32_16x16x32_bf16 v[112:115], v[224:227], a[0:3], v[112:115]\n"
"ds_read_b64_tr_b16 v[240:241], v248 offset:24576\n"
"v_max_f32 v132, 0, v132\n"
"v_max_f32 v133, 0, v133\n"
"v_mfma_f32_16x16x32_bf16 v[112:115], v[228:231], a[16:19], v[112:115]\n"
"buffer_store_dword v30, v28, s[36:39], s68 offen offset:1024 sc1\n"
"v_mfma_f32_16x16x32_bf16 v[116:119], v[224:227], a[4:7], v[116:119]\n"
"ds_read_b64_tr_b16 v[242:243], v248 offset:26624\n"
"v_max_f32 v134, 0, v134\n"
"v_max_f32 v135, 0, v135\n"
"v_mfma_f32_16x16x32_bf16 v[116:119], v[228:231], a[20:23], v[116:119]\n"
"ds_read_b64_tr_b16 v[244:245], v248 offset:28672\n"
"v_mul_f32 v132, v132, v148\n"
"v_mfma_f32_16x16x32_bf16 v[120:123], v[224:227], a[8:11], v[120:123]\n"
"ds_read_b64_tr_b16 v[246:247], v248 offset:30720\n"
"v_mfma_f32_16x16x32_bf16 v[120:123], v[228:231], a[24:27], v[120:123]\n"
"buffer_load_dwordx4 v22, s[20:23], s75 offen offset:2048 lds\n"
"v_mfma_f32_16x16x32_bf16 v[124:127], v[224:227], a[12:15], v[124:127]\n"
"v_mul_f32 v133, v133, v149\n"
"v_mul_f32 v134, v134, v150\n"
"v_mfma_f32_16x16x32_bf16 v[124:127], v[228:231], a[28:31], v[124:127]\n"
"v_mul_f32 v135, v135, v151\n"
"s_waitcnt lgkmcnt(4)\n"
"v_mfma_f32_16x16x32_bf16 v[64:67], v[232:235], a[32:35], v[64:67]\n"
"ds_read_b64_tr_b16 v[224:225], v251 offset:16384\n"
"v_cvt_pk_bf16_f32 v130, v132, v133\n"
"v_mfma_f32_16x16x32_bf16 v[64:67], v[236:239], a[48:51], v[64:67]\n"
"buffer_store_dword v30, v28, s[36:39], s68 offen offset:1280 sc1\n"
"v_mfma_f32_16x16x32_bf16 v[68:71], v[232:235], a[36:39], v[68:71]\n"
"ds_read_b64_tr_b16 v[226:227], v251 offset:18432\n"
"v_cvt_pk_bf16_f32 v131, v134, v135\n"
"v_permlane32_swap_b32 v128, v130\n"
"v_mfma_f32_16x16x32_bf16 v[68:71], v[236:239], a[52:55], v[68:71]\n"
"ds_read_b64_tr_b16 v[228:229], v251 offset:20480\n"
"v_permlane32_swap_b32 v129, v131\n"
"v_mfma_f32_16x16x32_bf16 v[72:75], v[232:235], a[40:43], v[72:75]\n"
"ds_read_b64_tr_b16 v[230:231], v251 offset:22528\n"
"v_mfma_f32_16x16x32_bf16 v[72:75], v[236:239], a[56:59], v[72:75]\n"
"buffer_store_dwordx4 v[128:131], v254, s[36:39], s56 offen sc1\n"
"v_mfma_f32_16x16x32_bf16 v[76:79], v[232:235], a[44:47], v[76:79]\n"
"v_max_f32 v136, 0, v136\n"
"v_mfma_f32_16x16x32_bf16 v[76:79], v[236:239], a[60:63], v[76:79]\n"
"v_max_f32 v137, 0, v137\n"
"s_waitcnt lgkmcnt(4)\n"
"v_mfma_f32_16x16x32_bf16 v[80:83], v[240:243], a[32:35], v[80:83]\n"
"ds_read_b64_tr_b16 v[232:233], v251 offset:24576\n"
"v_max_f32 v138, 0, v138\n"
"v_mfma_f32_16x16x32_bf16 v[80:83], v[244:247], a[48:51], v[80:83]\n"
"buffer_load_dwordx4 v23, s[20:23], s75 offen offset:3072 lds\n"
"v_mfma_f32_16x16x32_bf16 v[84:87], v[240:243], a[36:39], v[84:87]\n"
"ds_read_b64_tr_b16 v[234:235], v251 offset:26624\n"
"v_max_f32 v139, 0, v139\n"
"v_mul_f32 v136, v136, v152\n"
"v_mfma_f32_16x16x32_bf16 v[84:87], v[244:247], a[52:55], v[84:87]\n"
"ds_read_b64_tr_b16 v[236:237], v251 offset:28672\n"
"v_mfma_f32_16x16x32_bf16 v[88:91], v[240:243], a[40:43], v[88:91]\n"
"ds_read_b64_tr_b16 v[238:239], v251 offset:30720\n"
"v_mul_f32 v137, v137, v153\n"
"v_mfma_f32_16x16x32_bf16 v[88:91], v[244:247], a[56:59], v[88:91]\n"
"buffer_store_dword v30, v28, s[36:39], s68 offen offset:1536 sc1\n"
"v_mfma_f32_16x16x32_bf16 v[92:95], v[240:243], a[44:47], v[92:95]\n"
"s_add_u32 s35, s40, 256\n"
"v_mul_f32 v138, v138, v154\n"
"v_mul_f32 v139, v139, v155\n"
"v_mfma_f32_16x16x32_bf16 v[92:95], v[244:247], a[60:63], v[92:95]\n"
"s_waitcnt vmcnt(36) lgkmcnt(4)\n"
"s_barrier\n"
"v_cvt_pk_bf16_f32 v136, v136, v137\n"
"v_mfma_f32_16x16x32_bf16 v[96:99], v[224:227], a[32:35], v[96:99]\n"
"ds_read_b64_tr_b16 v[240:241], v248 offset:32768\n"
"s_add_u32 m0, s34, 0\n"
"v_mfma_f32_16x16x32_bf16 v[96:99], v[228:231], a[48:51], v[96:99]\n"
"buffer_load_dwordx4 v20, s[20:23], s35 offen lds\n"
"v_mfma_f32_16x16x32_bf16 v[100:103], v[224:227], a[36:39], v[100:103]\n"
"ds_read_b64_tr_b16 v[242:243], v248 offset:34816\n"
"v_cvt_pk_bf16_f32 v137, v138, v139\n"
"v_max_f32 v140, 0, v140\n"
"v_mfma_f32_16x16x32_bf16 v[100:103], v[228:231], a[52:55], v[100:103]\n"
"ds_read_b64_tr_b16 v[244:245], v248 offset:36864\n"
"v_max_f32 v141, 0, v141\n"
"v_mfma_f32_16x16x32_bf16 v[104:107], v[224:227], a[40:43], v[104:107]\n"
"ds_read_b64_tr_b16 v[246:247], v248 offset:38912\n"
"v_max_f32 v142, 0, v142\n"
"v_mfma_f32_16x16x32_bf16 v[104:107], v[228:231], a[56:59], v[104:107]\n"
"buffer_load_dwordx4 v21, s[20:23], s35 offen offset:1024 lds\n"
"v_mfma_f32_16x16x32_bf16 v[108:111], v[224:227], a[44:47], v[108:111]\n"
"buffer_store_dword v30, v28, s[36:39], s68 offen offset:1792 sc1\n"
"v_mfma_f32_16x16x32_bf16 v[108:111], v[228:231], a[60:63], v[108:111]\n"
"v_max_f32 v143, 0, v143\n"
"v_mul_f32 v140, v140, v156\n"
"s_waitcnt lgkmcnt(4)\n"
"v_mfma_f32_16x16x32_bf16 v[112:115], v[232:235], a[32:35], v[112:115]\n"
"ds_read_b64_tr_b16 v[224:225], v248 offset:40960\n"
"v_mul_f32 v141, v141, v157\n"
"v_mfma_f32_16x16x32_bf16 v[112:115], v[236:239], a[48:51], v[112:115]\n"
"buffer_store_dword v30, v28, s[36:39], s68 offen offset:2048 sc1\n"
"v_mfma_f32_16x16x32_bf16 v[116:119], v[232:235], a[36:39], v[116:119]\n"
"ds_read_b64_tr_b16 v[226:227], v248 offset:43008\n"
"v_mul_f32 v142, v142, v158\n"
"v_mul_f32 v143, v143, v159\n"
"v_mfma_f32_16x16x32_bf16 v[116:119], v[236:239], a[52:55], v[116:119]\n"
"ds_read_b64_tr_b16 v[228:229], v248 offset:45056\n"
"v_mfma_f32_16x16x32_bf16 v[120:123], v[232:235], a[40:43], v[120:123]\n"
"ds_read_b64_tr_b16 v[230:231], v248 offset:47104\n"
"v_cvt_pk_bf16_f32 v138, v140, v141\n"
"v_mfma_f32_16x16x32_bf16 v[120:123], v[236:239], a[56:59], v[120:123]\n"
"buffer_load_dwordx4 v22, s[20:23], s35 offen offset:2048 lds\n"
"v_mfma_f32_16x16x32_bf16 v[124:127], v[232:235], a[44:47], v[124:127]\n"
"v_cvt_pk_bf16_f32 v139, v142, v143\n"
"v_permlane32_swap_b32 v136, v138\n"
"v_mfma_f32_16x16x32_bf16 v[124:127], v[236:239], a[60:63], v[124:127]\n"
"v_permlane32_swap_b32 v137, v139\n"
"s_waitcnt lgkmcnt(4)\n"
"v_mfma_f32_16x16x32_bf16 v[64:67], v[240:243], a[64:67], v[64:67]\n"
"ds_read_b64_tr_b16 v[232:233], v251 offset:32768\n"
"v_mfma_f32_16x16x32_bf16 v[64:67], v[244:247], a[80:83], v[64:67]\n"
"buffer_store_dwordx4 v[136:139], v254, s[36:39], s56 offen offset:2048 sc1\n"
"v_mfma_f32_16x16x32_bf16 v[68:71], v[240:243], a[68:71], v[68:71]\n"
"ds_read_b64_tr_b16 v[234:235], v251 offset:34816\n"
"v_mfma_f32_16x16x32_bf16 v[68:71], v[244:247], a[84:87], v[68:71]\n"
"ds_read_b64_tr_b16 v[236:237], v251 offset:36864\n"
"v_mfma_f32_16x16x32_bf16 v[72:75], v[240:243], a[72:75], v[72:75]\n"
"ds_read_b64_tr_b16 v[238:239], v251 offset:38912\n"
"v_mfma_f32_16x16x32_bf16 v[72:75], v[244:247], a[88:91], v[72:75]\n"
"buffer_store_dword v30, v28, s[36:39], s68 offen offset:2304 sc1\n"
"v_mfma_f32_16x16x32_bf16 v[76:79], v[240:243], a[76:79], v[76:79]\n"
"v_mfma_f32_16x16x32_bf16 v[76:79], v[244:247], a[92:95], v[76:79]\n"
"s_waitcnt lgkmcnt(4)\n"
"v_mfma_f32_16x16x32_bf16 v[80:83], v[224:227], a[64:67], v[80:83]\n"
"ds_read_b64_tr_b16 v[240:241], v251 offset:40960\n"
"v_mfma_f32_16x16x32_bf16 v[80:83], v[228:231], a[80:83], v[80:83]\n"
"buffer_load_dwordx4 v23, s[20:23], s35 offen offset:3072 lds\n"
"v_mfma_f32_16x16x32_bf16 v[84:87], v[224:227], a[68:71], v[84:87]\n"
"ds_read_b64_tr_b16 v[242:243], v251 offset:43008\n"
"v_mfma_f32_16x16x32_bf16 v[84:87], v[228:231], a[84:87], v[84:87]\n"
"ds_read_b64_tr_b16 v[244:245], v251 offset:45056\n"
"v_mfma_f32_16x16x32_bf16 v[88:91], v[224:227], a[72:75], v[88:91]\n"
"ds_read_b64_tr_b16 v[246:247], v251 offset:47104\n"
"s_add_u32 s75, s40, 590080\n"
"v_mfma_f32_16x16x32_bf16 v[88:91], v[228:231], a[88:91], v[88:91]\n"
"buffer_store_dword v30, v28, s[36:39], s68 offen offset:2560 sc1\n"
"v_mfma_f32_16x16x32_bf16 v[92:95], v[224:227], a[76:79], v[92:95]\n"
"buffer_store_dword v30, v28, s[36:39], s68 offen offset:2816 sc1\n"
"v_mfma_f32_16x16x32_bf16 v[92:95], v[228:231], a[92:95], v[92:95]\n"
"s_waitcnt vmcnt(41) lgkmcnt(4)\n"
"s_barrier\n"
"v_mfma_f32_16x16x32_bf16 v[96:99], v[232:235], a[64:67], v[96:99]\n"
"ds_read_b64_tr_b16 v[224:225], v248 offset:49152\n"
"s_add_u32 m0, s34, 16384\n"
"v_mfma_f32_16x16x32_bf16 v[96:99], v[236:239], a[80:83], v[96:99]\n"
"buffer_load_dwordx4 v20, s[20:23], s75 offen lds\n"
"v_mfma_f32_16x16x32_bf16 v[100:103], v[232:235], a[68:71], v[100:103]\n"
"ds_read_b64_tr_b16 v[226:227], v248 offset:51200\n"
"v_mfma_f32_16x16x32_bf16 v[100:103], v[236:239], a[84:87], v[100:103]\n"
"ds_read_b64_tr_b16 v[228:229], v248 offset:53248\n"
"v_mfma_f32_16x16x32_bf16 v[104:107], v[232:235], a[72:75], v[104:107]\n"
"ds_read_b64_tr_b16 v[230:231], v248 offset:55296\n"
"v_mfma_f32_16x16x32_bf16 v[104:107], v[236:239], a[88:91], v[104:107]\n"
"v_mfma_f32_16x16x32_bf16 v[108:111], v[232:235], a[76:79], v[108:111]\n"
"buffer_store_dword v30, v28, s[36:39], s68 offen offset:3072 sc1\n"
"v_mfma_f32_16x16x32_bf16 v[108:111], v[236:239], a[92:95], v[108:111]\n"
"s_waitcnt lgkmcnt(4)\n"
"v_mfma_f32_16x16x32_bf16 v[112:115], v[240:243], a[64:67], v[112:115]\n"
"ds_read_b64_tr_b16 v[232:233], v248 offset:57344\n"
"v_mfma_f32_16x16x32_bf16 v[112:115], v[244:247], a[80:83], v[112:115]\n"
"buffer_load_dwordx4 v21, s[20:23], s75 offen offset:1024 lds\n"
"v_mfma_f32_16x16x32_bf16 v[116:119], v[240:243], a[68:71], v[116:119]\n"
"ds_read_b64_tr_b16 v[234:235], v248 offset:59392\n"
"v_mfma_f32_16x16x32_bf16 v[116:119], v[244:247], a[84:87], v[116:119]\n"
"ds_read_b64_tr_b16 v[236:237], v248 offset:61440\n"
"v_mfma_f32_16x16x32_bf16 v[120:123], v[240:243], a[72:75], v[120:123]\n"
"ds_read_b64_tr_b16 v[238:239], v248 offset:63488\n"
"v_mfma_f32_16x16x32_bf16 v[120:123], v[244:247], a[88:91], v[120:123]\n"
"v_mfma_f32_16x16x32_bf16 v[124:127], v[240:243], a[76:79], v[124:127]\n"
"buffer_store_dword v30, v28, s[36:39], s68 offen offset:3328 sc1\n"
"v_mfma_f32_16x16x32_bf16 v[124:127], v[244:247], a[92:95], v[124:127]\n"
"s_waitcnt lgkmcnt(4)\n"
"v_mfma_f32_16x16x32_bf16 v[64:67], v[224:227], a[96:99], v[64:67]\n"
"ds_read_b64_tr_b16 v[240:241], v251 offset:49152\n"
"v_mfma_f32_16x16x32_bf16 v[64:67], v[228:231], a[112:115], v[64:67]\n"
"buffer_load_dwordx4 v22, s[20:23], s75 offen offset:2048 lds\n"
"v_mfma_f32_16x16x32_bf16 v[68:71], v[224:227], a[100:103], v[68:71]\n"
"ds_read_b64_tr_b16 v[242:243], v251 offset:51200\n"
"v_mfma_f32_16x16x32_bf16 v[68:71], v[228:231], a[116:119], v[68:71]\n"
"ds_read_b64_tr_b16 v[244:245], v251 offset:53248\n"
"v_mfma_f32_16x16x32_bf16 v[72:75], v[224:227], a[104:107], v[72:75]\n"
"ds_read_b64_tr_b16 v[246:247], v251 offset:55296\n"
"v_mfma_f32_16x16x32_bf16 v[72:75], v[228:231], a[120:123], v[72:75]\n"
"v_mfma_f32_16x16x32_bf16 v[76:79], v[224:227], a[108:111], v[76:79]\n"
"buffer_store_dword v30, v28, s[36:39], s68 offen offset:3584 sc1\n"
"v_mfma_f32_16x16x32_bf16 v[76:79], v[228:231], a[124:127], v[76:79]\n"
"s_waitcnt lgkmcnt(4)\n"
"v_mfma_f32_16x16x32_bf16 v[80:83], v[232:235], a[96:99], v[80:83]\n"
"ds_read_b64_tr_b16 v[224:225], v251 offset:57344\n"
"v_mfma_f32_16x16x32_bf16 v[80:83], v[236:239], a[112:115], v[80:83]\n"
"buffer_load_dwordx4 v23, s[20:23], s75 offen offset:3072 lds\n"
"v_mfma_f32_16x16x32_bf16 v[84:87], v[232:235], a[100:103], v[84:87]\n"
"ds_read_b64_tr_b16 v[226:227], v251 offset:59392\n"
"v_mfma_f32_16x16x32_bf16 v[84:87], v[236:239], a[116:119], v[84:87]\n"
"ds_read_b64_tr_b16 v[228:229], v251 offset:61440\n"
"v_mfma_f32_16x16x32_bf16 v[88:91], v[232:235], a[104:107], v[88:91]\n"
"ds_read_b64_tr_b16 v[230:231], v251 offset:63488\n"
"v_mfma_f32_16x16x32_bf16 v[88:91], v[236:239], a[120:123], v[88:91]\n"
"buffer_store_dword v30, v28, s[36:39], s68 offen offset:3840 sc1\n"
"v_mfma_f32_16x16x32_bf16 v[92:95], v[232:235], a[108:111], v[92:95]\n"
"s_add_u32 s35, s40, 1179904\n"
"v_mfma_f32_16x16x32_bf16 v[92:95], v[236:239], a[124:127], v[92:95]\n"
"s_waitcnt vmcnt(44) lgkmcnt(4)\n"
"s_barrier\n"
"v_mfma_f32_16x16x32_bf16 v[96:99], v[240:243], a[96:99], v[96:99]\n"
"ds_read_b64_tr_b16 v[232:233], v249 offset:0\n"
"s_add_u32 m0, s34, 32768\n"
"v_mfma_f32_16x16x32_bf16 v[96:99], v[244:247], a[112:115], v[96:99]\n"
"buffer_load_dwordx4 v20, s[20:23], s35 offen lds\n"
"v_mfma_f32_16x16x32_bf16 v[100:103], v[240:243], a[100:103], v[100:103]\n"
"ds_read_b64_tr_b16 v[234:235], v249 offset:2048\n"
"v_mfma_f32_16x16x32_bf16 v[100:103], v[244:247], a[116:119], v[100:103]\n"
"ds_read_b64_tr_b16 v[236:237], v249 offset:4096\n"
"v_mfma_f32_16x16x32_bf16 v[104:107], v[240:243], a[104:107], v[104:107]\n"
"ds_read_b64_tr_b16 v[238:239], v249 offset:6144\n"
"v_mfma_f32_16x16x32_bf16 v[104:107], v[244:247], a[120:123], v[104:107]\n"
"buffer_store_dword v30, v28, s[36:39], s69 offen sc1\n"
"v_mfma_f32_16x16x32_bf16 v[108:111], v[240:243], a[108:111], v[108:111]\n"
"v_mfma_f32_16x16x32_bf16 v[108:111], v[244:247], a[124:127], v[108:111]\n"
"s_waitcnt lgkmcnt(4)\n"
"v_mfma_f32_16x16x32_bf16 v[112:115], v[224:227], a[96:99], v[112:115]\n"
"ds_read_b64_tr_b16 v[240:241], v249 offset:8192\n"
"v_mfma_f32_16x16x32_bf16 v[112:115], v[228:231], a[112:115], v[112:115]\n"
"buffer_load_dwordx4 v21, s[20:23], s35 offen offset:1024 lds\n"
"v_mfma_f32_16x16x32_bf16 v[116:119], v[224:227], a[100:103], v[116:119]\n"
"ds_read_b64_tr_b16 v[242:243], v249 offset:10240\n"
"v_mfma_f32_16x16x32_bf16 v[116:119], v[228:231], a[116:119], v[116:119]\n"
"ds_read_b64_tr_b16 v[244:245], v249 offset:12288\n"
"v_mfma_f32_16x16x32_bf16 v[120:123], v[224:227], a[104:107], v[120:123]\n"
"ds_read_b64_tr_b16 v[246:247], v249 offset:14336\n"
"v_mfma_f32_16x16x32_bf16 v[120:123], v[228:231], a[120:123], v[120:123]\n"
"buffer_store_dword v30, v28, s[36:39], s69 offen offset:256 sc1\n"
"v_mfma_f32_16x16x32_bf16 v[124:127], v[224:227], a[108:111], v[124:127]\n"
"buffer_store_dword v30, v28, s[36:39], s69 offen offset:512 sc1\n"
"v_mfma_f32_16x16x32_bf16 v[124:127], v[228:231], a[124:127], v[124:127]\n"
"s_waitcnt lgkmcnt(4)\n"
"v_mfma_f32_16x16x32_bf16 v[64:67], v[232:235], a[128:131], v[64:67]\n"
"ds_read_b64_tr_b16 v[224:225], v252 offset:0\n"
"v_mfma_f32_16x16x32_bf16 v[64:67], v[236:239], a[144:147], v[64:67]\n"
"buffer_load_dwordx4 v22, s[20:23], s35 offen offset:2048 lds\n"
"v_mfma_f32_16x16x32_bf16 v[68:71], v[232:235], a[132:135], v[68:71]\n"
"ds_read_b64_tr_b16 v[226:227], v252 offset:2048\n"
"v_mfma_f32_16x16x32_bf16 v[68:71], v[236:239], a[148:151], v[68:71]\n"
"ds_read_b64_tr_b16 v[228:229], v252 offset:4096\n"
"v_mfma_f32_16x16x32_bf16 v[72:75], v[232:235], a[136:139], v[72:75]\n"
"ds_read_b64_tr_b16 v[230:231], v252 offset:6144\n"
"v_mfma_f32_16x16x32_bf16 v[72:75], v[236:239], a[152:155], v[72:75]\n"
"v_mfma_f32_16x16x32_bf16 v[76:79], v[232:235], a[140:143], v[76:79]\n"
"buffer_store_dword v30, v28, s[36:39], s69 offen offset:768 sc1\n"
"v_mfma_f32_16x16x32_bf16 v[76:79], v[236:239], a[156:159], v[76:79]\n"
"s_waitcnt lgkmcnt(4)\n"
"v_mfma_f32_16x16x32_bf16 v[80:83], v[240:243], a[128:131], v[80:83]\n"
"ds_read_b64_tr_b16 v[232:233], v252 offset:8192\n"
"v_mfma_f32_16x16x32_bf16 v[80:83], v[244:247], a[144:147], v[80:83]\n"
"buffer_load_dwordx4 v23, s[20:23], s35 offen offset:3072 lds\n"
"v_mfma_f32_16x16x32_bf16 v[84:87], v[240:243], a[132:135], v[84:87]\n"
"ds_read_b64_tr_b16 v[234:235], v252 offset:10240\n"
"v_mfma_f32_16x16x32_bf16 v[84:87], v[244:247], a[148:151], v[84:87]\n"
"ds_read_b64_tr_b16 v[236:237], v252 offset:12288\n"
"v_mfma_f32_16x16x32_bf16 v[88:91], v[240:243], a[136:139], v[88:91]\n"
"ds_read_b64_tr_b16 v[238:239], v252 offset:14336\n"
"v_mfma_f32_16x16x32_bf16 v[88:91], v[244:247], a[152:155], v[88:91]\n"
"v_mfma_f32_16x16x32_bf16 v[92:95], v[240:243], a[140:143], v[92:95]\n"
"buffer_store_dword v30, v28, s[36:39], s69 offen offset:1024 sc1\n"
"v_mfma_f32_16x16x32_bf16 v[92:95], v[244:247], a[156:159], v[92:95]\n"
"s_waitcnt vmcnt(21) lgkmcnt(4)\n"
"s_barrier\n"
"v_mfma_f32_16x16x32_bf16 v[96:99], v[224:227], a[128:131], v[96:99]\n"
"ds_read_b64_tr_b16 v[240:241], v249 offset:16384\n"
"v_mfma_f32_16x16x32_bf16 v[96:99], v[228:231], a[144:147], v[96:99]\n"
"buffer_store_dword v19, v18, s[36:39], 0 offen offset:16 sc1\n"
"v_mfma_f32_16x16x32_bf16 v[100:103], v[224:227], a[132:135], v[100:103]\n"
"ds_read_b64_tr_b16 v[242:243], v249 offset:18432\n"
"v_mfma_f32_16x16x32_bf16 v[100:103], v[228:231], a[148:151], v[100:103]\n"
"ds_read_b64_tr_b16 v[244:245], v249 offset:20480\n"
"s_add_u32 s75, s40, 1769728\n"
"v_mfma_f32_16x16x32_bf16 v[104:107], v[224:227], a[136:139], v[104:107]\n"
"ds_read_b64_tr_b16 v[246:247], v249 offset:22528\n"
"s_add_u32 m0, s34, 49152\n"
"v_mfma_f32_16x16x32_bf16 v[104:107], v[228:231], a[152:155], v[104:107]\n"
"buffer_load_dwordx4 v20, s[20:23], s75 offen lds\n"
"v_mfma_f32_16x16x32_bf16 v[108:111], v[224:227], a[140:143], v[108:111]\n"
"v_mfma_f32_16x16x32_bf16 v[108:111], v[228:231], a[156:159], v[108:111]\n"
"s_waitcnt lgkmcnt(4)\n"
"v_mfma_f32_16x16x32_bf16 v[112:115], v[232:235], a[128:131], v[112:115]\n"
"ds_read_b64_tr_b16 v[224:225], v249 offset:24576\n"
"v_mfma_f32_16x16x32_bf16 v[112:115], v[236:239], a[144:147], v[112:115]\n"
"buffer_load_dwordx4 v21, s[20:23], s75 offen offset:1024 lds\n"
"v_mfma_f32_16x16x32_bf16 v[116:119], v[232:235], a[132:135], v[116:119]\n"
"ds_read_b64_tr_b16 v[226:227], v249 offset:26624\n"
"v_mfma_f32_16x16x32_bf16 v[116:119], v[236:239], a[148:151], v[116:119]\n"
"ds_read_b64_tr_b16 v[228:229], v249 offset:28672\n"
"v_mfma_f32_16x16x32_bf16 v[120:123], v[232:235], a[136:139], v[120:123]\n"
"ds_read_b64_tr_b16 v[230:231], v249 offset:30720\n"
"v_mfma_f32_16x16x32_bf16 v[120:123], v[236:239], a[152:155], v[120:123]\n"
"buffer_store_dword v30, v28, s[36:39], s69 offen offset:1280 sc1\n"
"v_mfma_f32_16x16x32_bf16 v[124:127], v[232:235], a[140:143], v[124:127]\n"
"buffer_store_dword v30, v28, s[36:39], s69 offen offset:1536 sc1\n"
"v_mfma_f32_16x16x32_bf16 v[124:127], v[236:239], a[156:159], v[124:127]\n"
"s_waitcnt lgkmcnt(4)\n"
"v_mfma_f32_16x16x32_bf16 v[64:67], v[240:243], a[160:163], v[64:67]\n"
"ds_read_b64_tr_b16 v[232:233], v252 offset:16384\n"
"v_mfma_f32_16x16x32_bf16 v[64:67], v[244:247], a[176:179], v[64:67]\n"
"buffer_load_dwordx4 v22, s[20:23], s75 offen offset:2048 lds\n"
"v_mfma_f32_16x16x32_bf16 v[68:71], v[240:243], a[164:167], v[68:71]\n"
"ds_read_b64_tr_b16 v[234:235], v252 offset:18432\n"
"v_mfma_f32_16x16x32_bf16 v[68:71], v[244:247], a[180:183], v[68:71]\n"
"ds_read_b64_tr_b16 v[236:237], v252 offset:20480\n"
"v_mfma_f32_16x16x32_bf16 v[72:75], v[240:243], a[168:171], v[72:75]\n"
"ds_read_b64_tr_b16 v[238:239], v252 offset:22528\n"
"v_mfma_f32_16x16x32_bf16 v[72:75], v[244:247], a[184:187], v[72:75]\n"
"v_mfma_f32_16x16x32_bf16 v[76:79], v[240:243], a[172:175], v[76:79]\n"
"buffer_store_dword v30, v28, s[36:39], s69 offen offset:1792 sc1\n"
"v_mfma_f32_16x16x32_bf16 v[76:79], v[244:247], a[188:191], v[76:79]\n"
"s_waitcnt lgkmcnt(4)\n"
"v_mfma_f32_16x16x32_bf16 v[80:83], v[224:227], a[160:163], v[80:83]\n"
"ds_read_b64_tr_b16 v[240:241], v252 offset:24576\n"
"v_mfma_f32_16x16x32_bf16 v[80:83], v[228:231], a[176:179], v[80:83]\n"
"buffer_load_dwordx4 v23, s[20:23], s75 offen offset:3072 lds\n"
"v_mfma_f32_16x16x32_bf16 v[84:87], v[224:227], a[164:167], v[84:87]\n"
"ds_read_b64_tr_b16 v[242:243], v252 offset:26624\n"
"v_mfma_f32_16x16x32_bf16 v[84:87], v[228:231], a[180:183], v[84:87]\n"
"ds_read_b64_tr_b16 v[244:245], v252 offset:28672\n"
"v_mfma_f32_16x16x32_bf16 v[88:91], v[224:227], a[168:171], v[88:91]\n"
"ds_read_b64_tr_b16 v[246:247], v252 offset:30720\n"
"v_mfma_f32_16x16x32_bf16 v[88:91], v[228:231], a[184:187], v[88:91]\n"
"v_mfma_f32_16x16x32_bf16 v[92:95], v[224:227], a[172:175], v[92:95]\n"
"buffer_store_dword v30, v28, s[36:39], s69 offen offset:2048 sc1\n"
"v_mfma_f32_16x16x32_bf16 v[92:95], v[228:231], a[188:191], v[92:95]\n"
"s_waitcnt lgkmcnt(4)\n"
"s_barrier\n"
"v_mfma_f32_16x16x32_bf16 v[96:99], v[232:235], a[160:163], v[96:99]\n"
"ds_read_b64_tr_b16 v[224:225], v249 offset:32768\n"
"v_mfma_f32_16x16x32_bf16 v[96:99], v[236:239], a[176:179], v[96:99]\n"
"buffer_store_dword v30, v28, s[36:39], s69 offen offset:2304 sc1\n"
"v_mfma_f32_16x16x32_bf16 v[100:103], v[232:235], a[164:167], v[100:103]\n"
"ds_read_b64_tr_b16 v[226:227], v249 offset:34816\n"
"v_mfma_f32_16x16x32_bf16 v[100:103], v[236:239], a[180:183], v[100:103]\n"
"ds_read_b64_tr_b16 v[228:229], v249 offset:36864\n"
"v_mfma_f32_16x16x32_bf16 v[104:107], v[232:235], a[168:171], v[104:107]\n"
"ds_read_b64_tr_b16 v[230:231], v249 offset:38912\n"
"v_mfma_f32_16x16x32_bf16 v[104:107], v[236:239], a[184:187], v[104:107]\n"
"v_mfma_f32_16x16x32_bf16 v[108:111], v[232:235], a[172:175], v[108:111]\n"
"buffer_store_dword v30, v28, s[36:39], s69 offen offset:2560 sc1\n"
"v_mfma_f32_16x16x32_bf16 v[108:111], v[236:239], a[188:191], v[108:111]\n"
"s_waitcnt lgkmcnt(4)\n"
"v_mfma_f32_16x16x32_bf16 v[112:115], v[240:243], a[160:163], v[112:115]\n"
"ds_read_b64_tr_b16 v[232:233], v249 offset:40960\n"
"v_mfma_f32_16x16x32_bf16 v[112:115], v[244:247], a[176:179], v[112:115]\n"
"ds_read_b64_tr_b16 v[234:235], v249 offset:43008\n"
"v_mfma_f32_16x16x32_bf16 v[116:119], v[240:243], a[164:167], v[116:119]\n"
"ds_read_b64_tr_b16 v[236:237], v249 offset:45056\n"
"v_mfma_f32_16x16x32_bf16 v[116:119], v[244:247], a[180:183], v[116:119]\n"
"ds_read_b64_tr_b16 v[238:239], v249 offset:47104\n"
"v_mfma_f32_16x16x32_bf16 v[120:123], v[240:243], a[168:171], v[120:123]\n"
"v_mfma_f32_16x16x32_bf16 v[120:123], v[244:247], a[184:187], v[120:123]\n"
"buffer_store_dword v30, v28, s[36:39], s69 offen offset:2816 sc1\n"
"v_mfma_f32_16x16x32_bf16 v[124:127], v[240:243], a[172:175], v[124:127]\n"
"v_mfma_f32_16x16x32_bf16 v[124:127], v[244:247], a[188:191], v[124:127]\n"
"s_waitcnt lgkmcnt(4)\n"
"v_mfma_f32_16x16x32_bf16 v[64:67], v[224:227], a[192:195], v[64:67]\n"
"ds_read_b64_tr_b16 v[240:241], v252 offset:32768\n"
"v_mfma_f32_16x16x32_bf16 v[64:67], v[228:231], a[208:211], v[64:67]\n"
"ds_read_b64_tr_b16 v[242:243], v252 offset:34816\n"
"v_mfma_f32_16x16x32_bf16 v[68:71], v[224:227], a[196:199], v[68:71]\n"
"ds_read_b64_tr_b16 v[244:245], v252 offset:36864\n"
"v_mfma_f32_16x16x32_bf16 v[68:71], v[228:231], a[212:215], v[68:71]\n"
"ds_read_b64_tr_b16 v[246:247], v252 offset:38912\n"
"v_mfma_f32_16x16x32_bf16 v[72:75], v[224:227], a[200:203], v[72:75]\n"
"v_mfma_f32_16x16x32_bf16 v[72:75], v[228:231], a[216:219], v[72:75]\n"
"buffer_load_dword v255, v59, s[28:31], 0 offen\n"
"v_mfma_f32_16x16x32_bf16 v[76:79], v[224:227], a[204:207], v[76:79]\n"
"v_mfma_f32_16x16x32_bf16 v[76:79], v[228:231], a[220:223], v[76:79]\n"
"s_waitcnt lgkmcnt(4)\n"
"v_mfma_f32_16x16x32_bf16 v[80:83], v[232:235], a[192:195], v[80:83]\n"
"ds_read_b64_tr_b16 v[224:225], v252 offset:40960\n"
"v_mfma_f32_16x16x32_bf16 v[80:83], v[236:239], a[208:211], v[80:83]\n"
"ds_read_b64_tr_b16 v[226:227], v252 offset:43008\n"
"v_mfma_f32_16x16x32_bf16 v[84:87], v[232:235], a[196:199], v[84:87]\n"
"ds_read_b64_tr_b16 v[228:229], v252 offset:45056\n"
"v_mfma_f32_16x16x32_bf16 v[84:87], v[236:239], a[212:215], v[84:87]\n"
"ds_read_b64_tr_b16 v[230:231], v252 offset:47104\n"
"v_mfma_f32_16x16x32_bf16 v[88:91], v[232:235], a[200:203], v[88:91]\n"
"v_mfma_f32_16x16x32_bf16 v[88:91], v[236:239], a[216:219], v[88:91]\n"
"v_mfma_f32_16x16x32_bf16 v[92:95], v[232:235], a[204:207], v[92:95]\n"
"v_mfma_f32_16x16x32_bf16 v[92:95], v[236:239], a[220:223], v[92:95]\n"
"s_waitcnt lgkmcnt(4)\n"
"s_barrier\n"
"v_mfma_f32_16x16x32_bf16 v[96:99], v[240:243], a[192:195], v[96:99]\n"
"ds_read_b64_tr_b16 v[232:233], v249 offset:49152\n"
"v_mfma_f32_16x16x32_bf16 v[96:99], v[244:247], a[208:211], v[96:99]\n"
"ds_read_b64_tr_b16 v[234:235], v249 offset:51200\n"
"v_mfma_f32_16x16x32_bf16 v[100:103], v[240:243], a[196:199], v[100:103]\n"
"ds_read_b64_tr_b16 v[236:237], v249 offset:53248\n"
"v_mfma_f32_16x16x32_bf16 v[100:103], v[244:247], a[212:215], v[100:103]\n"
"ds_read_b64_tr_b16 v[238:239], v249 offset:55296\n"
"v_mfma_f32_16x16x32_bf16 v[104:107], v[240:243], a[200:203], v[104:107]\n"
"ds_read_b64 v[48:49], v47 offset:1024\n"
"v_mfma_f32_16x16x32_bf16 v[104:107], v[244:247], a[216:219], v[104:107]\n"
"ds_read_b64 v[50:51], v47 offset:3072\n"
"v_mfma_f32_16x16x32_bf16 v[108:111], v[240:243], a[204:207], v[108:111]\n"
"ds_read_b64 v[52:53], v47 offset:1152\n"
"v_mfma_f32_16x16x32_bf16 v[108:111], v[244:247], a[220:223], v[108:111]\n"
"ds_read_b64 v[54:55], v47 offset:3200\n"
"s_waitcnt lgkmcnt(8)\n"
"v_mfma_f32_16x16x32_bf16 v[112:115], v[224:227], a[192:195], v[112:115]\n"
"ds_read_b64_tr_b16 v[240:241], v249 offset:57344\n"
"v_mfma_f32_16x16x32_bf16 v[112:115], v[228:231], a[208:211], v[112:115]\n"
"ds_read_b64_tr_b16 v[242:243], v249 offset:59392\n"
"v_mfma_f32_16x16x32_bf16 v[116:119], v[224:227], a[196:199], v[116:119]\n"
"ds_read_b64_tr_b16 v[244:245], v249 offset:61440\n"
"v_mfma_f32_16x16x32_bf16 v[116:119], v[228:231], a[212:215], v[116:119]\n"
"ds_read_b64_tr_b16 v[246:247], v249 offset:63488\n"
"v_mfma_f32_16x16x32_bf16 v[120:123], v[224:227], a[200:203], v[120:123]\n"
"v_mfma_f32_16x16x32_bf16 v[120:123], v[228:231], a[216:219], v[120:123]\n"
"v_mfma_f32_16x16x32_bf16 v[124:127], v[224:227], a[204:207], v[124:127]\n"
"v_mfma_f32_16x16x32_bf16 v[124:127], v[228:231], a[220:223], v[124:127]\n"
"s_waitcnt lgkmcnt(4)\n"
"v_mfma_f32_16x16x4_4b_bf16 v[128:143], v[48:49], v[56:57], 0\n"
"v_mfma_f32_16x16x4_4b_bf16 v[144:159], v[50:51], v[56:57], 0\n"
"v_mfma_f32_16x16x4_4b_bf16 v[160:175], v[52:53], v[56:57], 0\n"
"v_mfma_f32_16x16x4_4b_bf16 v[176:191], v[54:55], v[56:57], 0\n"
"v_mfma_f32_16x16x32_bf16 v[64:67], v[232:235], a[224:227], v[64:67]\n"
"ds_read_b64_tr_b16 v[224:225], v252 offset:49152\n"
"v_mfma_f32_16x16x32_bf16 v[64:67], v[236:239], a[240:243], v[64:67]\n"
"ds_read_b64_tr_b16 v[226:227], v252 offset:51200\n"
"v_mfma_f32_16x16x32_bf16 v[68:71], v[232:235], a[228:231], v[68:71]\n"
"ds_read_b64_tr_b16 v[228:229], v252 offset:53248\n"
"v_mfma_f32_16x16x32_bf16 v[68:71], v[236:239], a[244:247], v[68:71]\n"
"ds_read_b64_tr_b16 v[230:231], v252 offset:55296\n"
"v_mfma_f32_16x16x32_bf16 v[72:75], v[232:235], a[232:235], v[72:75]\n"
"v_mfma_f32_16x16x32_bf16 v[72:75], v[236:239], a[248:251], v[72:75]\n"
"v_mfma_f32_16x16x32_bf16 v[76:79], v[232:235], a[236:239], v[76:79]\n"
"v_mfma_f32_16x16x32_bf16 v[76:79], v[236:239], a[252:255], v[76:79]\n"
"s_waitcnt lgkmcnt(4)\n"
"v_mfma_f32_16x16x32_bf16 v[80:83], v[240:243], a[224:227], v[80:83]\n"
"ds_read_b64_tr_b16 v[232:233], v252 offset:57344\n"
"v_mfma_f32_16x16x32_bf16 v[80:83], v[244:247], a[240:243], v[80:83]\n"
"ds_read_b64_tr_b16 v[234:235], v252 offset:59392\n"
"v_mfma_f32_16x16x32_bf16 v[84:87], v[240:243], a[228:231], v[84:87]\n"
"ds_read_b64_tr_b16 v[236:237], v252 offset:61440\n"
"v_mfma_f32_16x16x32_bf16 v[84:87], v[244:247], a[244:247], v[84:87]\n"
"ds_read_b64_tr_b16 v[238:239], v252 offset:63488\n"
"v_mfma_f32_16x16x32_bf16 v[88:91], v[240:243], a[232:235], v[88:91]\n"
"v_mfma_f32_16x16x32_bf16 v[88:91], v[244:247], a[248:251], v[88:91]\n"
"v_mfma_f32_16x16x32_bf16 v[92:95], v[240:243], a[236:239], v[92:95]\n"
"s_add_u32 s35, s40, 384\n"
"v_mfma_f32_16x16x32_bf16 v[92:95], v[244:247], a[252:255], v[92:95]\n"
"s_waitcnt lgkmcnt(4)\n"
"s_barrier\n"
"v_mfma_f32_16x16x32_bf16 v[96:99], v[224:227], a[224:227], v[96:99]\n"
"ds_read_b64_tr_b16 v[240:241], v250 offset:0\n"
"s_add_u32 m0, s34, 65536\n"
"v_mfma_f32_16x16x32_bf16 v[96:99], v[228:231], a[240:243], v[96:99]\n"
"buffer_load_dwordx4 v20, s[20:23], s35 offen lds\n"
"v_mfma_f32_16x16x32_bf16 v[100:103], v[224:227], a[228:231], v[100:103]\n"
"ds_read_b64_tr_b16 v[242:243], v250 offset:2048\n"
"v_mfma_f32_16x16x32_bf16 v[100:103], v[228:231], a[244:247], v[100:103]\n"
"ds_read_b64_tr_b16 v[244:245], v250 offset:4096\n"
"v_mfma_f32_16x16x32_bf16 v[104:107], v[224:227], a[232:235], v[104:107]\n"
"ds_read_b64_tr_b16 v[246:247], v250 offset:6144\n"
"v_mfma_f32_16x16x32_bf16 v[104:107], v[228:231], a[248:251], v[104:107]\n"
"buffer_load_dwordx4 v21, s[20:23], s35 offen offset:1024 lds\n"
"v_mfma_f32_16x16x32_bf16 v[108:111], v[224:227], a[236:239], v[108:111]\n"
"v_mfma_f32_16x16x32_bf16 v[108:111], v[228:231], a[252:255], v[108:111]\n"
"s_waitcnt lgkmcnt(4)\n"
"v_mfma_f32_16x16x32_bf16 v[112:115], v[232:235], a[224:227], v[112:115]\n"
"ds_read_b64_tr_b16 v[224:225], v250 offset:8192\n"
"v_mfma_f32_16x16x32_bf16 v[112:115], v[236:239], a[240:243], v[112:115]\n"
"buffer_load_dwordx4 v22, s[20:23], s35 offen offset:2048 lds\n"
"v_mfma_f32_16x16x32_bf16 v[116:119], v[232:235], a[228:231], v[116:119]\n"
"ds_read_b64_tr_b16 v[226:227], v250 offset:10240\n"
"v_mfma_f32_16x16x32_bf16 v[116:119], v[236:239], a[244:247], v[116:119]\n"
"ds_read_b64_tr_b16 v[228:229], v250 offset:12288\n"
"v_mfma_f32_16x16x32_bf16 v[120:123], v[232:235], a[232:235], v[120:123]\n"
"ds_read_b64_tr_b16 v[230:231], v250 offset:14336\n"
"v_mfma_f32_16x16x32_bf16 v[120:123], v[236:239], a[248:251], v[120:123]\n"
"buffer_load_dwordx4 v23, s[20:23], s35 offen offset:3072 lds\n"
"v_mfma_f32_16x16x32_bf16 v[124:127], v[232:235], a[236:239], v[124:127]\n"
"v_mfma_f32_16x16x32_bf16 v[124:127], v[236:239], a[252:255], v[124:127]\n"
"s_waitcnt lgkmcnt(4)\n"
"v_mfma_f32_16x16x32_bf16 v[64:67], v[240:243], v[192:195], v[64:67]\n"
"ds_read_b64_tr_b16 v[232:233], v253 offset:0\n"
"s_add_u32 m0, s77, 152576\n"
"v_mfma_f32_16x16x32_bf16 v[64:67], v[244:247], v[208:211], v[64:67]\n"
"buffer_load_dwordx4 v8, s[36:39], s81 offen sc1 lds\n"
"v_mfma_f32_16x16x32_bf16 v[68:71], v[240:243], v[196:199], v[68:71]\n"
"ds_read_b64_tr_b16 v[234:235], v253 offset:2048\n"
"v_mfma_f32_16x16x32_bf16 v[68:71], v[244:247], v[212:215], v[68:71]\n"
"ds_read_b64_tr_b16 v[236:237], v253 offset:4096\n"
"v_mfma_f32_16x16x32_bf16 v[72:75], v[240:243], v[200:203], v[72:75]\n"
"ds_read_b64_tr_b16 v[238:239], v253 offset:6144\n"
"v_mfma_f32_16x16x32_bf16 v[72:75], v[244:247], v[216:219], v[72:75]\n"
"buffer_load_dwordx4 v8, s[36:39], s81 offen offset:1024 sc1 lds\n"
"v_mfma_f32_16x16x32_bf16 v[76:79], v[240:243], v[204:207], v[76:79]\n"
"v_mfma_f32_16x16x32_bf16 v[76:79], v[244:247], v[220:223], v[76:79]\n"
"s_waitcnt lgkmcnt(4)\n"
"v_mfma_f32_16x16x32_bf16 v[80:83], v[224:227], v[192:195], v[80:83]\n"
"ds_read_b64_tr_b16 v[240:241], v253 offset:8192\n"
"s_mov_b32 m0, 160768\n"
"v_mfma_f32_16x16x32_bf16 v[80:83], v[228:231], v[208:211], v[80:83]\n"
"buffer_load_dwordx4 v8, s[36:39], s80 offen sc1 lds\n"
"v_mfma_f32_16x16x32_bf16 v[84:87], v[224:227], v[196:199], v[84:87]\n"
"ds_read_b64_tr_b16 v[242:243], v253 offset:10240\n"
"v_mfma_f32_16x16x32_bf16 v[84:87], v[228:231], v[212:215], v[84:87]\n"
"ds_read_b64_tr_b16 v[244:245], v253 offset:12288\n"
"v_mfma_f32_16x16x32_bf16 v[88:91], v[224:227], v[200:203], v[88:91]\n"
"ds_read_b64_tr_b16 v[246:247], v253 offset:14336\n"
"v_mfma_f32_16x16x32_bf16 v[88:91], v[228:231], v[216:219], v[88:91]\n"
"v_mfma_f32_16x16x32_bf16 v[92:95], v[224:227], v[204:207], v[92:95]\n"
"s_add_u32 s75, s40, 2359552\n"
"v_mfma_f32_16x16x32_bf16 v[92:95], v[228:231], v[220:223], v[92:95]\n"
"s_waitcnt vmcnt(39) lgkmcnt(4)\n"
"s_barrier\n"
"v_mfma_f32_16x16x32_bf16 v[96:99], v[232:235], v[192:195], v[96:99]\n"
"ds_read_b64_tr_b16 v[224:225], v248 offset:0\n"
"s_add_u32 m0, s34, 81920\n"
"v_mfma_f32_16x16x32_bf16 v[96:99], v[236:239], v[208:211], v[96:99]\n"
"buffer_load_dwordx4 v20, s[20:23], s75 offen lds\n"
"v_mfma_f32_16x16x32_bf16 v[100:103], v[232:235], v[196:199], v[100:103]\n"
"ds_read_b64_tr_b16 v[226:227], v248 offset:2048\n"
"v_mfma_f32_16x16x32_bf16 v[100:103], v[236:239], v[212:215], v[100:103]\n"
"ds_read_b64_tr_b16 v[228:229], v248 offset:4096\n"
"v_mfma_f32_16x16x32_bf16 v[104:107], v[232:235], v[200:203], v[104:107]\n"
"ds_read_b64_tr_b16 v[230:231], v248 offset:6144\n"
"v_mfma_f32_16x16x32_bf16 v[104:107], v[236:239], v[216:219], v[104:107]\n"
"v_mfma_f32_16x16x32_bf16 v[108:111], v[232:235], v[204:207], v[108:111]\n"
"v_mfma_f32_16x16x32_bf16 v[108:111], v[236:239], v[220:223], v[108:111]\n"
"s_waitcnt lgkmcnt(4)\n"
"v_mfma_f32_16x16x32_bf16 v[112:115], v[240:243], v[192:195], v[112:115]\n"
"ds_read_b64_tr_b16 v[232:233], v248 offset:8192\n"
"v_mfma_f32_16x16x32_bf16 v[112:115], v[244:247], v[208:211], v[112:115]\n"
"buffer_load_dwordx4 v21, s[20:23], s75 offen offset:1024 lds\n"
"v_mfma_f32_16x16x32_bf16 v[116:119], v[240:243], v[196:199], v[116:119]\n"
"ds_read_b64_tr_b16 v[234:235], v248 offset:10240\n"
"v_mfma_f32_16x16x32_bf16 v[116:119], v[244:247], v[212:215], v[116:119]\n"
"ds_read_b64_tr_b16 v[236:237], v248 offset:12288\n"
"v_mfma_f32_16x16x32_bf16 v[120:123], v[240:243], v[200:203], v[120:123]\n"
"ds_read_b64_tr_b16 v[238:239], v248 offset:14336\n"
"v_mfma_f32_16x16x32_bf16 v[120:123], v[244:247], v[216:219], v[120:123]\n"
"v_mfma_f32_16x16x32_bf16 v[124:127], v[240:243], v[204:207], v[124:127]\n"
"v_mfma_f32_16x16x32_bf16 v[124:127], v[244:247], v[220:223], v[124:127]\n"
"s_waitcnt lgkmcnt(4)\n"
"v_mfma_f32_16x16x32_bf16 v[128:131], v[224:227], a[0:3], v[128:131]\n"
"ds_read_b64_tr_b16 v[240:241], v251 offset:0\n"
"v_mfma_f32_16x16x32_bf16 v[128:131], v[228:231], a[16:19], v[128:131]\n"
"buffer_load_dwordx4 v22, s[20:23], s75 offen offset:2048 lds\n"
"v_mfma_f32_16x16x32_bf16 v[132:135], v[224:227], a[4:7], v[132:135]\n"
"ds_read_b64_tr_b16 v[242:243], v251 offset:2048\n"
"v_max_f32 v64, 0, v64\n"
"v_max_f32 v65, 0, v65\n"
"v_mfma_f32_16x16x32_bf16 v[132:135], v[228:231], a[20:23], v[132:135]\n"
"ds_read_b64_tr_b16 v[244:245], v251 offset:4096\n"
"v_max_f32 v66, 0, v66\n"
"v_mfma_f32_16x16x32_bf16 v[136:139], v[224:227], a[8:11], v[136:139]\n"
"ds_read_b64_tr_b16 v[246:247], v251 offset:6144\n"
"v_max_f32 v67, 0, v67\n"
"v_mfma_f32_16x16x32_bf16 v[136:139], v[228:231], a[24:27], v[136:139]\n"
"v_mul_f32 v64, v64, v80\n"
"v_mfma_f32_16x16x32_bf16 v[140:143], v[224:227], a[12:15], v[140:143]\n"
"v_mul_f32 v65, v65, v81\n"
"v_mfma_f32_16x16x32_bf16 v[140:143], v[228:231], a[28:31], v[140:143]\n"
"v_mul_f32 v66, v66, v82\n"
"s_waitcnt lgkmcnt(4)\n"
"v_mfma_f32_16x16x32_bf16 v[144:147], v[232:235], a[0:3], v[144:147]\n"
"ds_read_b64_tr_b16 v[224:225], v251 offset:8192\n"
"v_mul_f32 v67, v67, v83\n"
"v_mfma_f32_16x16x32_bf16 v[144:147], v[236:239], a[16:19], v[144:147]\n"
"buffer_load_dwordx4 v23, s[20:23], s75 offen offset:3072 lds\n"
"v_mfma_f32_16x16x32_bf16 v[148:151], v[232:235], a[4:7], v[148:151]\n"
"ds_read_b64_tr_b16 v[226:227], v251 offset:10240\n"
"v_cvt_pk_bf16_f32 v64, v64, v65\n"
"v_cvt_pk_bf16_f32 v65, v66, v67\n"
"v_mfma_f32_16x16x32_bf16 v[148:151], v[236:239], a[20:23], v[148:151]\n"
"ds_read_b64_tr_b16 v[228:229], v251 offset:12288\n"
"v_max_f32 v96, 0, v96\n"
"v_mfma_f32_16x16x32_bf16 v[152:155], v[232:235], a[8:11], v[152:155]\n"
"ds_read_b64_tr_b16 v[230:231], v251 offset:14336\n"
"v_max_f32 v97, 0, v97\n"
"v_mfma_f32_16x16x32_bf16 v[152:155], v[236:239], a[24:27], v[152:155]\n"
"v_max_f32 v98, 0, v98\n"
"v_mfma_f32_16x16x32_bf16 v[156:159], v[232:235], a[12:15], v[156:159]\n"
"s_add_u32 s35, s40, 590208\n"
"v_max_f32 v99, 0, v99\n"
"v_mfma_f32_16x16x32_bf16 v[156:159], v[236:239], a[28:31], v[156:159]\n"
"s_waitcnt vmcnt(34) lgkmcnt(4)\n"
"s_barrier\n"
"v_mul_f32 v96, v96, v112\n"
"v_mfma_f32_16x16x32_bf16 v[160:163], v[240:243], a[0:3], v[160:163]\n"
"ds_read_b64_tr_b16 v[232:233], v248 offset:16384\n"
"s_add_u32 m0, s34, 98304\n"
"v_mul_f32 v97, v97, v113\n"
"v_mfma_f32_16x16x32_bf16 v[160:163], v[244:247], a[16:19], v[160:163]\n"
"buffer_load_dwordx4 v20, s[20:23], s35 offen lds\n"
"v_mfma_f32_16x16x32_bf16 v[164:167], v[240:243], a[4:7], v[164:167]\n"
"ds_read_b64_tr_b16 v[234:235], v248 offset:18432\n"
"v_mul_f32 v98, v98, v114\n"
"v_mul_f32 v99, v99, v115\n"
"v_mfma_f32_16x16x32_bf16 v[164:167], v[244:247], a[20:23], v[164:167]\n"
"ds_read_b64_tr_b16 v[236:237], v248 offset:20480\n"
"v_cvt_pk_bf16_f32 v66, v96, v97\n"
"v_mfma_f32_16x16x32_bf16 v[168:171], v[240:243], a[8:11], v[168:171]\n"
"ds_read_b64_tr_b16 v[238:239], v248 offset:22528\n"
"v_cvt_pk_bf16_f32 v67, v98, v99\n"
"v_mfma_f32_16x16x32_bf16 v[168:171], v[244:247], a[24:27], v[168:171]\n"
"buffer_store_dwordx4 v[64:67], v8, s[36:39], s49 offen sc1\n"
"v_mfma_f32_16x16x32_bf16 v[172:175], v[240:243], a[12:15], v[172:175]\n"
"v_max_f32 v68, 0, v68\n"
"v_mfma_f32_16x16x32_bf16 v[172:175], v[244:247], a[28:31], v[172:175]\n"
"v_max_f32 v69, 0, v69\n"
"s_waitcnt lgkmcnt(4)\n"
"v_mfma_f32_16x16x32_bf16 v[176:179], v[224:227], a[0:3], v[176:179]\n"
"ds_read_b64_tr_b16 v[240:241], v248 offset:24576\n"
"v_max_f32 v70, 0, v70\n"
"v_mfma_f32_16x16x32_bf16 v[176:179], v[228:231], a[16:19], v[176:179]\n"
"buffer_load_dwordx4 v21, s[20:23], s35 offen offset:1024 lds\n"
"v_mfma_f32_16x16x32_bf16 v[180:183], v[224:227], a[4:7], v[180:183]\n"
"ds_read_b64_tr_b16 v[242:243], v248 offset:26624\n"
"v_max_f32 v71, 0, v71\n"
"v_mfma_f32_16x16x32_bf16 v[180:183], v[228:231], a[20:23], v[180:183]\n"
"ds_read_b64_tr_b16 v[244:245], v248 offset:28672\n"
"v_mul_f32 v68, v68, v84\n"
"v_mfma_f32_16x16x32_bf16 v[184:187], v[224:227], a[8:11], v[184:187]\n"
"ds_read_b64_tr_b16 v[246:247], v248 offset:30720\n"
"v_mul_f32 v69, v69, v85\n"
"v_mfma_f32_16x16x32_bf16 v[184:187], v[228:231], a[24:27], v[184:187]\n"
"buffer_load_dwordx4 v22, s[20:23], s35 offen offset:2048 lds\n"
"v_mfma_f32_16x16x32_bf16 v[188:191], v[224:227], a[12:15], v[188:191]\n"
"v_mul_f32 v70, v70, v86\n"
"v_mul_f32 v71, v71, v87\n"
"v_mfma_f32_16x16x32_bf16 v[188:191], v[228:231], a[28:31], v[188:191]\n"
"v_cvt_pk_bf16_f32 v68, v68, v69\n"
"s_waitcnt lgkmcnt(4)\n"
"v_mfma_f32_16x16x32_bf16 v[128:131], v[232:235], a[32:35], v[128:131]\n"
"ds_read_b64_tr_b16 v[224:225], v251 offset:16384\n"
"v_cvt_pk_bf16_f32 v69, v70, v71\n"
"v_mfma_f32_16x16x32_bf16 v[128:131], v[236:239], a[48:51], v[128:131]\n"
"buffer_load_dwordx4 v[24:27], off, s[36:39], s59 offset:16 sc1\n"
"v_mfma_f32_16x16x32_bf16 v[132:135], v[232:235], a[36:39], v[132:135]\n"
"ds_read_b64_tr_b16 v[226:227], v251 offset:18432\n"
"v_max_f32 v100, 0, v100\n"
"v_mfma_f32_16x16x32_bf16 v[132:135], v[236:239], a[52:55], v[132:135]\n"
"ds_read_b64_tr_b16 v[228:229], v251 offset:20480\n"
"v_max_f32 v101, 0, v101\n"
"v_mfma_f32_16x16x32_bf16 v[136:139], v[232:235], a[40:43], v[136:139]\n"
"ds_read_b64_tr_b16 v[230:231], v251 offset:22528\n"
"v_max_f32 v102, 0, v102\n"
"v_mfma_f32_16x16x32_bf16 v[136:139], v[236:239], a[56:59], v[136:139]\n"
"buffer_load_dwordx4 v23, s[20:23], s35 offen offset:3072 lds\n"
"v_mfma_f32_16x16x32_bf16 v[140:143], v[232:235], a[44:47], v[140:143]\n"
"v_max_f32 v103, 0, v103\n"
"v_mul_f32 v100, v100, v116\n"
"v_mfma_f32_16x16x32_bf16 v[140:143], v[236:239], a[60:63], v[140:143]\n"
"v_mul_f32 v101, v101, v117\n"
"s_waitcnt lgkmcnt(4)\n"
"v_mfma_f32_16x16x32_bf16 v[144:147], v[240:243], a[32:35], v[144:147]\n"
"ds_read_b64_tr_b16 v[232:233], v251 offset:24576\n"
"v_mul_f32 v102, v102, v118\n"
"v_mfma_f32_16x16x32_bf16 v[144:147], v[244:247], a[48:51], v[144:147]\n"
"ds_read_b64_tr_b16 v[234:235], v251 offset:26624\n"
"v_mul_f32 v103, v103, v119\n"
"v_mfma_f32_16x16x32_bf16 v[148:151], v[240:243], a[36:39], v[148:151]\n"
"ds_read_b64_tr_b16 v[236:237], v251 offset:28672\n"
"v_cvt_pk_bf16_f32 v70, v100, v101\n"
"v_mfma_f32_16x16x32_bf16 v[148:151], v[244:247], a[52:55], v[148:151]\n"
"ds_read_b64_tr_b16 v[238:239], v251 offset:30720\n"
"v_cvt_pk_bf16_f32 v71, v102, v103\n"
"v_mfma_f32_16x16x32_bf16 v[152:155], v[240:243], a[40:43], v[152:155]\n"
"v_mfma_f32_16x16x32_bf16 v[152:155], v[244:247], a[56:59], v[152:155]\n"
"buffer_store_dwordx4 v[68:71], v8, s[36:39], s49 offen offset:1024 sc1\n"
"v_mfma_f32_16x16x32_bf16 v[156:159], v[240:243], a[44:47], v[156:159]\n"
"s_add_u32 s75, s40, 2949376\n"
"v_max_f32 v72, 0, v72\n"
"v_mfma_f32_16x16x32_bf16 v[156:159], v[244:247], a[60:63], v[156:159]\n"
"s_waitcnt vmcnt(32) lgkmcnt(4)\n"
"s_barrier\n"
"v_mfma_f32_16x16x32_bf16 v[160:163], v[224:227], a[32:35], v[160:163]\n"
"ds_read_b64_tr_b16 v[240:241], v248 offset:32768\n"
"s_add_u32 m0, s34, 114688\n"
"v_mfma_f32_16x16x32_bf16 v[160:163], v[228:231], a[48:51], v[160:163]\n"
"buffer_load_dwordx4 v20, s[20:23], s75 offen lds\n"
"v_mfma_f32_16x16x32_bf16 v[164:167], v[224:227], a[36:39], v[164:167]\n"
"ds_read_b64_tr_b16 v[242:243], v248 offset:34816\n"
"v_max_f32 v73, 0, v73\n"
"v_mfma_f32_16x16x32_bf16 v[164:167], v[228:231], a[52:55], v[164:167]\n"
"ds_read_b64_tr_b16 v[244:245], v248 offset:36864\n"
"v_max_f32 v74, 0, v74\n"
"v_mfma_f32_16x16x32_bf16 v[168:171], v[224:227], a[40:43], v[168:171]\n"
"ds_read_b64_tr_b16 v[246:247], v248 offset:38912\n"
"v_max_f32 v75, 0, v75\n"
"v_mfma_f32_16x16x32_bf16 v[168:171], v[228:231], a[56:59], v[168:171]\n"
"buffer_load_dwordx4 v21, s[20:23], s75 offen offset:1024 lds\n"
"v_mfma_f32_16x16x32_bf16 v[172:175], v[224:227], a[44:47], v[172:175]\n"
"v_mul_f32 v72, v72, v88\n"
"v_mul_f32 v73, v73, v89\n"
"v_mfma_f32_16x16x32_bf16 v[172:175], v[228:231], a[60:63], v[172:175]\n"
"v_mul_f32 v74, v74, v90\n"
"s_waitcnt lgkmcnt(4)\n"
"v_mfma_f32_16x16x32_bf16 v[176:179], v[232:235], a[32:35], v[176:179]\n"
"ds_read_b64_tr_b16 v[224:225], v248 offset:40960\n"
"v_mul_f32 v75, v75, v91\n"
"v_mfma_f32_16x16x32_bf16 v[176:179], v[236:239], a[48:51], v[176:179]\n"
"ds_read_b64_tr_b16 v[226:227], v248 offset:43008\n"
"v_cvt_pk_bf16_f32 v72, v72, v73\n"
"v_mfma_f32_16x16x32_bf16 v[180:183], v[232:235], a[36:39], v[180:183]\n"
"ds_read_b64_tr_b16 v[228:229], v248 offset:45056\n"
"v_cvt_pk_bf16_f32 v73, v74, v75\n"
"v_mfma_f32_16x16x32_bf16 v[180:183], v[236:239], a[52:55], v[180:183]\n"
"ds_read_b64_tr_b16 v[230:231], v248 offset:47104\n"
"v_max_f32 v104, 0, v104\n"
"v_mfma_f32_16x16x32_bf16 v[184:187], v[232:235], a[40:43], v[184:187]\n"
"v_max_f32 v105, 0, v105\n"
"v_max_f32 v106, 0, v106\n"
"v_mfma_f32_16x16x32_bf16 v[184:187], v[236:239], a[56:59], v[184:187]\n"
"buffer_load_dwordx4 v22, s[20:23], s75 offen offset:2048 lds\n"
"v_mfma_f32_16x16x32_bf16 v[188:191], v[232:235], a[44:47], v[188:191]\n"
"v_max_f32 v107, 0, v107\n"
"v_mul_f32 v104, v104, v120\n"
"v_mfma_f32_16x16x32_bf16 v[188:191], v[236:239], a[60:63], v[188:191]\n"
"v_mul_f32 v105, v105, v121\n"
"s_waitcnt lgkmcnt(4)\n"
"v_mfma_f32_16x16x32_bf16 v[128:131], v[240:243], a[64:67], v[128:131]\n"
"ds_read_b64_tr_b16 v[232:233], v251 offset:32768\n"
"v_mul_f32 v106, v106, v122\n"
"v_mfma_f32_16x16x32_bf16 v[128:131], v[244:247], a[80:83], v[128:131]\n"
"ds_read_b64_tr_b16 v[234:235], v251 offset:34816\n"
"v_mul_f32 v107, v107, v123\n"
"v_mfma_f32_16x16x32_bf16 v[132:135], v[240:243], a[68:71], v[132:135]\n"
"ds_read_b64_tr_b16 v[236:237], v251 offset:36864\n"
"v_cvt_pk_bf16_f32 v74, v104, v105\n"
"v_mfma_f32_16x16x32_bf16 v[132:135], v[244:247], a[84:87], v[132:135]\n"
"ds_read_b64_tr_b16 v[238:239], v251 offset:38912\n"
"v_cvt_pk_bf16_f32 v75, v106, v107\n"
"v_mfma_f32_16x16x32_bf16 v[136:139], v[240:243], a[72:75], v[136:139]\n"
"v_mfma_f32_16x16x32_bf16 v[136:139], v[244:247], a[88:91], v[136:139]\n"
"buffer_store_dwordx4 v[72:75], v8, s[36:39], s49 offen offset:2048 sc1\n"
"v_mfma_f32_16x16x32_bf16 v[140:143], v[240:243], a[76:79], v[140:143]\n"
"v_max_f32 v76, 0, v76\n"
"v_max_f32 v77, 0, v77\n"
"v_mfma_f32_16x16x32_bf16 v[140:143], v[244:247], a[92:95], v[140:143]\n"
"v_max_f32 v78, 0, v78\n"
"s_waitcnt lgkmcnt(4)\n"
"v_mfma_f32_16x16x32_bf16 v[144:147], v[224:227], a[64:67], v[144:147]\n"
"ds_read_b64_tr_b16 v[240:241], v251 offset:40960\n"
"v_max_f32 v79, 0, v79\n"
"v_mfma_f32_16x16x32_bf16 v[144:147], v[228:231], a[80:83], v[144:147]\n"
"buffer_load_dwordx4 v23, s[20:23], s75 offen offset:3072 lds\n"
"v_mfma_f32_16x16x32_bf16 v[148:151], v[224:227], a[68:71], v[148:151]\n"
"ds_read_b64_tr_b16 v[242:243], v251 offset:43008\n"
"v_mul_f32 v76, v76, v92\n"
"v_mul_f32 v77, v77, v93\n"
"v_mfma_f32_16x16x32_bf16 v[148:151], v[228:231], a[84:87], v[148:151]\n"
"ds_read_b64_tr_b16 v[244:245], v251 offset:45056\n"
"v_mul_f32 v78, v78, v94\n"
"v_mfma_f32_16x16x32_bf16 v[152:155], v[224:227], a[72:75], v[152:155]\n"
"ds_read_b64_tr_b16 v[246:247], v251 offset:47104\n"
"v_mul_f32 v79, v79, v95\n"
"v_mfma_f32_16x16x32_bf16 v[152:155], v[228:231], a[88:91], v[152:155]\n"
"v_cvt_pk_bf16_f32 v76, v76, v77\n"
"v_mfma_f32_16x16x32_bf16 v[156:159], v[224:227], a[76:79], v[156:159]\n"
"s_add_u32 s35, s40, 1180032\n"
"v_cvt_pk_bf16_f32 v77, v78, v79\n"
"v_mfma_f32_16x16x32_bf16 v[156:159], v[228:231], a[92:95], v[156:159]\n"
"s_waitcnt vmcnt(28) lgkmcnt(4)\n"
"s_barrier\n"
"v_max_f32 v108, 0, v108\n"
"v_mfma_f32_16x16x32_bf16 v[160:163], v[232:235], a[64:67], v[160:163]\n"
"ds_read_b64_tr_b16 v[224:225], v248 offset:49152\n"
"s_add_u32 m0, s34, 131072\n"
"v_max_f32 v109, 0, v109\n"
"v_mfma_f32_16x16x32_bf16 v[160:163], v[236:239], a[80:83], v[160:163]\n"
"buffer_load_dwordx4 v20, s[20:23], s35 offen lds\n"
"v_mfma_f32_16x16x32_bf16 v[164:167], v[232:235], a[68:71], v[164:167]\n"
"ds_read_b64_tr_b16 v[226:227], v248 offset:51200\n"
"v_max_f32 v110, 0, v110\n"
"v_max_f32 v111, 0, v111\n"
"v_mfma_f32_16x16x32_bf16 v[164:167], v[236:239], a[84:87], v[164:167]\n"
"ds_read_b64_tr_b16 v[228:229], v248 offset:53248\n"
"v_mul_f32 v108, v108, v124\n"
"v_mfma_f32_16x16x32_bf16 v[168:171], v[232:235], a[72:75], v[168:171]\n"
"ds_read_b64_tr_b16 v[230:231], v248 offset:55296\n"
"v_mul_f32 v109, v109, v125\n"
"v_mfma_f32_16x16x32_bf16 v[168:171], v[236:239], a[88:91], v[168:171]\n"
"v_mul_f32 v110, v110, v126\n"
"v_mfma_f32_16x16x32_bf16 v[172:175], v[232:235], a[76:79], v[172:175]\n"
"v_mul_f32 v111, v111, v127\n"
"v_mfma_f32_16x16x32_bf16 v[172:175], v[236:239], a[92:95], v[172:175]\n"
"v_cvt_pk_bf16_f32 v78, v108, v109\n"
"s_waitcnt lgkmcnt(4)\n"
"v_mfma_f32_16x16x32_bf16 v[176:179], v[240:243], a[64:67], v[176:179]\n"
"ds_read_b64_tr_b16 v[232:233], v248 offset:57344\n"
"v_cvt_pk_bf16_f32 v79, v110, v111\n"
"v_mfma_f32_16x16x32_bf16 v[176:179], v[244:247], a[80:83], v[176:179]\n"
"buffer_store_dwordx4 v[76:79], v8, s[36:39], s49 offen offset:3072 sc1\n"
"v_mfma_f32_16x16x32_bf16 v[180:183], v[240:243], a[68:71], v[180:183]\n"
"ds_read_b64_tr_b16 v[234:235], v248 offset:59392\n"
"s_add_u32 s49, s49, 16384\n"
"v_mfma_f32_16x16x32_bf16 v[180:183], v[244:247], a[84:87], v[180:183]\n"
"ds_read_b64_tr_b16 v[236:237], v248 offset:61440\n"
"v_mfma_f32_16x16x32_bf16 v[184:187], v[240:243], a[72:75], v[184:187]\n"
"ds_read_b64_tr_b16 v[238:239], v248 offset:63488\n"
"ds_read_b64 v[48:49], v47 offset:1536\n"
"v_mfma_f32_16x16x32_bf16 v[184:187], v[244:247], a[88:91], v[184:187]\n"
"buffer_load_dwordx4 v21, s[20:23], s35 offen offset:1024 lds\n"
"v_mfma_f32_16x16x32_bf16 v[188:191], v[240:243], a[76:79], v[188:191]\n"
"ds_read_b64 v[50:51], v47 offset:3584\n"
"ds_read_b64 v[52:53], v47 offset:1664\n"
"v_mfma_f32_16x16x32_bf16 v[188:191], v[244:247], a[92:95], v[188:191]\n"
"ds_read_b64 v[54:55], v47 offset:3712\n"
"s_waitcnt lgkmcnt(8)\n"
"v_mfma_f32_16x16x32_bf16 v[128:131], v[224:227], a[96:99], v[128:131]\n"
"ds_read_b64_tr_b16 v[240:241], v251 offset:49152\n"
"v_mfma_f32_16x16x32_bf16 v[128:131], v[228:231], a[112:115], v[128:131]\n"
"ds_read_b64_tr_b16 v[242:243], v251 offset:51200\n"
"v_mfma_f32_16x16x32_bf16 v[132:135], v[224:227], a[100:103], v[132:135]\n"
"ds_read_b64_tr_b16 v[244:245], v251 offset:53248\n"
"v_mfma_f32_16x16x32_bf16 v[132:135], v[228:231], a[116:119], v[132:135]\n"
"ds_read_b64_tr_b16 v[246:247], v251 offset:55296\n"
"v_mfma_f32_16x16x32_bf16 v[136:139], v[224:227], a[104:107], v[136:139]\n"
"v_mfma_f32_16x16x32_bf16 v[136:139], v[228:231], a[120:123], v[136:139]\n"
"buffer_load_dwordx4 v22, s[20:23], s35 offen offset:2048 lds\n"
"v_mfma_f32_16x16x32_bf16 v[140:143], v[224:227], a[108:111], v[140:143]\n"
"v_mfma_f32_16x16x32_bf16 v[140:143], v[228:231], a[124:127], v[140:143]\n"
"s_waitcnt vmcnt(11)\n"
"v_xor_b32 v24, s86, v24\n"
"s_waitcnt lgkmcnt(4)\n"
"v_mfma_f32_16x16x4_4b_bf16 v[64:79], v[48:49], v[56:57], 0\n"
"v_mfma_f32_16x16x4_4b_bf16 v[80:95], v[50:51], v[56:57], 0\n"
"v_bitop3_b32 v24, v24, v25, s86 bitop3:0xf6\n"
"v_mfma_f32_16x16x4_4b_bf16 v[96:111], v[52:53], v[56:57], 0\n"
"buffer_load_dwordx4 v23, s[20:23], s35 offen offset:3072 lds\n"
"v_mfma_f32_16x16x4_4b_bf16 v[112:127], v[54:55], v[56:57], 0\n"
"v_bitop3_b32 v24, v24, v26, s86 bitop3:0xf6\n"
"v_mfma_f32_16x16x32_bf16 v[144:147], v[232:235], a[96:99], v[144:147]\n"
"ds_read_b64_tr_b16 v[224:225], v251 offset:57344\n"
"v_mfma_f32_16x16x32_bf16 v[144:147], v[236:239], a[112:115], v[144:147]\n"
"ds_read_b64_tr_b16 v[226:227], v251 offset:59392\n"
"v_bitop3_b32 v24, v24, v27, s86 bitop3:0xf6\n"
"v_mfma_f32_16x16x32_bf16 v[148:151], v[232:235], a[100:103], v[148:151]\n"
"ds_read_b64_tr_b16 v[228:229], v251 offset:61440\n"
"v_mfma_f32_16x16x32_bf16 v[148:151], v[236:239], a[116:119], v[148:151]\n"
"ds_read_b64_tr_b16 v[230:231], v251 offset:63488\n"
"v_cmp_ne_u32 vcc, 0, v24\n"
"v_mfma_f32_16x16x32_bf16 v[152:155], v[232:235], a[104:107], v[152:155]\n"
"v_mfma_f32_16x16x32_bf16 v[152:155], v[236:239], a[120:123], v[152:155]\n"
"s_cbranch_vccnz SLOW1_%=\n"
"BACK1_%=:\n"
"v_mfma_f32_16x16x32_bf16 v[156:159], v[232:235], a[108:111], v[156:159]\n"
"s_add_u32 s75, s40, 3539200\n"
"v_mfma_f32_16x16x32_bf16 v[156:159], v[236:239], a[124:127], v[156:159]\n"
"s_waitcnt lgkmcnt(4)\n"
"s_barrier\n"
"v_mfma_f32_16x16x32_bf16 v[160:163], v[240:243], a[96:99], v[160:163]\n"
"ds_read_b64_tr_b16 v[232:233], v249 offset:0\n"
"s_add_u32 m0, s34, 0\n"
"v_mfma_f32_16x16x32_bf16 v[160:163], v[244:247], a[112:115], v[160:163]\n"
"buffer_load_dwordx4 v20, s[20:23], s75 offen lds\n"
"v_mfma_f32_16x16x32_bf16 v[164:167], v[240:243], a[100:103], v[164:167]\n"
"ds_read_b64_tr_b16 v[234:235], v249 offset:2048\n"
"v_mfma_f32_16x16x32_bf16 v[164:167], v[244:247], a[116:119], v[164:167]\n"
"ds_read_b64_tr_b16 v[236:237], v249 offset:4096\n"
"v_mfma_f32_16x16x32_bf16 v[168:171], v[240:243], a[104:107], v[168:171]\n"
"ds_read_b64_tr_b16 v[238:239], v249 offset:6144\n"
"v_mfma_f32_16x16x32_bf16 v[168:171], v[244:247], a[120:123], v[168:171]\n"
"v_mfma_f32_16x16x32_bf16 v[172:175], v[240:243], a[108:111], v[172:175]\n"
"v_mfma_f32_16x16x32_bf16 v[172:175], v[244:247], a[124:127], v[172:175]\n"
"s_waitcnt lgkmcnt(4)\n"
"v_mfma_f32_16x16x32_bf16 v[176:179], v[224:227], a[96:99], v[176:179]\n"
"ds_read_b64_tr_b16 v[240:241], v249 offset:8192\n"
"v_mfma_f32_16x16x32_bf16 v[176:179], v[228:231], a[112:115], v[176:179]\n"
"buffer_load_dwordx4 v21, s[20:23], s75 offen offset:1024 lds\n"
"v_mfma_f32_16x16x32_bf16 v[180:183], v[224:227], a[100:103], v[180:183]\n"
"ds_read_b64_tr_b16 v[242:243], v249 offset:10240\n"
"v_mfma_f32_16x16x32_bf16 v[180:183], v[228:231], a[116:119], v[180:183]\n"
"ds_read_b64_tr_b16 v[244:245], v249 offset:12288\n"
"v_mfma_f32_16x16x32_bf16 v[184:187], v[224:227], a[104:107], v[184:187]\n"
"ds_read_b64_tr_b16 v[246:247], v249 offset:14336\n"
"v_mfma_f32_16x16x32_bf16 v[184:187], v[228:231], a[120:123], v[184:187]\n"
"v_mfma_f32_16x16x32_bf16 v[188:191], v[224:227], a[108:111], v[188:191]\n"
"v_mfma_f32_16x16x32_bf16 v[188:191], v[228:231], a[124:127], v[188:191]\n"
"s_waitcnt lgkmcnt(4)\n"
"v_mfma_f32_16x16x32_bf16 v[64:67], v[232:235], a[0:3], v[64:67]\n"
"ds_read_b64_tr_b16 v[224:225], v252 offset:0\n"
"v_mfma_f32_16x16x32_bf16 v[64:67], v[236:239], a[16:19], v[64:67]\n"
"buffer_load_dwordx4 v22, s[20:23], s75 offen offset:2048 lds\n"
"v_mfma_f32_16x16x32_bf16 v[68:71], v[232:235], a[4:7], v[68:71]\n"
"ds_read_b64_tr_b16 v[226:227], v252 offset:2048\n"
"v_mfma_f32_16x16x32_bf16 v[68:71], v[236:239], a[20:23], v[68:71]\n"
"ds_read_b64_tr_b16 v[228:229], v252 offset:4096\n"
"v_mfma_f32_16x16x32_bf16 v[72:75], v[232:235], a[8:11], v[72:75]\n"
"ds_read_b64_tr_b16 v[230:231], v252 offset:6144\n"
"v_mfma_f32_16x16x32_bf16 v[72:75], v[236:239], a[24:27], v[72:75]\n"
"v_mfma_f32_16x16x32_bf16 v[76:79], v[232:235], a[12:15], v[76:79]\n"
"v_mfma_f32_16x16x32_bf16 v[76:79], v[236:239], a[28:31], v[76:79]\n"
"s_waitcnt lgkmcnt(4)\n"
"v_mfma_f32_16x16x32_bf16 v[80:83], v[240:243], a[0:3], v[80:83]\n"
"ds_read_b64_tr_b16 v[232:233], v252 offset:8192\n"
"v_mfma_f32_16x16x32_bf16 v[80:83], v[244:247], a[16:19], v[80:83]\n"
"buffer_load_dwordx4 v23, s[20:23], s75 offen offset:3072 lds\n"
"v_mfma_f32_16x16x32_bf16 v[84:87], v[240:243], a[4:7], v[84:87]\n"
"ds_read_b64_tr_b16 v[234:235], v252 offset:10240\n"
"v_mfma_f32_16x16x32_bf16 v[84:87], v[244:247], a[20:23], v[84:87]\n"
"ds_read_b64_tr_b16 v[236:237], v252 offset:12288\n"
"v_mfma_f32_16x16x32_bf16 v[88:91], v[240:243], a[8:11], v[88:91]\n"
"ds_read_b64_tr_b16 v[238:239], v252 offset:14336\n"
"v_mfma_f32_16x16x32_bf16 v[88:91], v[244:247], a[24:27], v[88:91]\n"
"v_mfma_f32_16x16x32_bf16 v[92:95], v[240:243], a[12:15], v[92:95]\n"
"s_add_u32 s35, s40, 1769856\n"
"v_mfma_f32_16x16x32_bf16 v[92:95], v[244:247], a[28:31], v[92:95]\n"
"s_waitcnt lgkmcnt(4)\n"
"s_barrier\n"
"v_mfma_f32_16x16x32_bf16 v[96:99], v[224:227], a[0:3], v[96:99]\n"
"ds_read_b64_tr_b16 v[240:241], v249 offset:16384\n"
"s_add_u32 m0, s34, 16384\n"
"v_mfma_f32_16x16x32_bf16 v[96:99], v[228:231], a[16:19], v[96:99]\n"
"buffer_load_dwordx4 v20, s[20:23], s35 offen lds\n"
"v_mfma_f32_16x16x32_bf16 v[100:103], v[224:227], a[4:7], v[100:103]\n"
"ds_read_b64_tr_b16 v[242:243], v249 offset:18432\n"
"v_mfma_f32_16x16x32_bf16 v[100:103], v[228:231], a[20:23], v[100:103]\n"
"ds_read_b64_tr_b16 v[244:245], v249 offset:20480\n"
"v_mfma_f32_16x16x32_bf16 v[104:107], v[224:227], a[8:11], v[104:107]\n"
"ds_read_b64_tr_b16 v[246:247], v249 offset:22528\n"
"v_mfma_f32_16x16x32_bf16 v[104:107], v[228:231], a[24:27], v[104:107]\n"
"v_mfma_f32_16x16x32_bf16 v[108:111], v[224:227], a[12:15], v[108:111]\n"
"v_mfma_f32_16x16x32_bf16 v[108:111], v[228:231], a[28:31], v[108:111]\n"
"s_waitcnt lgkmcnt(4)\n"
"v_mfma_f32_16x16x32_bf16 v[112:115], v[232:235], a[0:3], v[112:115]\n"
"ds_read_b64_tr_b16 v[224:225], v249 offset:24576\n"
"v_mfma_f32_16x16x32_bf16 v[112:115], v[236:239], a[16:19], v[112:115]\n"
"buffer_load_dwordx4 v21, s[20:23], s35 offen offset:1024 lds\n"
"v_mfma_f32_16x16x32_bf16 v[116:119], v[232:235], a[4:7], v[116:119]\n"
"ds_read_b64_tr_b16 v[226:227], v249 offset:26624\n"
"v_mfma_f32_16x16x32_bf16 v[116:119], v[236:239], a[20:23], v[116:119]\n"
"ds_read_b64_tr_b16 v[228:229], v249 offset:28672\n"
"v_mfma_f32_16x16x32_bf16 v[120:123], v[232:235], a[8:11], v[120:123]\n"
"ds_read_b64_tr_b16 v[230:231], v249 offset:30720\n"
"v_mfma_f32_16x16x32_bf16 v[120:123], v[236:239], a[24:27], v[120:123]\n"
"v_mfma_f32_16x16x32_bf16 v[124:127], v[232:235], a[12:15], v[124:127]\n"
"v_mfma_f32_16x16x32_bf16 v[124:127], v[236:239], a[28:31], v[124:127]\n"
"s_waitcnt lgkmcnt(4)\n"
"v_mfma_f32_16x16x32_bf16 v[128:131], v[240:243], a[128:131], v[128:131]\n"
"ds_read_b64_tr_b16 v[232:233], v252 offset:16384\n"
"v_mfma_f32_16x16x32_bf16 v[128:131], v[244:247], a[144:147], v[128:131]\n"
"buffer_load_dwordx4 v22, s[20:23], s35 offen offset:2048 lds\n"
"v_mfma_f32_16x16x32_bf16 v[132:135], v[240:243], a[132:135], v[132:135]\n"
"ds_read_b64_tr_b16 v[234:235], v252 offset:18432\n"
"v_mfma_f32_16x16x32_bf16 v[132:135], v[244:247], a[148:151], v[132:135]\n"
"ds_read_b64_tr_b16 v[236:237], v252 offset:20480\n"
"v_mfma_f32_16x16x32_bf16 v[136:139], v[240:243], a[136:139], v[136:139]\n"
"ds_read_b64_tr_b16 v[238:239], v252 offset:22528\n"
"v_mfma_f32_16x16x32_bf16 v[136:139], v[244:247], a[152:155], v[136:139]\n"
"v_mfma_f32_16x16x32_bf16 v[140:143], v[240:243], a[140:143], v[140:143]\n"
"v_mfma_f32_16x16x32_bf16 v[140:143], v[244:247], a[156:159], v[140:143]\n"
"s_waitcnt lgkmcnt(4)\n"
"v_mfma_f32_16x16x32_bf16 v[144:147], v[224:227], a[128:131], v[144:147]\n"
"ds_read_b64_tr_b16 v[240:241], v252 offset:24576\n"
"v_mfma_f32_16x16x32_bf16 v[144:147], v[228:231], a[144:147], v[144:147]\n"
"buffer_load_dwordx4 v23, s[20:23], s35 offen offset:3072 lds\n"
"v_mfma_f32_16x16x32_bf16 v[148:151], v[224:227], a[132:135], v[148:151]\n"
"ds_read_b64_tr_b16 v[242:243], v252 offset:26624\n"
"v_mfma_f32_16x16x32_bf16 v[148:151], v[228:231], a[148:151], v[148:151]\n"
"ds_read_b64_tr_b16 v[244:245], v252 offset:28672\n"
"v_mfma_f32_16x16x32_bf16 v[152:155], v[224:227], a[136:139], v[152:155]\n"
"ds_read_b64_tr_b16 v[246:247], v252 offset:30720\n"
"s_add_u32 s58, s57, 0\n"
"v_mfma_f32_16x16x32_bf16 v[152:155], v[228:231], a[152:155], v[152:155]\n"
"buffer_load_dwordx4 a[0:3], v13, s[36:39], s58 offen sc1\n"
"v_mfma_f32_16x16x32_bf16 v[156:159], v[224:227], a[140:143], v[156:159]\n"
"v_mfma_f32_16x16x32_bf16 v[156:159], v[228:231], a[156:159], v[156:159]\n"
"s_waitcnt vmcnt(12) lgkmcnt(4)\n"
"s_barrier\n"
"v_mfma_f32_16x16x32_bf16 v[160:163], v[232:235], a[128:131], v[160:163]\n"
"ds_read_b64_tr_b16 v[224:225], v249 offset:32768\n"
"v_mfma_f32_16x16x32_bf16 v[160:163], v[236:239], a[144:147], v[160:163]\n"
"buffer_store_dword v19, v18, s[36:39], 0 offen offset:32 sc1\n"
"v_mfma_f32_16x16x32_bf16 v[164:167], v[232:235], a[132:135], v[164:167]\n"
"ds_read_b64_tr_b16 v[226:227], v249 offset:34816\n"
"v_mfma_f32_16x16x32_bf16 v[164:167], v[236:239], a[148:151], v[164:167]\n"
"ds_read_b64_tr_b16 v[228:229], v249 offset:36864\n"
"s_add_u32 s75, s40, 4129024\n"
"v_mfma_f32_16x16x32_bf16 v[168:171], v[232:235], a[136:139], v[168:171]\n"
"ds_read_b64_tr_b16 v[230:231], v249 offset:38912\n"
"s_add_u32 m0, s34, 32768\n"
"v_mfma_f32_16x16x32_bf16 v[168:171], v[236:239], a[152:155], v[168:171]\n"
"buffer_load_dwordx4 v20, s[20:23], s75 offen lds\n"
"v_mfma_f32_16x16x32_bf16 v[172:175], v[232:235], a[140:143], v[172:175]\n"
"v_mfma_f32_16x16x32_bf16 v[172:175], v[236:239], a[156:159], v[172:175]\n"
"s_waitcnt lgkmcnt(4)\n"
"v_mfma_f32_16x16x32_bf16 v[176:179], v[240:243], a[128:131], v[176:179]\n"
"ds_read_b64_tr_b16 v[232:233], v249 offset:40960\n"
"v_mfma_f32_16x16x32_bf16 v[176:179], v[244:247], a[144:147], v[176:179]\n"
"buffer_load_dwordx4 v21, s[20:23], s75 offen offset:1024 lds\n"
"v_mfma_f32_16x16x32_bf16 v[180:183], v[240:243], a[132:135], v[180:183]\n"
"ds_read_b64_tr_b16 v[234:235], v249 offset:43008\n"
"v_mfma_f32_16x16x32_bf16 v[180:183], v[244:247], a[148:151], v[180:183]\n"
"ds_read_b64_tr_b16 v[236:237], v249 offset:45056\n"
"v_mfma_f32_16x16x32_bf16 v[184:187], v[240:243], a[136:139], v[184:187]\n"
"ds_read_b64_tr_b16 v[238:239], v249 offset:47104\n"
"v_mfma_f32_16x16x32_bf16 v[184:187], v[244:247], a[152:155], v[184:187]\n"
"buffer_load_dwordx4 a[4:7], v13, s[36:39], s58 offen offset:1024 sc1\n"
"v_mfma_f32_16x16x32_bf16 v[188:191], v[240:243], a[140:143], v[188:191]\n"
"v_mfma_f32_16x16x32_bf16 v[188:191], v[244:247], a[156:159], v[188:191]\n"
"s_waitcnt lgkmcnt(4)\n"
"v_mfma_f32_16x16x32_bf16 v[64:67], v[224:227], a[32:35], v[64:67]\n"
"ds_read_b64_tr_b16 v[240:241], v252 offset:32768\n"
"v_mfma_f32_16x16x32_bf16 v[64:67], v[228:231], a[48:51], v[64:67]\n"
"buffer_load_dwordx4 v22, s[20:23], s75 offen offset:2048 lds\n"
"v_mfma_f32_16x16x32_bf16 v[68:71], v[224:227], a[36:39], v[68:71]\n"
"ds_read_b64_tr_b16 v[242:243], v252 offset:34816\n"
"v_mfma_f32_16x16x32_bf16 v[68:71], v[228:231], a[52:55], v[68:71]\n"
"ds_read_b64_tr_b16 v[244:245], v252 offset:36864\n"
"v_mfma_f32_16x16x32_bf16 v[72:75], v[224:227], a[40:43], v[72:75]\n"
"ds_read_b64_tr_b16 v[246:247], v252 offset:38912\n"
"v_mfma_f32_16x16x32_bf16 v[72:75], v[228:231], a[56:59], v[72:75]\n"
"buffer_load_dwordx4 a[8:11], v14, s[36:39], s58 offen sc1\n"
"v_mfma_f32_16x16x32_bf16 v[76:79], v[224:227], a[44:47], v[76:79]\n"
"v_mfma_f32_16x16x32_bf16 v[76:79], v[228:231], a[60:63], v[76:79]\n"
"s_waitcnt lgkmcnt(4)\n"
"v_mfma_f32_16x16x32_bf16 v[80:83], v[232:235], a[32:35], v[80:83]\n"
"ds_read_b64_tr_b16 v[224:225], v252 offset:40960\n"
"v_mfma_f32_16x16x32_bf16 v[80:83], v[236:239], a[48:51], v[80:83]\n"
"buffer_load_dwordx4 v23, s[20:23], s75 offen offset:3072 lds\n"
"v_mfma_f32_16x16x32_bf16 v[84:87], v[232:235], a[36:39], v[84:87]\n"
"ds_read_b64_tr_b16 v[226:227], v252 offset:43008\n"
"v_mfma_f32_16x16x32_bf16 v[84:87], v[236:239], a[52:55], v[84:87]\n"
"ds_read_b64_tr_b16 v[228:229], v252 offset:45056\n"
"v_mfma_f32_16x16x32_bf16 v[88:91], v[232:235], a[40:43], v[88:91]\n"
"ds_read_b64_tr_b16 v[230:231], v252 offset:47104\n"
"v_mfma_f32_16x16x32_bf16 v[88:91], v[236:239], a[56:59], v[88:91]\n"
"buffer_load_dwordx4 a[12:15], v14, s[36:39], s58 offen offset:1024 sc1\n"
"v_mfma_f32_16x16x32_bf16 v[92:95], v[232:235], a[44:47], v[92:95]\n"
"s_add_u32 s35, s40, 2359680\n"
"v_mfma_f32_16x16x32_bf16 v[92:95], v[236:239], a[60:63], v[92:95]\n"
"s_waitcnt lgkmcnt(4)\n"
"s_barrier\n"
"v_mfma_f32_16x16x32_bf16 v[96:99], v[240:243], a[32:35], v[96:99]\n"
"ds_read_b64_tr_b16 v[232:233], v249 offset:49152\n"
"s_add_u32 m0, s34, 49152\n"
"v_mfma_f32_16x16x32_bf16 v[96:99], v[244:247], a[48:51], v[96:99]\n"
"buffer_load_dwordx4 v20, s[20:23], s35 offen lds\n"
"v_mfma_f32_16x16x32_bf16 v[100:103], v[240:243], a[36:39], v[100:103]\n"
"ds_read_b64_tr_b16 v[234:235], v249 offset:51200\n"
"v_mfma_f32_16x16x32_bf16 v[100:103], v[244:247], a[52:55], v[100:103]\n"
"ds_read_b64_tr_b16 v[236:237], v249 offset:53248\n"
"v_mfma_f32_16x16x32_bf16 v[104:107], v[240:243], a[40:43], v[104:107]\n"
"ds_read_b64_tr_b16 v[238:239], v249 offset:55296\n"
"v_mfma_f32_16x16x32_bf16 v[104:107], v[244:247], a[56:59], v[104:107]\n"
"buffer_load_dwordx4 a[16:19], v15, s[36:39], s58 offen sc1\n"
"v_mfma_f32_16x16x32_bf16 v[108:111], v[240:243], a[44:47], v[108:111]\n"
"v_mfma_f32_16x16x32_bf16 v[108:111], v[244:247], a[60:63], v[108:111]\n"
"s_waitcnt lgkmcnt(4)\n"
"v_mfma_f32_16x16x32_bf16 v[112:115], v[224:227], a[32:35], v[112:115]\n"
"ds_read_b64_tr_b16 v[240:241], v249 offset:57344\n"
"v_mfma_f32_16x16x32_bf16 v[112:115], v[228:231], a[48:51], v[112:115]\n"
"buffer_load_dwordx4 v21, s[20:23], s35 offen offset:1024 lds\n"
"v_mfma_f32_16x16x32_bf16 v[116:119], v[224:227], a[36:39], v[116:119]\n"
"ds_read_b64_tr_b16 v[242:243], v249 offset:59392\n"
"v_mfma_f32_16x16x32_bf16 v[116:119], v[228:231], a[52:55], v[116:119]\n"
"ds_read_b64_tr_b16 v[244:245], v249 offset:61440\n"
"v_mfma_f32_16x16x32_bf16 v[120:123], v[224:227], a[40:43], v[120:123]\n"
"ds_read_b64_tr_b16 v[246:247], v249 offset:63488\n"
"v_mfma_f32_16x16x32_bf16 v[120:123], v[228:231], a[56:59], v[120:123]\n"
"buffer_load_dwordx4 a[20:23], v15, s[36:39], s58 offen offset:1024 sc1\n"
"v_mfma_f32_16x16x32_bf16 v[124:127], v[224:227], a[44:47], v[124:127]\n"
"v_mfma_f32_16x16x32_bf16 v[124:127], v[228:231], a[60:63], v[124:127]\n"
"s_waitcnt lgkmcnt(4)\n"
"v_mfma_f32_16x16x32_bf16 v[128:131], v[232:235], a[160:163], v[128:131]\n"
"ds_read_b64_tr_b16 v[224:225], v252 offset:49152\n"
"v_mfma_f32_16x16x32_bf16 v[128:131], v[236:239], a[176:179], v[128:131]\n"
"buffer_load_dwordx4 v22, s[20:23], s35 offen offset:2048 lds\n"
"v_mfma_f32_16x16x32_bf16 v[132:135], v[232:235], a[164:167], v[132:135]\n"
"ds_read_b64_tr_b16 v[226:227], v252 offset:51200\n"
"v_mfma_f32_16x16x32_bf16 v[132:135], v[236:239], a[180:183], v[132:135]\n"
"ds_read_b64_tr_b16 v[228:229], v252 offset:53248\n"
"v_mfma_f32_16x16x32_bf16 v[136:139], v[232:235], a[168:171], v[136:139]\n"
"ds_read_b64_tr_b16 v[230:231], v252 offset:55296\n"
"v_mfma_f32_16x16x32_bf16 v[136:139], v[236:239], a[184:187], v[136:139]\n"
"buffer_load_dwordx4 a[24:27], v16, s[36:39], s58 offen sc1\n"
"v_mfma_f32_16x16x32_bf16 v[140:143], v[232:235], a[172:175], v[140:143]\n"
"v_mfma_f32_16x16x32_bf16 v[140:143], v[236:239], a[188:191], v[140:143]\n"
"s_waitcnt lgkmcnt(4)\n"
"v_mfma_f32_16x16x32_bf16 v[144:147], v[240:243], a[160:163], v[144:147]\n"
"ds_read_b64_tr_b16 v[232:233], v252 offset:57344\n"
"v_mfma_f32_16x16x32_bf16 v[144:147], v[244:247], a[176:179], v[144:147]\n"
"buffer_load_dwordx4 v23, s[20:23], s35 offen offset:3072 lds\n"
"v_mfma_f32_16x16x32_bf16 v[148:151], v[240:243], a[164:167], v[148:151]\n"
"ds_read_b64_tr_b16 v[234:235], v252 offset:59392\n"
"v_mfma_f32_16x16x32_bf16 v[148:151], v[244:247], a[180:183], v[148:151]\n"
"ds_read_b64_tr_b16 v[236:237], v252 offset:61440\n"
"v_mfma_f32_16x16x32_bf16 v[152:155], v[240:243], a[168:171], v[152:155]\n"
"ds_read_b64_tr_b16 v[238:239], v252 offset:63488\n"
"v_mfma_f32_16x16x32_bf16 v[152:155], v[244:247], a[184:187], v[152:155]\n"
"buffer_load_dwordx4 a[28:31], v16, s[36:39], s58 offen offset:1024 sc1\n"
"v_mfma_f32_16x16x32_bf16 v[156:159], v[240:243], a[172:175], v[156:159]\n"
"s_add_u32 s75, s40, 4718848\n"
"v_mfma_f32_16x16x32_bf16 v[156:159], v[244:247], a[188:191], v[156:159]\n"
"s_waitcnt vmcnt(25) lgkmcnt(4)\n"
"s_barrier\n"
"v_mfma_f32_16x16x32_bf16 v[160:163], v[224:227], a[160:163], v[160:163]\n"
"ds_read_b64_tr_b16 v[240:241], v250 offset:0\n"
"s_add_u32 m0, s34, 65536\n"
"v_mfma_f32_16x16x32_bf16 v[160:163], v[228:231], a[176:179], v[160:163]\n"
"buffer_load_dwordx4 v20, s[20:23], s75 offen lds\n"
"v_mfma_f32_16x16x32_bf16 v[164:167], v[224:227], a[164:167], v[164:167]\n"
"ds_read_b64_tr_b16 v[242:243], v250 offset:2048\n"
"v_mfma_f32_16x16x32_bf16 v[164:167], v[228:231], a[180:183], v[164:167]\n"
"ds_read_b64_tr_b16 v[244:245], v250 offset:4096\n"
"v_mfma_f32_16x16x32_bf16 v[168:171], v[224:227], a[168:171], v[168:171]\n"
"ds_read_b64_tr_b16 v[246:247], v250 offset:6144\n"
"s_add_u32 s58, s57, 8192\n"
"v_mfma_f32_16x16x32_bf16 v[168:171], v[228:231], a[184:187], v[168:171]\n"
"buffer_load_dwordx4 a[32:35], v13, s[36:39], s58 offen sc1\n"
"v_mfma_f32_16x16x32_bf16 v[172:175], v[224:227], a[172:175], v[172:175]\n"
"v_mfma_f32_16x16x32_bf16 v[172:175], v[228:231], a[188:191], v[172:175]\n"
"s_waitcnt lgkmcnt(4)\n"
"v_mfma_f32_16x16x32_bf16 v[176:179], v[232:235], a[160:163], v[176:179]\n"
"ds_read_b64_tr_b16 v[224:225], v250 offset:8192\n"
"v_mfma_f32_16x16x32_bf16 v[176:179], v[236:239], a[176:179], v[176:179]\n"
"buffer_load_dwordx4 v21, s[20:23], s75 offen offset:1024 lds\n"
"v_mfma_f32_16x16x32_bf16 v[180:183], v[232:235], a[164:167], v[180:183]\n"
"ds_read_b64_tr_b16 v[226:227], v250 offset:10240\n"
"v_mfma_f32_16x16x32_bf16 v[180:183], v[236:239], a[180:183], v[180:183]\n"
"ds_read_b64_tr_b16 v[228:229], v250 offset:12288\n"
"v_mfma_f32_16x16x32_bf16 v[184:187], v[232:235], a[168:171], v[184:187]\n"
"ds_read_b64_tr_b16 v[230:231], v250 offset:14336\n"
"v_mfma_f32_16x16x32_bf16 v[184:187], v[236:239], a[184:187], v[184:187]\n"
"buffer_load_dwordx4 a[36:39], v13, s[36:39], s58 offen offset:1024 sc1\n"
"v_mfma_f32_16x16x32_bf16 v[188:191], v[232:235], a[172:175], v[188:191]\n"
"v_mfma_f32_16x16x32_bf16 v[188:191], v[236:239], a[188:191], v[188:191]\n"
"s_waitcnt lgkmcnt(4)\n"
"v_mfma_f32_16x16x32_bf16 v[64:67], v[240:243], a[64:67], v[64:67]\n"
"ds_read_b64_tr_b16 v[232:233], v253 offset:0\n"
"v_mfma_f32_16x16x32_bf16 v[64:67], v[244:247], a[80:83], v[64:67]\n"
"buffer_load_dwordx4 v22, s[20:23], s75 offen offset:2048 lds\n"
"v_mfma_f32_16x16x32_bf16 v[68:71], v[240:243], a[68:71], v[68:71]\n"
"ds_read_b64_tr_b16 v[234:235], v253 offset:2048\n"
"v_mfma_f32_16x16x32_bf16 v[68:71], v[244:247], a[84:87], v[68:71]\n"
"ds_read_b64_tr_b16 v[236:237], v253 offset:4096\n"
"v_mfma_f32_16x16x32_bf16 v[72:75], v[240:243], a[72:75], v[72:75]\n"
"ds_read_b64_tr_b16 v[238:239], v253 offset:6144\n"
"v_mfma_f32_16x16x32_bf16 v[72:75], v[244:247], a[88:91], v[72:75]\n"
"v_mfma_f32_16x16x32_bf16 v[76:79], v[240:243], a[76:79], v[76:79]\n"
"v_mfma_f32_16x16x32_bf16 v[76:79], v[244:247], a[92:95], v[76:79]\n"
"s_waitcnt lgkmcnt(4)\n"
"v_mfma_f32_16x16x32_bf16 v[80:83], v[224:227], a[64:67], v[80:83]\n"
"ds_read_b64_tr_b16 v[240:241], v253 offset:8192\n"
"v_mfma_f32_16x16x32_bf16 v[80:83], v[228:231], a[80:83], v[80:83]\n"
"buffer_load_dwordx4 v23, s[20:23], s75 offen offset:3072 lds\n"
"v_mfma_f32_16x16x32_bf16 v[84:87], v[224:227], a[68:71], v[84:87]\n"
"ds_read_b64_tr_b16 v[242:243], v253 offset:10240\n"
"v_mfma_f32_16x16x32_bf16 v[84:87], v[228:231], a[84:87], v[84:87]\n"
"ds_read_b64_tr_b16 v[244:245], v253 offset:12288\n"
"v_mfma_f32_16x16x32_bf16 v[88:91], v[224:227], a[72:75], v[88:91]\n"
"ds_read_b64_tr_b16 v[246:247], v253 offset:14336\n"
"v_mfma_f32_16x16x32_bf16 v[88:91], v[228:231], a[88:91], v[88:91]\n"
"buffer_load_dwordx4 a[40:43], v14, s[36:39], s58 offen sc1\n"
"v_mfma_f32_16x16x32_bf16 v[92:95], v[224:227], a[76:79], v[92:95]\n"
"s_add_u32 s35, s40, 2949504\n"
"v_mfma_f32_16x16x32_bf16 v[92:95], v[228:231], a[92:95], v[92:95]\n"
"s_waitcnt vmcnt(28) lgkmcnt(4)\n"
"s_barrier\n"
"v_mfma_f32_16x16x32_bf16 v[96:99], v[232:235], a[64:67], v[96:99]\n"
"ds_read_b64_tr_b16 v[224:225], v248 offset:0\n"
"s_add_u32 m0, s34, 81920\n"
"v_mfma_f32_16x16x32_bf16 v[96:99], v[236:239], a[80:83], v[96:99]\n"
"buffer_load_dwordx4 v20, s[20:23], s35 offen lds\n"
"v_mfma_f32_16x16x32_bf16 v[100:103], v[232:235], a[68:71], v[100:103]\n"
"ds_read_b64_tr_b16 v[226:227], v248 offset:2048\n"
"v_mfma_f32_16x16x32_bf16 v[100:103], v[236:239], a[84:87], v[100:103]\n"
"ds_read_b64_tr_b16 v[228:229], v248 offset:4096\n"
"v_mfma_f32_16x16x32_bf16 v[104:107], v[232:235], a[72:75], v[104:107]\n"
"ds_read_b64_tr_b16 v[230:231], v248 offset:6144\n"
"v_mfma_f32_16x16x32_bf16 v[104:107], v[236:239], a[88:91], v[104:107]\n"
"buffer_load_dwordx4 a[44:47], v14, s[36:39], s58 offen offset:1024 sc1\n"
"v_mfma_f32_16x16x32_bf16 v[108:111], v[232:235], a[76:79], v[108:111]\n"
"v_mfma_f32_16x16x32_bf16 v[108:111], v[236:239], a[92:95], v[108:111]\n"
"s_waitcnt lgkmcnt(4)\n"
"v_mfma_f32_16x16x32_bf16 v[112:115], v[240:243], a[64:67], v[112:115]\n"
"ds_read_b64_tr_b16 v[232:233], v248 offset:8192\n"
"v_mfma_f32_16x16x32_bf16 v[112:115], v[244:247], a[80:83], v[112:115]\n"
"buffer_load_dwordx4 v21, s[20:23], s35 offen offset:1024 lds\n"
"v_mfma_f32_16x16x32_bf16 v[116:119], v[240:243], a[68:71], v[116:119]\n"
"ds_read_b64_tr_b16 v[234:235], v248 offset:10240\n"
"v_mfma_f32_16x16x32_bf16 v[116:119], v[244:247], a[84:87], v[116:119]\n"
"ds_read_b64_tr_b16 v[236:237], v248 offset:12288\n"
"v_mfma_f32_16x16x32_bf16 v[120:123], v[240:243], a[72:75], v[120:123]\n"
"ds_read_b64_tr_b16 v[238:239], v248 offset:14336\n"
"v_mfma_f32_16x16x32_bf16 v[120:123], v[244:247], a[88:91], v[120:123]\n"
"buffer_load_dwordx4 a[48:51], v15, s[36:39], s58 offen sc1\n"
"v_mfma_f32_16x16x32_bf16 v[124:127], v[240:243], a[76:79], v[124:127]\n"
"v_mfma_f32_16x16x32_bf16 v[124:127], v[244:247], a[92:95], v[124:127]\n"
"s_waitcnt lgkmcnt(4)\n"
"v_mfma_f32_16x16x32_bf16 v[128:131], v[224:227], a[192:195], v[128:131]\n"
"ds_read_b64_tr_b16 v[240:241], v251 offset:0\n"
"v_mfma_f32_16x16x32_bf16 v[128:131], v[228:231], a[208:211], v[128:131]\n"
"buffer_load_dwordx4 v22, s[20:23], s35 offen offset:2048 lds\n"
"v_mfma_f32_16x16x32_bf16 v[132:135], v[224:227], a[196:199], v[132:135]\n"
"ds_read_b64_tr_b16 v[242:243], v251 offset:2048\n"
"v_mfma_f32_16x16x32_bf16 v[132:135], v[228:231], a[212:215], v[132:135]\n"
"ds_read_b64_tr_b16 v[244:245], v251 offset:4096\n"
"v_mfma_f32_16x16x32_bf16 v[136:139], v[224:227], a[200:203], v[136:139]\n"
"ds_read_b64_tr_b16 v[246:247], v251 offset:6144\n"
"v_mfma_f32_16x16x32_bf16 v[136:139], v[228:231], a[216:219], v[136:139]\n"
"buffer_load_dwordx4 a[52:55], v15, s[36:39], s58 offen offset:1024 sc1\n"
"v_mfma_f32_16x16x32_bf16 v[140:143], v[224:227], a[204:207], v[140:143]\n"
"v_mfma_f32_16x16x32_bf16 v[140:143], v[228:231], a[220:223], v[140:143]\n"
"s_waitcnt lgkmcnt(4)\n"
"v_mfma_f32_16x16x32_bf16 v[144:147], v[232:235], a[192:195], v[144:147]\n"
"ds_read_b64_tr_b16 v[224:225], v251 offset:8192\n"
"v_mfma_f32_16x16x32_bf16 v[144:147], v[236:239], a[208:211], v[144:147]\n"
"buffer_load_dwordx4 v23, s[20:23], s35 offen offset:3072 lds\n"
"v_mfma_f32_16x16x32_bf16 v[148:151], v[232:235], a[196:199], v[148:151]\n"
"ds_read_b64_tr_b16 v[226:227], v251 offset:10240\n"
"v_mfma_f32_16x16x32_bf16 v[148:151], v[236:239], a[212:215], v[148:151]\n"
"ds_read_b64_tr_b16 v[228:229], v251 offset:12288\n"
"v_mfma_f32_16x16x32_bf16 v[152:155], v[232:235], a[200:203], v[152:155]\n"
"ds_read_b64_tr_b16 v[230:231], v251 offset:14336\n"
"v_mfma_f32_16x16x32_bf16 v[152:155], v[236:239], a[216:219], v[152:155]\n"
"buffer_load_dwordx4 a[56:59], v16, s[36:39], s58 offen sc1\n"
"v_mfma_f32_16x16x32_bf16 v[156:159], v[232:235], a[204:207], v[156:159]\n"
"s_add_u32 s75, s40, 3539328\n"
"v_mfma_f32_16x16x32_bf16 v[156:159], v[236:239], a[220:223], v[156:159]\n"
"s_waitcnt vmcnt(32) lgkmcnt(4)\n"
"s_barrier\n"
"v_mfma_f32_16x16x32_bf16 v[160:163], v[240:243], a[192:195], v[160:163]\n"
"ds_read_b64_tr_b16 v[232:233], v248 offset:16384\n"
"s_add_u32 m0, s34, 98304\n"
"v_mfma_f32_16x16x32_bf16 v[160:163], v[244:247], a[208:211], v[160:163]\n"
"buffer_load_dwordx4 v20, s[20:23], s75 offen lds\n"
"v_mfma_f32_16x16x32_bf16 v[164:167], v[240:243], a[196:199], v[164:167]\n"
"ds_read_b64_tr_b16 v[234:235], v248 offset:18432\n"
"v_mfma_f32_16x16x32_bf16 v[164:167], v[244:247], a[212:215], v[164:167]\n"
"ds_read_b64_tr_b16 v[236:237], v248 offset:20480\n"
"v_mfma_f32_16x16x32_bf16 v[168:171], v[240:243], a[200:203], v[168:171]\n"
"ds_read_b64_tr_b16 v[238:239], v248 offset:22528\n"
"v_mfma_f32_16x16x32_bf16 v[168:171], v[244:247], a[216:219], v[168:171]\n"
"buffer_load_dwordx4 v21, s[20:23], s75 offen offset:1024 lds\n"
"v_mfma_f32_16x16x32_bf16 v[172:175], v[240:243], a[204:207], v[172:175]\n"
"v_mfma_f32_16x16x32_bf16 v[172:175], v[244:247], a[220:223], v[172:175]\n"
"s_waitcnt lgkmcnt(4)\n"
"v_mfma_f32_16x16x32_bf16 v[176:179], v[224:227], a[192:195], v[176:179]\n"
"ds_read_b64_tr_b16 v[240:241], v248 offset:24576\n"
"v_mfma_f32_16x16x32_bf16 v[176:179], v[228:231], a[208:211], v[176:179]\n"
"buffer_load_dwordx4 v22, s[20:23], s75 offen offset:2048 lds\n"
"v_mfma_f32_16x16x32_bf16 v[180:183], v[224:227], a[196:199], v[180:183]\n"
"ds_read_b64_tr_b16 v[242:243], v248 offset:26624\n"
"v_mfma_f32_16x16x32_bf16 v[180:183], v[228:231], a[212:215], v[180:183]\n"
"ds_read_b64_tr_b16 v[244:245], v248 offset:28672\n"
"v_mfma_f32_16x16x32_bf16 v[184:187], v[224:227], a[200:203], v[184:187]\n"
"ds_read_b64_tr_b16 v[246:247], v248 offset:30720\n"
"v_mfma_f32_16x16x32_bf16 v[184:187], v[228:231], a[216:219], v[184:187]\n"
"buffer_load_dwordx4 v23, s[20:23], s75 offen offset:3072 lds\n"
"v_mfma_f32_16x16x32_bf16 v[188:191], v[224:227], a[204:207], v[188:191]\n"
"v_mfma_f32_16x16x32_bf16 v[188:191], v[228:231], a[220:223], v[188:191]\n"
"s_waitcnt lgkmcnt(4)\n"
"v_mfma_f32_16x16x32_bf16 v[64:67], v[232:235], a[96:99], v[64:67]\n"
"ds_read_b64_tr_b16 v[224:225], v251 offset:16384\n"
"v_mfma_f32_16x16x32_bf16 v[64:67], v[236:239], a[112:115], v[64:67]\n"
"buffer_load_dwordx4 v[24:27], off, s[36:39], s59 offset:32 sc1\n"
"v_mfma_f32_16x16x32_bf16 v[68:71], v[232:235], a[100:103], v[68:71]\n"
"ds_read_b64_tr_b16 v[226:227], v251 offset:18432\n"
"v_mfma_f32_16x16x32_bf16 v[68:71], v[236:239], a[116:119], v[68:71]\n"
"ds_read_b64_tr_b16 v[228:229], v251 offset:20480\n"
"s_add_u32 s35, s40, 4129152\n"
"v_mfma_f32_16x16x32_bf16 v[72:75], v[232:235], a[104:107], v[72:75]\n"
"ds_read_b64_tr_b16 v[230:231], v251 offset:22528\n"
"s_add_u32 m0, s34, 114688\n"
"v_mfma_f32_16x16x32_bf16 v[72:75], v[236:239], a[120:123], v[72:75]\n"
"buffer_load_dwordx4 v20, s[20:23], s35 offen lds\n"
"v_mfma_f32_16x16x32_bf16 v[76:79], v[232:235], a[108:111], v[76:79]\n"
"v_mfma_f32_16x16x32_bf16 v[76:79], v[236:239], a[124:127], v[76:79]\n"
"s_waitcnt lgkmcnt(4)\n"
"v_mfma_f32_16x16x32_bf16 v[80:83], v[240:243], a[96:99], v[80:83]\n"
"ds_read_b64_tr_b16 v[232:233], v251 offset:24576\n"
"v_mfma_f32_16x16x32_bf16 v[80:83], v[244:247], a[112:115], v[80:83]\n"
"buffer_load_dwordx4 v21, s[20:23], s35 offen offset:1024 lds\n"
"v_mfma_f32_16x16x32_bf16 v[84:87], v[240:243], a[100:103], v[84:87]\n"
"ds_read_b64_tr_b16 v[234:235], v251 offset:26624\n"
"v_mfma_f32_16x16x32_bf16 v[84:87], v[244:247], a[116:119], v[84:87]\n"
"ds_read_b64_tr_b16 v[236:237], v251 offset:28672\n"
"v_mfma_f32_16x16x32_bf16 v[88:91], v[240:243], a[104:107], v[88:91]\n"
"ds_read_b64_tr_b16 v[238:239], v251 offset:30720\n"
"v_mfma_f32_16x16x32_bf16 v[88:91], v[244:247], a[120:123], v[88:91]\n"
"buffer_load_dwordx4 v22, s[20:23], s35 offen offset:2048 lds\n"
"v_mfma_f32_16x16x32_bf16 v[92:95], v[240:243], a[108:111], v[92:95]\n"
"v_mfma_f32_16x16x32_bf16 v[92:95], v[244:247], a[124:127], v[92:95]\n"
"s_waitcnt vmcnt(32) lgkmcnt(4)\n"
"s_barrier\n"
"v_mfma_f32_16x16x32_bf16 v[96:99], v[224:227], a[96:99], v[96:99]\n"
"ds_read_b64_tr_b16 v[240:241], v248 offset:32768\n"
"v_mfma_f32_16x16x32_bf16 v[96:99], v[228:231], a[112:115], v[96:99]\n"
"buffer_load_dwordx4 v23, s[20:23], s35 offen offset:3072 lds\n"
"v_mfma_f32_16x16x32_bf16 v[100:103], v[224:227], a[100:103], v[100:103]\n"
"ds_read_b64_tr_b16 v[242:243], v248 offset:34816\n"
"v_mfma_f32_16x16x32_bf16 v[100:103], v[228:231], a[116:119], v[100:103]\n"
"ds_read_b64_tr_b16 v[244:245], v248 offset:36864\n"
"s_add_u32 s75, s40, 4718976\n"
"v_mfma_f32_16x16x32_bf16 v[104:107], v[224:227], a[104:107], v[104:107]\n"
"ds_read_b64_tr_b16 v[246:247], v248 offset:38912\n"
"s_add_u32 m0, s34, 131072\n"
"v_mfma_f32_16x16x32_bf16 v[104:107], v[228:231], a[120:123], v[104:107]\n"
"buffer_load_dwordx4 v20, s[20:23], s75 offen lds\n"
"v_mfma_f32_16x16x32_bf16 v[108:111], v[224:227], a[108:111], v[108:111]\n"
"v_mfma_f32_16x16x32_bf16 v[108:111], v[228:231], a[124:127], v[108:111]\n"
"s_waitcnt lgkmcnt(4)\n"
"v_mfma_f32_16x16x32_bf16 v[112:115], v[232:235], a[96:99], v[112:115]\n"
"ds_read_b64_tr_b16 v[224:225], v248 offset:40960\n"
"v_mfma_f32_16x16x32_bf16 v[112:115], v[236:239], a[112:115], v[112:115]\n"
"buffer_load_dwordx4 v21, s[20:23], s75 offen offset:1024 lds\n"
"v_mfma_f32_16x16x32_bf16 v[116:119], v[232:235], a[100:103], v[116:119]\n"
"ds_read_b64_tr_b16 v[226:227], v248 offset:43008\n"
"v_mfma_f32_16x16x32_bf16 v[116:119], v[236:239], a[116:119], v[116:119]\n"
"ds_read_b64_tr_b16 v[228:229], v248 offset:45056\n"
"v_mfma_f32_16x16x32_bf16 v[120:123], v[232:235], a[104:107], v[120:123]\n"
"ds_read_b64_tr_b16 v[230:231], v248 offset:47104\n"
"v_mfma_f32_16x16x32_bf16 v[120:123], v[236:239], a[120:123], v[120:123]\n"
"buffer_load_dwordx4 a[60:63], v16, s[36:39], s58 offen offset:1024 sc1\n"
"v_mfma_f32_16x16x32_bf16 v[124:127], v[232:235], a[108:111], v[124:127]\n"
"v_mfma_f32_16x16x32_bf16 v[124:127], v[236:239], a[124:127], v[124:127]\n"
"s_waitcnt lgkmcnt(4)\n"
"v_mfma_f32_16x16x32_bf16 v[128:131], v[240:243], a[224:227], v[128:131]\n"
"ds_read_b64_tr_b16 v[232:233], v251 offset:32768\n"
"v_mfma_f32_16x16x32_bf16 v[128:131], v[244:247], a[240:243], v[128:131]\n"
"buffer_load_dwordx4 v22, s[20:23], s75 offen offset:2048 lds\n"
"v_mfma_f32_16x16x32_bf16 v[132:135], v[240:243], a[228:231], v[132:135]\n"
"ds_read_b64_tr_b16 v[234:235], v251 offset:34816\n"
"v_mfma_f32_16x16x32_bf16 v[132:135], v[244:247], a[244:247], v[132:135]\n"
"ds_read_b64_tr_b16 v[236:237], v251 offset:36864\n"
"v_mfma_f32_16x16x32_bf16 v[136:139], v[240:243], a[232:235], v[136:139]\n"
"ds_read_b64_tr_b16 v[238:239], v251 offset:38912\n"
"s_add_u32 s58, s57, 65536\n"
"v_mfma_f32_16x16x32_bf16 v[136:139], v[244:247], a[248:251], v[136:139]\n"
"buffer_load_dwordx4 a[64:67], v13, s[36:39], s58 offen sc1\n"
"v_mfma_f32_16x16x32_bf16 v[140:143], v[240:243], a[236:239], v[140:143]\n"
"v_mfma_f32_16x16x32_bf16 v[140:143], v[244:247], a[252:255], v[140:143]\n"
"s_waitcnt lgkmcnt(4)\n"
"v_mfma_f32_16x16x32_bf16 v[144:147], v[224:227], a[224:227], v[144:147]\n"
"ds_read_b64_tr_b16 v[240:241], v251 offset:40960\n"
"v_mfma_f32_16x16x32_bf16 v[144:147], v[228:231], a[240:243], v[144:147]\n"
"buffer_load_dwordx4 v23, s[20:23], s75 offen offset:3072 lds\n"
"v_mfma_f32_16x16x32_bf16 v[148:151], v[224:227], a[228:231], v[148:151]\n"
"ds_read_b64_tr_b16 v[242:243], v251 offset:43008\n"
"v_mfma_f32_16x16x32_bf16 v[148:151], v[228:231], a[244:247], v[148:151]\n"
"ds_read_b64_tr_b16 v[244:245], v251 offset:45056\n"
"v_mfma_f32_16x16x32_bf16 v[152:155], v[224:227], a[232:235], v[152:155]\n"
"ds_read_b64_tr_b16 v[246:247], v251 offset:47104\n"
"v_mfma_f32_16x16x32_bf16 v[152:155], v[228:231], a[248:251], v[152:155]\n"
"buffer_load_dwordx4 a[68:71], v13, s[36:39], s58 offen offset:1024 sc1\n"
"v_mfma_f32_16x16x32_bf16 v[156:159], v[224:227], a[236:239], v[156:159]\n"
"v_mfma_f32_16x16x32_bf16 v[156:159], v[228:231], a[252:255], v[156:159]\n"
"s_waitcnt vmcnt(32) lgkmcnt(4)\n"
"s_barrier\n"
"v_mfma_f32_16x16x32_bf16 v[160:163], v[232:235], a[224:227], v[160:163]\n"
"ds_read_b64_tr_b16 v[224:225], v248 offset:49152\n"
"v_mfma_f32_16x16x32_bf16 v[160:163], v[236:239], a[240:243], v[160:163]\n"
"buffer_load_dwordx4 a[72:75], v14, s[36:39], s58 offen sc1\n"
"v_mfma_f32_16x16x32_bf16 v[164:167], v[232:235], a[228:231], v[164:167]\n"
"ds_read_b64_tr_b16 v[226:227], v248 offset:51200\n"
"v_mfma_f32_16x16x32_bf16 v[164:167], v[236:239], a[244:247], v[164:167]\n"
"ds_read_b64_tr_b16 v[228:229], v248 offset:53248\n"
"v_mfma_f32_16x16x32_bf16 v[168:171], v[232:235], a[232:235], v[168:171]\n"
"ds_read_b64_tr_b16 v[230:231], v248 offset:55296\n"
"v_mfma_f32_16x16x32_bf16 v[168:171], v[236:239], a[248:251], v[168:171]\n"
"buffer_load_dwordx4 a[76:79], v14, s[36:39], s58 offen offset:1024 sc1\n"
"v_mfma_f32_16x16x32_bf16 v[172:175], v[232:235], a[236:239], v[172:175]\n"
"v_mfma_f32_16x16x32_bf16 v[172:175], v[236:239], a[252:255], v[172:175]\n"
"s_waitcnt lgkmcnt(4)\n"
"v_mfma_f32_16x16x32_bf16 v[176:179], v[240:243], a[224:227], v[176:179]\n"
"ds_read_b64_tr_b16 v[232:233], v248 offset:57344\n"
"v_mfma_f32_16x16x32_bf16 v[176:179], v[244:247], a[240:243], v[176:179]\n"
"ds_read_b64_tr_b16 v[234:235], v248 offset:59392\n"
"v_mfma_f32_16x16x32_bf16 v[180:183], v[240:243], a[228:231], v[180:183]\n"
"ds_read_b64_tr_b16 v[236:237], v248 offset:61440\n"
"v_mfma_f32_16x16x32_bf16 v[180:183], v[244:247], a[244:247], v[180:183]\n"
"ds_read_b64_tr_b16 v[238:239], v248 offset:63488\n"
"v_mfma_f32_16x16x32_bf16 v[184:187], v[240:243], a[232:235], v[184:187]\n"
"v_mfma_f32_16x16x32_bf16 v[184:187], v[244:247], a[248:251], v[184:187]\n"
"buffer_load_dwordx4 a[80:83], v15, s[36:39], s58 offen sc1\n"
"v_mfma_f32_16x16x32_bf16 v[188:191], v[240:243], a[236:239], v[188:191]\n"
"v_mfma_f32_16x16x32_bf16 v[188:191], v[244:247], a[252:255], v[188:191]\n"
"s_waitcnt lgkmcnt(4)\n"
"v_mfma_f32_16x16x32_bf16 v[64:67], v[224:227], a[128:131], v[64:67]\n"
"ds_read_b64_tr_b16 v[240:241], v251 offset:49152\n"
"v_mfma_f32_16x16x32_bf16 v[64:67], v[228:231], a[144:147], v[64:67]\n"
"buffer_load_dwordx4 a[84:87], v15, s[36:39], s58 offen offset:1024 sc1\n"
"v_mfma_f32_16x16x32_bf16 v[68:71], v[224:227], a[132:135], v[68:71]\n"
"ds_read_b64_tr_b16 v[242:243], v251 offset:51200\n"
"v_mfma_f32_16x16x32_bf16 v[68:71], v[228:231], a[148:151], v[68:71]\n"
"ds_read_b64_tr_b16 v[244:245], v251 offset:53248\n"
"v_mfma_f32_16x16x32_bf16 v[72:75], v[224:227], a[136:139], v[72:75]\n"
"ds_read_b64_tr_b16 v[246:247], v251 offset:55296\n"
"v_mfma_f32_16x16x32_bf16 v[72:75], v[228:231], a[152:155], v[72:75]\n"
"buffer_load_dwordx4 a[88:91], v16, s[36:39], s58 offen sc1\n"
"v_mfma_f32_16x16x32_bf16 v[76:79], v[224:227], a[140:143], v[76:79]\n"
"v_mfma_f32_16x16x32_bf16 v[76:79], v[228:231], a[156:159], v[76:79]\n"
"s_waitcnt vmcnt(16)\n"
"v_xor_b32 v24, s86, v24\n"
"s_waitcnt lgkmcnt(4)\n"
"v_mfma_f32_16x16x32_bf16 v[80:83], v[232:235], a[128:131], v[80:83]\n"
"ds_read_b64_tr_b16 v[224:225], v251 offset:57344\n"
"v_mfma_f32_16x16x32_bf16 v[80:83], v[236:239], a[144:147], v[80:83]\n"
"buffer_load_dwordx4 a[92:95], v16, s[36:39], s58 offen offset:1024 sc1\n"
"v_mfma_f32_16x16x32_bf16 v[84:87], v[232:235], a[132:135], v[84:87]\n"
"ds_read_b64_tr_b16 v[226:227], v251 offset:59392\n"
"v_bitop3_b32 v24, v24, v25, s86 bitop3:0xf6\n"
"v_mfma_f32_16x16x32_bf16 v[84:87], v[236:239], a[148:151], v[84:87]\n"
"ds_read_b64_tr_b16 v[228:229], v251 offset:61440\n"
"v_bitop3_b32 v24, v24, v26, s86 bitop3:0xf6\n"
"v_mfma_f32_16x16x32_bf16 v[88:91], v[232:235], a[136:139], v[88:91]\n"
"ds_read_b64_tr_b16 v[230:231], v251 offset:63488\n"
"v_mfma_f32_16x16x32_bf16 v[88:91], v[236:239], a[152:155], v[88:91]\n"
"v_bitop3_b32 v24, v24, v27, s86 bitop3:0xf6\n"
"v_mfma_f32_16x16x32_bf16 v[92:95], v[232:235], a[140:143], v[92:95]\n"
"s_add_u32 s35, s43, 0\n"
"v_mfma_f32_16x16x32_bf16 v[92:95], v[236:239], a[156:159], v[92:95]\n"
"s_waitcnt lgkmcnt(4)\n"
"s_barrier\n"
"v_mfma_f32_16x16x32_bf16 v[96:99], v[240:243], a[128:131], v[96:99]\n"
"ds_read_b64_tr_b16 v[232:233], v249 offset:0\n"
"s_add_u32 m0, s46, 0\n"
"v_mfma_f32_16x16x32_bf16 v[96:99], v[244:247], a[144:147], v[96:99]\n"
"buffer_load_dwordx4 v1, s[28:31], s35 offen lds\n"
"v_mfma_f32_16x16x32_bf16 v[100:103], v[240:243], a[132:135], v[100:103]\n"
"ds_read_b64_tr_b16 v[234:235], v249 offset:2048\n"
"v_cmp_ne_u32 vcc, 0, v24\n"
"v_mfma_f32_16x16x32_bf16 v[100:103], v[244:247], a[148:151], v[100:103]\n"
"ds_read_b64_tr_b16 v[236:237], v249 offset:4096\n"
"s_cbranch_vccnz SLOW2_%=\n"
"BACK2_%=:\n"
"v_mfma_f32_16x16x32_bf16 v[104:107], v[240:243], a[136:139], v[104:107]\n"
"ds_read_b64_tr_b16 v[238:239], v249 offset:6144\n"
"v_mfma_f32_16x16x32_bf16 v[104:107], v[244:247], a[152:155], v[104:107]\n"
"buffer_load_dwordx4 v2, s[28:31], s35 offen offset:1056 lds\n"
"v_mfma_f32_16x16x32_bf16 v[108:111], v[240:243], a[140:143], v[108:111]\n"
"v_mfma_f32_16x16x32_bf16 v[108:111], v[244:247], a[156:159], v[108:111]\n"
"s_waitcnt lgkmcnt(4)\n"
"v_mfma_f32_16x16x32_bf16 v[112:115], v[224:227], a[128:131], v[112:115]\n"
"ds_read_b64_tr_b16 v[240:241], v249 offset:8192\n"
"v_mfma_f32_16x16x32_bf16 v[112:115], v[228:231], a[144:147], v[112:115]\n"
"buffer_load_dwordx4 v3, s[28:31], s35 offen offset:2112 lds\n"
"v_mfma_f32_16x16x32_bf16 v[116:119], v[224:227], a[132:135], v[116:119]\n"
"ds_read_b64_tr_b16 v[242:243], v249 offset:10240\n"
"v_mfma_f32_16x16x32_bf16 v[116:119], v[228:231], a[148:151], v[116:119]\n"
"ds_read_b64_tr_b16 v[244:245], v249 offset:12288\n"
"v_mfma_f32_16x16x32_bf16 v[120:123], v[224:227], a[136:139], v[120:123]\n"
"ds_read_b64_tr_b16 v[246:247], v249 offset:14336\n"
"v_mfma_f32_16x16x32_bf16 v[120:123], v[228:231], a[152:155], v[120:123]\n"
"buffer_load_dwordx4 v4, s[28:31], s35 offen offset:3168 lds\n"
"v_mfma_f32_16x16x32_bf16 v[124:127], v[224:227], a[140:143], v[124:127]\n"
"v_mfma_f32_16x16x32_bf16 v[124:127], v[228:231], a[156:159], v[124:127]\n"
"s_add_u32 s76, s61, 0\n"
"s_waitcnt lgkmcnt(4)\n"
"v_mfma_f32_16x16x32_bf16 v[128:131], v[232:235], v[192:195], v[128:131]\n"
"ds_read_b64_tr_b16 v[224:225], v252 offset:0\n"
"s_add_u32 m0, s18, 33792\n"
"v_mfma_f32_16x16x32_bf16 v[128:131], v[236:239], v[208:211], v[128:131]\n"
"buffer_load_dwordx4 v8, s[36:39], s76 offen sc1 lds\n"
"v_mfma_f32_16x16x32_bf16 v[132:135], v[232:235], v[196:199], v[132:135]\n"
"ds_read_b64_tr_b16 v[226:227], v252 offset:2048\n"
"v_mfma_f32_16x16x32_bf16 v[132:135], v[236:239], v[212:215], v[132:135]\n"
"ds_read_b64_tr_b16 v[228:229], v252 offset:4096\n"
"s_add_u32 s75, s43, 4608\n"
"v_mfma_f32_16x16x32_bf16 v[136:139], v[232:235], v[200:203], v[136:139]\n"
"ds_read_b64_tr_b16 v[230:231], v252 offset:6144\n"
"s_add_u32 m0, s46, 4224\n"
"v_mfma_f32_16x16x32_bf16 v[136:139], v[236:239], v[216:219], v[136:139]\n"
"buffer_load_dwordx4 v1, s[28:31], s75 offen lds\n"
"v_mfma_f32_16x16x32_bf16 v[140:143], v[232:235], v[204:207], v[140:143]\n"
"v_mfma_f32_16x16x32_bf16 v[140:143], v[236:239], v[220:223], v[140:143]\n"
"s_waitcnt lgkmcnt(4)\n"
"v_mfma_f32_16x16x32_bf16 v[144:147], v[240:243], v[192:195], v[144:147]\n"
"ds_read_b64_tr_b16 v[232:233], v252 offset:8192\n"
"v_mfma_f32_16x16x32_bf16 v[144:147], v[244:247], v[208:211], v[144:147]\n"
"buffer_load_dword v29, v17, s[36:39], 0 offen sc1\n"
"v_mfma_f32_16x16x32_bf16 v[148:151], v[240:243], v[196:199], v[148:151]\n"
"ds_read_b64_tr_b16 v[234:235], v252 offset:10240\n"
"v_mfma_f32_16x16x32_bf16 v[148:151], v[244:247], v[212:215], v[148:151]\n"
"ds_read_b64_tr_b16 v[236:237], v252 offset:12288\n"
"v_mfma_f32_16x16x32_bf16 v[152:155], v[240:243], v[200:203], v[152:155]\n"
"ds_read_b64_tr_b16 v[238:239], v252 offset:14336\n"
"v_mfma_f32_16x16x32_bf16 v[152:155], v[244:247], v[216:219], v[152:155]\n"
"v_mfma_f32_16x16x32_bf16 v[156:159], v[240:243], v[204:207], v[156:159]\n"
"v_mfma_f32_16x16x32_bf16 v[156:159], v[244:247], v[220:223], v[156:159]\n"
"s_waitcnt lgkmcnt(4)\n"
"s_barrier\n"
"v_mfma_f32_16x16x32_bf16 v[160:163], v[224:227], v[192:195], v[160:163]\n"
"ds_read_b64_tr_b16 v[240:241], v249 offset:16384\n"
"v_mfma_f32_16x16x32_bf16 v[160:163], v[228:231], v[208:211], v[160:163]\n"
"buffer_load_dwordx4 v2, s[28:31], s75 offen offset:1056 lds\n"
"v_mfma_f32_16x16x32_bf16 v[164:167], v[224:227], v[196:199], v[164:167]\n"
"ds_read_b64_tr_b16 v[242:243], v249 offset:18432\n"
"v_mfma_f32_16x16x32_bf16 v[164:167], v[228:231], v[212:215], v[164:167]\n"
"ds_read_b64_tr_b16 v[244:245], v249 offset:20480\n"
"v_mfma_f32_16x16x32_bf16 v[168:171], v[224:227], v[200:203], v[168:171]\n"
"ds_read_b64_tr_b16 v[246:247], v249 offset:22528\n"
"v_mfma_f32_16x16x32_bf16 v[168:171], v[228:231], v[216:219], v[168:171]\n"
"buffer_load_dwordx4 v3, s[28:31], s75 offen offset:2112 lds\n"
"v_mfma_f32_16x16x32_bf16 v[172:175], v[224:227], v[204:207], v[172:175]\n"
"v_mfma_f32_16x16x32_bf16 v[172:175], v[228:231], v[220:223], v[172:175]\n"
"s_waitcnt lgkmcnt(4)\n"
"v_mfma_f32_16x16x32_bf16 v[176:179], v[232:235], v[192:195], v[176:179]\n"
"ds_read_b64_tr_b16 v[224:225], v249 offset:24576\n"
"v_mfma_f32_16x16x32_bf16 v[176:179], v[236:239], v[208:211], v[176:179]\n"
"buffer_load_dwordx4 v4, s[28:31], s75 offen offset:3168 lds\n"
"v_mfma_f32_16x16x32_bf16 v[180:183], v[232:235], v[196:199], v[180:183]\n"
"ds_read_b64_tr_b16 v[226:227], v249 offset:26624\n"
"v_mfma_f32_16x16x32_bf16 v[180:183], v[236:239], v[212:215], v[180:183]\n"
"ds_read_b64_tr_b16 v[228:229], v249 offset:28672\n"
"v_mfma_f32_16x16x32_bf16 v[184:187], v[232:235], v[200:203], v[184:187]\n"
"ds_read_b64_tr_b16 v[230:231], v249 offset:30720\n"
"s_add_u32 s58, s57, 16384\n"
"v_mfma_f32_16x16x32_bf16 v[184:187], v[236:239], v[216:219], v[184:187]\n"
"buffer_load_dwordx4 a[96:99], v13, s[36:39], s58 offen sc1\n"
"v_mfma_f32_16x16x32_bf16 v[188:191], v[232:235], v[204:207], v[188:191]\n"
"v_mfma_f32_16x16x32_bf16 v[188:191], v[236:239], v[220:223], v[188:191]\n"
"s_waitcnt lgkmcnt(4)\n"
"v_mfma_f32_16x16x32_bf16 v[64:67], v[240:243], a[160:163], v[64:67]\n"
"ds_read_b64_tr_b16 v[232:233], v252 offset:16384\n"
"v_mfma_f32_16x16x32_bf16 v[64:67], v[244:247], a[176:179], v[64:67]\n"
"buffer_load_dwordx4 a[100:103], v13, s[36:39], s58 offen offset:1024 sc1\n"
"v_mfma_f32_16x16x32_bf16 v[68:71], v[240:243], a[164:167], v[68:71]\n"
"ds_read_b64_tr_b16 v[234:235], v252 offset:18432\n"
"v_max_f32 v128, 0, v128\n"
"v_max_f32 v129, 0, v129\n"
"v_mfma_f32_16x16x32_bf16 v[68:71], v[244:247], a[180:183], v[68:71]\n"
"ds_read_b64_tr_b16 v[236:237], v252 offset:20480\n"
"v_max_f32 v130, 0, v130\n"
"v_mfma_f32_16x16x32_bf16 v[72:75], v[240:243], a[168:171], v[72:75]\n"
"ds_read_b64_tr_b16 v[238:239], v252 offset:22528\n"
"v_max_f32 v131, 0, v131\n"
"v_mfma_f32_16x16x32_bf16 v[72:75], v[244:247], a[184:187], v[72:75]\n"
"buffer_load_dwordx4 a[104:107], v14, s[36:39], s58 offen sc1\n"
"v_mfma_f32_16x16x32_bf16 v[76:79], v[240:243], a[172:175], v[76:79]\n"
"v_mul_f32 v128, v128, v144\n"
"v_mul_f32 v129, v129, v145\n"
"v_mfma_f32_16x16x32_bf16 v[76:79], v[244:247], a[188:191], v[76:79]\n"
"v_mul_f32 v130, v130, v146\n"
"s_waitcnt lgkmcnt(4)\n"
"v_mfma_f32_16x16x32_bf16 v[80:83], v[224:227], a[160:163], v[80:83]\n"
"ds_read_b64_tr_b16 v[240:241], v252 offset:24576\n"
"v_mul_f32 v131, v131, v147\n"
"v_mfma_f32_16x16x32_bf16 v[80:83], v[228:231], a[176:179], v[80:83]\n"
"buffer_load_dwordx4 a[108:111], v14, s[36:39], s58 offen offset:1024 sc1\n"
"v_mfma_f32_16x16x32_bf16 v[84:87], v[224:227], a[164:167], v[84:87]\n"
"ds_read_b64_tr_b16 v[242:243], v252 offset:26624\n"
"v_cvt_pk_bf16_f32 v128, v128, v129\n"
"v_cvt_pk_bf16_f32 v129, v130, v131\n"
"v_mfma_f32_16x16x32_bf16 v[84:87], v[228:231], a[180:183], v[84:87]\n"
"ds_read_b64_tr_b16 v[244:245], v252 offset:28672\n"
"v_max_f32 v160, 0, v160\n"
"v_mfma_f32_16x16x32_bf16 v[88:91], v[224:227], a[168:171], v[88:91]\n"
"ds_read_b64_tr_b16 v[246:247], v252 offset:30720\n"
"v_max_f32 v161, 0, v161\n"
"v_mfma_f32_16x16x32_bf16 v[88:91], v[228:231], a[184:187], v[88:91]\n"
"buffer_load_dwordx4 a[112:115], v15, s[36:39], s58 offen sc1\n"
"v_mfma_f32_16x16x32_bf16 v[92:95], v[224:227], a[172:175], v[92:95]\n"
"s_add_u32 s35, s43, 36864\n"
"v_max_f32 v162, 0, v162\n"
"v_max_f32 v163, 0, v163\n"
"v_mfma_f32_16x16x32_bf16 v[92:95], v[228:231], a[188:191], v[92:95]\n"
"s_waitcnt lgkmcnt(4)\n"
"s_barrier\n"
"v_mul_f32 v160, v160, v176\n"
"v_mfma_f32_16x16x32_bf16 v[96:99], v[232:235], a[160:163], v[96:99]\n"
"ds_read_b64_tr_b16 v[224:225], v249 offset:32768\n"
"s_add_u32 m0, s46, 38016\n"
"v_mul_f32 v161, v161, v177\n"
"v_mfma_f32_16x16x32_bf16 v[96:99], v[236:239], a[176:179], v[96:99]\n"
"buffer_load_dwordx4 v1, s[28:31], s35 offen lds\n"
"v_mfma_f32_16x16x32_bf16 v[100:103], v[232:235], a[164:167], v[100:103]\n"
"ds_read_b64_tr_b16 v[226:227], v249 offset:34816\n"
"v_mul_f32 v162, v162, v178\n"
"v_mul_f32 v163, v163, v179\n"
"v_mfma_f32_16x16x32_bf16 v[100:103], v[236:239], a[180:183], v[100:103]\n"
"ds_read_b64_tr_b16 v[228:229], v249 offset:36864\n"
"v_cvt_pk_bf16_f32 v130, v160, v161\n"
"v_mfma_f32_16x16x32_bf16 v[104:107], v[232:235], a[168:171], v[104:107]\n"
"ds_read_b64_tr_b16 v[230:231], v249 offset:38912\n"
"v_cvt_pk_bf16_f32 v131, v162, v163\n"
"v_mfma_f32_16x16x32_bf16 v[104:107], v[236:239], a[184:187], v[104:107]\n"
"buffer_store_dwordx4 v[128:131], v8, s[36:39], s49 offen sc1\n"
"v_mfma_f32_16x16x32_bf16 v[108:111], v[232:235], a[172:175], v[108:111]\n"
"v_max_f32 v132, 0, v132\n"
"v_max_f32 v133, 0, v133\n"
"v_mfma_f32_16x16x32_bf16 v[108:111], v[236:239], a[188:191], v[108:111]\n"
"v_max_f32 v134, 0, v134\n"
"s_waitcnt lgkmcnt(4)\n"
"v_mfma_f32_16x16x32_bf16 v[112:115], v[240:243], a[160:163], v[112:115]\n"
"ds_read_b64_tr_b16 v[232:233], v249 offset:40960\n"
"v_max_f32 v135, 0, v135\n"
"v_mfma_f32_16x16x32_bf16 v[112:115], v[244:247], a[176:179], v[112:115]\n"
"buffer_load_dwordx4 v2, s[28:31], s35 offen offset:1056 lds\n"
"v_mfma_f32_16x16x32_bf16 v[116:119], v[240:243], a[164:167], v[116:119]\n"
"ds_read_b64_tr_b16 v[234:235], v249 offset:43008\n"
"v_mul_f32 v132, v132, v148\n"
"v_mfma_f32_16x16x32_bf16 v[116:119], v[244:247], a[180:183], v[116:119]\n"
"ds_read_b64_tr_b16 v[236:237], v249 offset:45056\n"
"v_mul_f32 v133, v133, v149\n"
"v_mfma_f32_16x16x32_bf16 v[120:123], v[240:243], a[168:171], v[120:123]\n"
"ds_read_b64_tr_b16 v[238:239], v249 offset:47104\n"
"v_mul_f32 v134, v134, v150\n"
"v_mfma_f32_16x16x32_bf16 v[120:123], v[244:247], a[184:187], v[120:123]\n"
"v_mul_f32 v135, v135, v151\n"
"v_cvt_pk_bf16_f32 v132, v132, v133\n"
"v_mfma_f32_16x16x32_bf16 v[124:127], v[240:243], a[172:175], v[124:127]\n"
"v_cvt_pk_bf16_f32 v133, v134, v135\n"
"v_max_f32 v164, 0, v164\n"
"v_mfma_f32_16x16x32_bf16 v[124:127], v[244:247], a[188:191], v[124:127]\n"
"v_max_f32 v165, 0, v165\n"
"s_waitcnt lgkmcnt(4)\n"
"v_mfma_f32_16x16x32_bf16 v[64:67], v[224:227], a[192:195], v[64:67]\n"
"ds_read_b64_tr_b16 v[240:241], v252 offset:32768\n"
"v_max_f32 v166, 0, v166\n"
"v_mfma_f32_16x16x32_bf16 v[64:67], v[228:231], a[208:211], v[64:67]\n"
"buffer_load_dwordx4 v3, s[28:31], s35 offen offset:2112 lds\n"
"v_mfma_f32_16x16x32_bf16 v[68:71], v[224:227], a[196:199], v[68:71]\n"
"ds_read_b64_tr_b16 v[242:243], v252 offset:34816\n"
"v_max_f32 v167, 0, v167\n"
"v_mfma_f32_16x16x32_bf16 v[68:71], v[228:231], a[212:215], v[68:71]\n"
"ds_read_b64_tr_b16 v[244:245], v252 offset:36864\n"
"v_mul_f32 v164, v164, v180\n"
"v_mfma_f32_16x16x32_bf16 v[72:75], v[224:227], a[200:203], v[72:75]\n"
"ds_read_b64_tr_b16 v[246:247], v252 offset:38912\n"
"v_mul_f32 v165, v165, v181\n"
"v_mfma_f32_16x16x32_bf16 v[72:75], v[228:231], a[216:219], v[72:75]\n"
"buffer_load_dwordx4 v4, s[28:31], s35 offen offset:3168 lds\n"
"v_mfma_f32_16x16x32_bf16 v[76:79], v[224:227], a[204:207], v[76:79]\n"
"v_mul_f32 v166, v166, v182\n"
"v_mul_f32 v167, v167, v183\n"
"v_mfma_f32_16x16x32_bf16 v[76:79], v[228:231], a[220:223], v[76:79]\n"
"s_add_u32 s76, s61, 1024\n"
"s_waitcnt lgkmcnt(4)\n"
"v_mfma_f32_16x16x32_bf16 v[80:83], v[232:235], a[192:195], v[80:83]\n"
"ds_read_b64_tr_b16 v[224:225], v252 offset:40960\n"
"s_add_u32 m0, s18, 71808\n"
"v_mfma_f32_16x16x32_bf16 v[80:83], v[236:239], a[208:211], v[80:83]\n"
"buffer_load_dwordx4 v8, s[36:39], s76 offen sc1 lds\n"
"v_mfma_f32_16x16x32_bf16 v[84:87], v[232:235], a[196:199], v[84:87]\n"
"ds_read_b64_tr_b16 v[226:227], v252 offset:43008\n"
"v_cvt_pk_bf16_f32 v134, v164, v165\n"
"v_mfma_f32_16x16x32_bf16 v[84:87], v[236:239], a[212:215], v[84:87]\n"
"ds_read_b64_tr_b16 v[228:229], v252 offset:45056\n"
"v_cvt_pk_bf16_f32 v135, v166, v167\n"
"v_mfma_f32_16x16x32_bf16 v[88:91], v[232:235], a[200:203], v[88:91]\n"
"ds_read_b64_tr_b16 v[230:231], v252 offset:47104\n"
"v_mfma_f32_16x16x32_bf16 v[88:91], v[236:239], a[216:219], v[88:91]\n"
"buffer_store_dwordx4 v[132:135], v8, s[36:39], s49 offen offset:1024 sc1\n"
"v_mfma_f32_16x16x32_bf16 v[92:95], v[232:235], a[204:207], v[92:95]\n"
"s_add_u32 s75, s43, 41472\n"
"v_max_f32 v136, 0, v136\n"
"v_mfma_f32_16x16x32_bf16 v[92:95], v[236:239], a[220:223], v[92:95]\n"
"s_waitcnt vmcnt(35) lgkmcnt(4)\n"
"s_barrier\n"
"v_max_f32 v137, 0, v137\n"
"v_mfma_f32_16x16x32_bf16 v[96:99], v[240:243], a[192:195], v[96:99]\n"
"ds_read_b64_tr_b16 v[232:233], v249 offset:49152\n"
"s_add_u32 m0, s46, 42240\n"
"v_max_f32 v138, 0, v138\n"
"v_mfma_f32_16x16x32_bf16 v[96:99], v[244:247], a[208:211], v[96:99]\n"
"buffer_load_dwordx4 v1, s[28:31], s75 offen lds\n"
"v_mfma_f32_16x16x32_bf16 v[100:103], v[240:243], a[196:199], v[100:103]\n"
"ds_read_b64_tr_b16 v[234:235], v249 offset:51200\n"
"v_max_f32 v139, 0, v139\n"
"v_mul_f32 v136, v136, v152\n"
"v_mfma_f32_16x16x32_bf16 v[100:103], v[244:247], a[212:215], v[100:103]\n"
"ds_read_b64_tr_b16 v[236:237], v249 offset:53248\n"
"v_mul_f32 v137, v137, v153\n"
"v_mfma_f32_16x16x32_bf16 v[104:107], v[240:243], a[200:203], v[104:107]\n"
"ds_read_b64_tr_b16 v[238:239], v249 offset:55296\n"
"v_mul_f32 v138, v138, v154\n"
"v_mfma_f32_16x16x32_bf16 v[104:107], v[244:247], a[216:219], v[104:107]\n"
"buffer_load_dwordx4 v2, s[28:31], s75 offen offset:1056 lds\n"
"v_mfma_f32_16x16x32_bf16 v[108:111], v[240:243], a[204:207], v[108:111]\n"
"v_mul_f32 v139, v139, v155\n"
"v_mfma_f32_16x16x32_bf16 v[108:111], v[244:247], a[220:223], v[108:111]\n"
"v_cvt_pk_bf16_f32 v136, v136, v137\n"
"s_waitcnt lgkmcnt(4)\n"
"v_mfma_f32_16x16x32_bf16 v[112:115], v[224:227], a[192:195], v[112:115]\n"
"ds_read_b64_tr_b16 v[240:241], v249 offset:57344\n"
"v_cvt_pk_bf16_f32 v137, v138, v139\n"
"v_mfma_f32_16x16x32_bf16 v[112:115], v[228:231], a[208:211], v[112:115]\n"
"buffer_load_dwordx4 v3, s[28:31], s75 offen offset:2112 lds\n"
"v_mfma_f32_16x16x32_bf16 v[116:119], v[224:227], a[196:199], v[116:119]\n"
"ds_read_b64_tr_b16 v[242:243], v249 offset:59392\n"
"v_max_f32 v168, 0, v168\n"
"v_max_f32 v169, 0, v169\n"
"v_mfma_f32_16x16x32_bf16 v[116:119], v[228:231], a[212:215], v[116:119]\n"
"ds_read_b64_tr_b16 v[244:245], v249 offset:61440\n"
"v_max_f32 v170, 0, v170\n"
"v_mfma_f32_16x16x32_bf16 v[120:123], v[224:227], a[200:203], v[120:123]\n"
"ds_read_b64_tr_b16 v[246:247], v249 offset:63488\n"
"v_max_f32 v171, 0, v171\n"
"v_mfma_f32_16x16x32_bf16 v[120:123], v[228:231], a[216:219], v[120:123]\n"
"buffer_load_dwordx4 v4, s[28:31], s75 offen offset:3168 lds\n"
"v_mfma_f32_16x16x32_bf16 v[124:127], v[224:227], a[204:207], v[124:127]\n"
"v_mul_f32 v168, v168, v184\n"
"v_mfma_f32_16x16x32_bf16 v[124:127], v[228:231], a[220:223], v[124:127]\n"
"v_mul_f32 v169, v169, v185\n"
"s_waitcnt lgkmcnt(4)\n"
"v_mfma_f32_16x16x32_bf16 v[64:67], v[232:235], a[224:227], v[64:67]\n"
"ds_read_b64_tr_b16 v[224:225], v252 offset:49152\n"
"v_mul_f32 v170, v170, v186\n"
"v_mfma_f32_16x16x32_bf16 v[64:67], v[236:239], a[240:243], v[64:67]\n"
"buffer_load_dwordx4 a[116:119], v15, s[36:39], s58 offen offset:1024 sc1\n"
"v_mfma_f32_16x16x32_bf16 v[68:71], v[232:235], a[228:231], v[68:71]\n"
"ds_read_b64_tr_b16 v[226:227], v252 offset:51200\n"
"v_mul_f32 v171, v171, v187\n"
"v_cvt_pk_bf16_f32 v138, v168, v169\n"
"v_mfma_f32_16x16x32_bf16 v[68:71], v[236:239], a[244:247], v[68:71]\n"
"ds_read_b64_tr_b16 v[228:229], v252 offset:53248\n"
"s_waitcnt vmcnt(20)\n"
"v_cmp_ne_u32 vcc, s86, v29\n"
"v_cvt_pk_bf16_f32 v139, v170, v171\n"
"v_mfma_f32_16x16x32_bf16 v[72:75], v[232:235], a[232:235], v[72:75]\n"
"ds_read_b64_tr_b16 v[230:231], v252 offset:55296\n"
"v_mfma_f32_16x16x32_bf16 v[72:75], v[236:239], a[248:251], v[72:75]\n"
"buffer_store_dwordx4 v[136:139], v8, s[36:39], s49 offen offset:2048 sc1\n"
"v_mfma_f32_16x16x32_bf16 v[76:79], v[232:235], a[236:239], v[76:79]\n"
"s_cbranch_vccnz SLOWC_%=\n"
"BACKC_%=:\n"
"v_max_f32 v140, 0, v140\n"
"v_max_f32 v141, 0, v141\n"
"v_mfma_f32_16x16x32_bf16 v[76:79], v[236:239], a[252:255], v[76:79]\n"
"v_max_f32 v142, 0, v142\n"
"s_waitcnt lgkmcnt(4)\n"
"v_mfma_f32_16x16x32_bf16 v[80:83], v[240:243], a[224:227], v[80:83]\n"
"ds_read_b64_tr_b16 v[232:233], v252 offset:57344\n"
"v_max_f32 v143, 0, v143\n"
"v_mfma_f32_16x16x32_bf16 v[80:83], v[244:247], a[240:243], v[80:83]\n"
"buffer_load_dwordx4 a[120:123], v16, s[36:39], s58 offen sc1\n"
"v_mfma_f32_16x16x32_bf16 v[84:87], v[240:243], a[228:231], v[84:87]\n"
"ds_read_b64_tr_b16 v[234:235], v252 offset:59392\n"
"v_mul_f32 v140, v140, v156\n"
"v_mul_f32 v141, v141, v157\n"
"v_mfma_f32_16x16x32_bf16 v[84:87], v[244:247], a[244:247], v[84:87]\n"
"ds_read_b64_tr_b16 v[236:237], v252 offset:61440\n"
"v_mul_f32 v142, v142, v158\n"
"v_mfma_f32_16x16x32_bf16 v[88:91], v[240:243], a[232:235], v[88:91]\n"
"ds_read_b64_tr_b16 v[238:239], v252 offset:63488\n"
"v_mul_f32 v143, v143, v159\n"
"v_mfma_f32_16x16x32_bf16 v[88:91], v[244:247], a[248:251], v[88:91]\n"
"buffer_load_dwordx4 a[124:127], v16, s[36:39], s58 offen offset:1024 sc1\n"
"v_mfma_f32_16x16x32_bf16 v[92:95], v[240:243], a[236:239], v[92:95]\n"
"v_cvt_pk_bf16_f32 v140, v140, v141\n"
"v_cvt_pk_bf16_f32 v141, v142, v143\n"
"v_mfma_f32_16x16x32_bf16 v[92:95], v[244:247], a[252:255], v[92:95]\n"
"s_waitcnt lgkmcnt(4)\n"
"s_barrier\n"
"v_max_f32 v172, 0, v172\n"
"v_mfma_f32_16x16x32_bf16 v[96:99], v[224:227], a[224:227], v[96:99]\n"
"ds_read_b64_tr_b16 v[240:241], v250 offset:0\n"
"v_max_f32 v173, 0, v173\n"
"v_mfma_f32_16x16x32_bf16 v[96:99], v[228:231], a[240:243], v[96:99]\n"
"ds_read_b64_tr_b16 v[242:243], v250 offset:2048\n"
"v_max_f32 v174, 0, v174\n"
"v_mfma_f32_16x16x32_bf16 v[100:103], v[224:227], a[228:231], v[100:103]\n"
"ds_read_b64_tr_b16 v[244:245], v250 offset:4096\n"
"v_max_f32 v175, 0, v175\n"
"v_mfma_f32_16x16x32_bf16 v[100:103], v[228:231], a[244:247], v[100:103]\n"
"ds_read_b64_tr_b16 v[246:247], v250 offset:6144\n"
"v_mul_f32 v172, v172, v188\n"
"v_mfma_f32_16x16x32_bf16 v[104:107], v[224:227], a[232:235], v[104:107]\n"
"s_add_u32 m0, s46, 76032\n"
"s_add_u32 s35, s44, 0\n"
"v_mul_f32 v173, v173, v189\n"
"v_mfma_f32_16x16x32_bf16 v[104:107], v[228:231], a[248:251], v[104:107]\n"
"buffer_load_dwordx4 v1, s[28:31], s35 offen lds\n"
"v_mfma_f32_16x16x32_bf16 v[108:111], v[224:227], a[236:239], v[108:111]\n"
"v_mul_f32 v174, v174, v190\n"
"v_mul_f32 v175, v175, v191\n"
"v_mfma_f32_16x16x32_bf16 v[108:111], v[228:231], a[252:255], v[108:111]\n"
"v_cvt_pk_bf16_f32 v142, v172, v173\n"
"s_waitcnt lgkmcnt(4)\n"
"v_mfma_f32_16x16x32_bf16 v[112:115], v[232:235], a[224:227], v[112:115]\n"
"ds_read_b64_tr_b16 v[224:225], v250 offset:8192\n"
"v_cvt_pk_bf16_f32 v143, v174, v175\n"
"v_mfma_f32_16x16x32_bf16 v[112:115], v[236:239], a[240:243], v[112:115]\n"
"buffer_store_dwordx4 v[140:143], v8, s[36:39], s49 offen offset:3072 sc1\n"
"v_mfma_f32_16x16x32_bf16 v[116:119], v[232:235], a[228:231], v[116:119]\n"
"ds_read_b64_tr_b16 v[226:227], v250 offset:10240\n"
"s_add_u32 s49, s49, 16384\n"
"v_mfma_f32_16x16x32_bf16 v[116:119], v[236:239], a[244:247], v[116:119]\n"
"ds_read_b64_tr_b16 v[228:229], v250 offset:12288\n"
"v_mfma_f32_16x16x32_bf16 v[120:123], v[232:235], a[232:235], v[120:123]\n"
"ds_read_b64_tr_b16 v[230:231], v250 offset:14336\n"
"v_mfma_f32_16x16x32_bf16 v[120:123], v[236:239], a[248:251], v[120:123]\n"
"buffer_load_dwordx4 v2, s[28:31], s35 offen offset:1056 lds\n"
"v_mfma_f32_16x16x32_bf16 v[124:127], v[232:235], a[236:239], v[124:127]\n"
"v_mfma_f32_16x16x32_bf16 v[124:127], v[236:239], a[252:255], v[124:127]\n"
"s_waitcnt lgkmcnt(4)\n"
"v_mfma_f32_16x16x32_bf16 v[64:67], v[240:243], v[192:195], v[64:67]\n"
"ds_read_b64_tr_b16 v[232:233], v253 offset:0\n"
"v_mfma_f32_16x16x32_bf16 v[64:67], v[244:247], v[208:211], v[64:67]\n"
"buffer_load_dwordx4 v3, s[28:31], s35 offen offset:2112 lds\n"
"v_mfma_f32_16x16x32_bf16 v[68:71], v[240:243], v[196:199], v[68:71]\n"
"ds_read_b64_tr_b16 v[234:235], v253 offset:2048\n"
"v_mfma_f32_16x16x32_bf16 v[68:71], v[244:247], v[212:215], v[68:71]\n"
"ds_read_b64_tr_b16 v[236:237], v253 offset:4096\n"
"v_mfma_f32_16x16x32_bf16 v[72:75], v[240:243], v[200:203], v[72:75]\n"
"ds_read_b64_tr_b16 v[238:239], v253 offset:6144\n"
"v_mfma_f32_16x16x32_bf16 v[72:75], v[244:247], v[216:219], v[72:75]\n"
"buffer_load_dwordx4 v4, s[28:31], s35 offen offset:3168 lds\n"
"v_mfma_f32_16x16x32_bf16 v[76:79], v[240:243], v[204:207], v[76:79]\n"
"v_mfma_f32_16x16x32_bf16 v[76:79], v[244:247], v[220:223], v[76:79]\n"
"s_add_u32 s76, s17, 0\n"
"s_waitcnt lgkmcnt(4)\n"
"v_mfma_f32_16x16x32_bf16 v[80:83], v[224:227], v[192:195], v[80:83]\n"
"ds_read_b64_tr_b16 v[240:241], v253 offset:8192\n"
"s_add_u32 m0, s18, 109824\n"
"v_mfma_f32_16x16x32_bf16 v[80:83], v[228:231], v[208:211], v[80:83]\n"
"buffer_load_dwordx4 v8, s[36:39], s76 offen sc1 lds\n"
"v_mfma_f32_16x16x32_bf16 v[84:87], v[224:227], v[196:199], v[84:87]\n"
"ds_read_b64_tr_b16 v[242:243], v253 offset:10240\n"
"v_mfma_f32_16x16x32_bf16 v[84:87], v[228:231], v[212:215], v[84:87]\n"
"ds_read_b64_tr_b16 v[244:245], v253 offset:12288\n"
"s_add_u32 s75, s44, 4608\n"
"v_mfma_f32_16x16x32_bf16 v[88:91], v[224:227], v[200:203], v[88:91]\n"
"ds_read_b64_tr_b16 v[246:247], v253 offset:14336\n"
"s_add_u32 m0, s46, 80256\n"
"v_mfma_f32_16x16x32_bf16 v[88:91], v[228:231], v[216:219], v[88:91]\n"
"buffer_load_dwordx4 v1, s[28:31], s75 offen lds\n"
"v_mfma_f32_16x16x32_bf16 v[92:95], v[224:227], v[204:207], v[92:95]\n"
"v_mfma_f32_16x16x32_bf16 v[92:95], v[228:231], v[220:223], v[92:95]\n"
"s_waitcnt vmcnt(27) lgkmcnt(4)\n"
"s_barrier\n"
"v_mfma_f32_16x16x32_bf16 v[96:99], v[232:235], v[192:195], v[96:99]\n"
"ds_read_b64_tr_b16 v[24:25], v10 offset:0\n"
"ds_read_b64_tr_b16 v[26:27], v10 offset:512\n"
"v_mfma_f32_16x16x32_bf16 v[96:99], v[236:239], v[208:211], v[96:99]\n"
"buffer_load_dwordx4 v2, s[28:31], s75 offen offset:1056 lds\n"
"v_mfma_f32_16x16x32_bf16 v[100:103], v[232:235], v[196:199], v[100:103]\n"
"ds_read_b64_tr_b16 v[28:29], v10 offset:8448\n"
"ds_read_b64_tr_b16 v[30:31], v10 offset:8960\n"
"v_max_f32 v64, 0, v64\n"
"v_max_f32 v65, 0, v65\n"
"v_mfma_f32_16x16x32_bf16 v[100:103], v[236:239], v[212:215], v[100:103]\n"
"ds_read_b64_tr_b16 v[32:33], v10 offset:16896\n"
"v_max_f32 v66, 0, v66\n"
"v_mfma_f32_16x16x32_bf16 v[104:107], v[232:235], v[200:203], v[104:107]\n"
"ds_read_b64_tr_b16 v[34:35], v10 offset:17408\n"
"v_max_f32 v67, 0, v67\n"
"v_mfma_f32_16x16x32_bf16 v[104:107], v[236:239], v[216:219], v[104:107]\n"
"buffer_load_dwordx4 v3, s[28:31], s75 offen offset:2112 lds\n"
"v_mfma_f32_16x16x32_bf16 v[108:111], v[232:235], v[204:207], v[108:111]\n"
"ds_read_b64_tr_b16 v[36:37], v10 offset:25344\n"
"ds_read_b64_tr_b16 v[38:39], v10 offset:25856\n"
"v_mul_f32 v64, v64, v80\n"
"v_mul_f32 v65, v65, v81\n"
"v_mfma_f32_16x16x32_bf16 v[108:111], v[236:239], v[220:223], v[108:111]\n"
"ds_read_b64_tr_b16 v[40:41], v10 offset:32\n"
"v_mul_f32 v66, v66, v82\n"
"v_mul_f32 v67, v67, v83\n"
"s_waitcnt lgkmcnt(9)\n"
"v_mfma_f32_16x16x32_bf16 v[112:115], v[240:243], v[192:195], v[112:115]\n"
"ds_read_b64_tr_b16 v[42:43], v10 offset:544\n"
"v_cvt_pk_bf16_f32 v64, v64, v65\n"
"v_cvt_pk_bf16_f32 v65, v66, v67\n"
"v_mfma_f32_16x16x32_bf16 v[112:115], v[244:247], v[208:211], v[112:115]\n"
"buffer_load_dwordx4 v4, s[28:31], s75 offen offset:3168 lds\n"
"v_mfma_f32_16x16x32_bf16 v[116:119], v[240:243], v[196:199], v[116:119]\n"
"ds_read_b64_tr_b16 v[44:45], v10 offset:8480\n"
"ds_read_b64_tr_b16 v[46:47], v10 offset:8992\n"
"v_max_f32 v68, 0, v68\n"
"v_max_f32 v69, 0, v69\n"
"v_mfma_f32_16x16x32_bf16 v[116:119], v[244:247], v[212:215], v[116:119]\n"
"ds_read_b64_tr_b16 v[48:49], v10 offset:16928\n"
"v_max_f32 v70, 0, v70\n"
"v_max_f32 v71, 0, v71\n"
"v_mfma_f32_16x16x32_bf16 v[120:123], v[240:243], v[200:203], v[120:123]\n"
"ds_read_b64_tr_b16 v[50:51], v10 offset:17440\n"
"s_add_u32 s58, s57, 24576\n"
"v_mul_f32 v68, v68, v84\n"
"v_mfma_f32_16x16x32_bf16 v[120:123], v[244:247], v[216:219], v[120:123]\n"
"buffer_load_dwordx4 a[128:131], v13, s[36:39], s58 offen sc1\n"
"v_mfma_f32_16x16x32_bf16 v[124:127], v[240:243], v[204:207], v[124:127]\n"
"ds_read_b64_tr_b16 v[52:53], v10 offset:25376\n"
"ds_read_b64_tr_b16 v[54:55], v10 offset:25888\n"
"v_mul_f32 v69, v69, v85\n"
"v_mul_f32 v70, v70, v86\n"
"v_mfma_f32_16x16x32_bf16 v[124:127], v[244:247], v[220:223], v[124:127]\n"
"ds_read_b64_tr_b16 v[224:225], v10 offset:64\n"
"v_mul_f32 v71, v71, v87\n"
"v_cvt_pk_bf16_f32 v68, v68, v69\n"
"s_waitcnt lgkmcnt(9)\n"
"v_mfma_f32_16x16x32_bf16 v[128:131], v[24:27], a[0:3], 0\n"
"ds_read_b64_tr_b16 v[226:227], v10 offset:576\n"
"v_cvt_pk_bf16_f32 v69, v70, v71\n"
"v_mfma_f32_16x16x32_bf16 v[128:131], v[28:31], a[8:11], v[128:131]\n"
"ds_read_b64_tr_b16 v[228:229], v10 offset:8512\n"
"v_max_f32 v72, 0, v72\n"
"v_max_f32 v73, 0, v73\n"
"v_mfma_f32_16x16x32_bf16 v[128:131], v[32:35], a[16:19], v[128:131]\n"
"ds_read_b64_tr_b16 v[230:231], v10 offset:9024\n"
"v_max_f32 v74, 0, v74\n"
"v_mfma_f32_16x16x32_bf16 v[128:131], v[36:39], a[24:27], v[128:131]\n"
"ds_read_b64_tr_b16 v[232:233], v10 offset:16960\n"
"v_max_f32 v75, 0, v75\n"
"v_mfma_f32_16x16x32_bf16 v[132:135], v[24:27], a[4:7], 0\n"
"ds_read_b64_tr_b16 v[234:235], v10 offset:17472\n"
"v_mul_f32 v72, v72, v88\n"
"v_mul_f32 v73, v73, v89\n"
"v_mfma_f32_16x16x32_bf16 v[132:135], v[28:31], a[12:15], v[132:135]\n"
"buffer_load_dwordx4 a[132:135], v13, s[36:39], s58 offen offset:1024 sc1\n"
"v_mfma_f32_16x16x32_bf16 v[132:135], v[32:35], a[20:23], v[132:135]\n"
"ds_read_b64_tr_b16 v[236:237], v10 offset:25408\n"
"ds_read_b64_tr_b16 v[238:239], v10 offset:25920\n"
"v_mul_f32 v74, v74, v90\n"
"v_mul_f32 v75, v75, v91\n"
"v_mfma_f32_16x16x32_bf16 v[132:135], v[36:39], a[28:31], v[132:135]\n"
"ds_read_b64_tr_b16 v[24:25], v10 offset:96\n"
"v_cvt_pk_bf16_f32 v72, v72, v73\n"
"v_cvt_pk_bf16_f32 v73, v74, v75\n"
"s_waitcnt lgkmcnt(9)\n"
"v_mfma_f32_16x16x32_bf16 v[136:139], v[40:43], a[0:3], 0\n"
"ds_read_b64_tr_b16 v[26:27], v10 offset:608\n"
"v_max_f32 v76, 0, v76\n"
"v_mfma_f32_16x16x32_bf16 v[136:139], v[44:47], a[8:11], v[136:139]\n"
"buffer_load_dwordx4 a[136:139], v14, s[36:39], s58 offen sc1\n"
"v_mfma_f32_16x16x32_bf16 v[136:139], v[48:51], a[16:19], v[136:139]\n"
"ds_read_b64_tr_b16 v[28:29], v10 offset:8544\n"
"ds_read_b64_tr_b16 v[30:31], v10 offset:9056\n"
"v_max_f32 v77, 0, v77\n"
"v_max_f32 v78, 0, v78\n"
"v_mfma_f32_16x16x32_bf16 v[136:139], v[52:55], a[24:27], v[136:139]\n"
"ds_read_b64_tr_b16 v[32:33], v10 offset:16992\n"
"v_max_f32 v79, 0, v79\n"
"v_mul_f32 v76, v76, v92\n"
"v_mfma_f32_16x16x32_bf16 v[140:143], v[40:43], a[4:7], 0\n"
"ds_read_b64_tr_b16 v[34:35], v10 offset:17504\n"
"v_mul_f32 v77, v77, v93\n"
"v_mfma_f32_16x16x32_bf16 v[140:143], v[44:47], a[12:15], v[140:143]\n"
"buffer_load_dwordx4 a[140:143], v14, s[36:39], s58 offen offset:1024 sc1\n"
"v_mfma_f32_16x16x32_bf16 v[140:143], v[48:51], a[20:23], v[140:143]\n"
"ds_read_b64_tr_b16 v[36:37], v10 offset:25440\n"
"ds_read_b64_tr_b16 v[38:39], v10 offset:25952\n"
"v_mul_f32 v78, v78, v94\n"
"v_mul_f32 v79, v79, v95\n"
"v_mfma_f32_16x16x32_bf16 v[140:143], v[52:55], a[28:31], v[140:143]\n"
"ds_read_b64_tr_b16 v[40:41], v10 offset:128\n"
"v_cvt_pk_bf16_f32 v76, v76, v77\n"
"v_cvt_pk_bf16_f32 v77, v78, v79\n"
"s_waitcnt lgkmcnt(9)\n"
"v_mfma_f32_16x16x32_bf16 v[144:147], v[224:227], a[0:3], 0\n"
"ds_read_b64_tr_b16 v[42:43], v10 offset:640\n"
"v_max_f32 v96, 0, v96\n"
"v_max_f32 v97, 0, v97\n"
"v_mfma_f32_16x16x32_bf16 v[144:147], v[228:231], a[8:11], v[144:147]\n"
"buffer_load_dwordx4 a[144:147], v15, s[36:39], s58 offen sc1\n"
"v_mfma_f32_16x16x32_bf16 v[144:147], v[232:235], a[16:19], v[144:147]\n"
"ds_read_b64_tr_b16 v[44:45], v10 offset:8576\n"
"ds_read_b64_tr_b16 v[46:47], v10 offset:9088\n"
"v_max_f32 v98, 0, v98\n"
"v_mfma_f32_16x16x32_bf16 v[144:147], v[236:239], a[24:27], v[144:147]\n"
"ds_read_b64_tr_b16 v[48:49], v10 offset:17024\n"
"v_max_f32 v99, 0, v99\n"
"v_mul_f32 v96, v96, v112\n"
"v_mfma_f32_16x16x32_bf16 v[148:151], v[224:227], a[4:7], 0\n"
"ds_read_b64_tr_b16 v[50:51], v10 offset:17536\n"
"v_mul_f32 v97, v97, v113\n"
"v_mfma_f32_16x16x32_bf16 v[148:151], v[228:231], a[12:15], v[148:151]\n"
"buffer_load_dwordx4 a[148:151], v15, s[36:39], s58 offen offset:1024 sc1\n"
"v_mfma_f32_16x16x32_bf16 v[148:151], v[232:235], a[20:23], v[148:151]\n"
"ds_read_b64_tr_b16 v[52:53], v10 offset:25472\n"
"ds_read_b64_tr_b16 v[54:55], v10 offset:25984\n"
"v_mul_f32 v98, v98, v114\n"
"v_mfma_f32_16x16x32_bf16 v[148:151], v[236:239], a[28:31], v[148:151]\n"
"ds_read_b64_tr_b16 v[224:225], v10 offset:160\n"
"v_mul_f32 v99, v99, v115\n"
"s_waitcnt lgkmcnt(9)\n"
"v_mfma_f32_16x16x32_bf16 v[152:155], v[24:27], a[0:3], 0\n"
"ds_read_b64_tr_b16 v[226:227], v10 offset:672\n"
"v_cvt_pk_bf16_f32 v66, v96, v97\n"
"v_cvt_pk_bf16_f32 v67, v98, v99\n"
"v_mfma_f32_16x16x32_bf16 v[152:155], v[28:31], a[8:11], v[152:155]\n"
"buffer_store_dwordx4 v[64:67], v8, s[36:39], s49 offen sc1\n"
"v_mfma_f32_16x16x32_bf16 v[152:155], v[32:35], a[16:19], v[152:155]\n"
"ds_read_b64_tr_b16 v[228:229], v10 offset:8608\n"
"ds_read_b64_tr_b16 v[230:231], v10 offset:9120\n"
"v_max_f32 v100, 0, v100\n"
"v_mfma_f32_16x16x32_bf16 v[152:155], v[36:39], a[24:27], v[152:155]\n"
"ds_read_b64_tr_b16 v[232:233], v10 offset:17056\n"
"v_max_f32 v101, 0, v101\n"
"v_mfma_f32_16x16x32_bf16 v[156:159], v[24:27], a[4:7], 0\n"
"ds_read_b64_tr_b16 v[234:235], v10 offset:17568\n"
"v_max_f32 v102, 0, v102\n"
"v_mfma_f32_16x16x32_bf16 v[156:159], v[28:31], a[12:15], v[156:159]\n"
"buffer_load_dwordx4 a[152:155], v16, s[36:39], s58 offen sc1\n"
"v_mfma_f32_16x16x32_bf16 v[156:159], v[32:35], a[20:23], v[156:159]\n"
"ds_read_b64_tr_b16 v[236:237], v10 offset:25504\n"
"ds_read_b64_tr_b16 v[238:239], v10 offset:26016\n"
"v_max_f32 v103, 0, v103\n"
"v_mfma_f32_16x16x32_bf16 v[156:159], v[36:39], a[28:31], v[156:159]\n"
"ds_read_b64_tr_b16 v[24:25], v10 offset:192\n"
"v_mul_f32 v100, v100, v116\n"
"s_waitcnt lgkmcnt(9)\n"
"v_mfma_f32_16x16x32_bf16 v[160:163], v[40:43], a[0:3], 0\n"
"ds_read_b64_tr_b16 v[26:27], v10 offset:704\n"
"v_mul_f32 v101, v101, v117\n"
"v_mul_f32 v102, v102, v118\n"
"v_mfma_f32_16x16x32_bf16 v[160:163], v[44:47], a[8:11], v[160:163]\n"
"buffer_load_dwordx4 a[156:159], v16, s[36:39], s58 offen offset:1024 sc1\n"
"v_mfma_f32_16x16x32_bf16 v[160:163], v[48:51], a[16:19], v[160:163]\n"
"ds_read_b64_tr_b16 v[28:29], v10 offset:8640\n"
"ds_read_b64_tr_b16 v[30:31], v10 offset:9152\n"
"v_mul_f32 v103, v103, v119\n"
"v_mfma_f32_16x16x32_bf16 v[160:163], v[52:55], a[24:27], v[160:163]\n"
"ds_read_b64_tr_b16 v[32:33], v10 offset:17088\n"
"v_cvt_pk_bf16_f32 v70, v100, v101\n"
"v_cvt_pk_bf16_f32 v71, v102, v103\n"
"v_mfma_f32_16x16x32_bf16 v[164:167], v[40:43], a[4:7], 0\n"
"ds_read_b64_tr_b16 v[34:35], v10 offset:17600\n"
"v_mfma_f32_16x16x32_bf16 v[164:167], v[44:47], a[12:15], v[164:167]\n"
"buffer_store_dwordx4 v[68:71], v8, s[36:39], s49 offen offset:1024 sc1\n"
"v_mfma_f32_16x16x32_bf16 v[164:167], v[48:51], a[20:23], v[164:167]\n"
"ds_read_b64_tr_b16 v[36:37], v10 offset:25536\n"
"ds_read_b64_tr_b16 v[38:39], v10 offset:26048\n"
"v_max_f32 v104, 0, v104\n"
"v_mfma_f32_16x16x32_bf16 v[164:167], v[52:55], a[28:31], v[164:167]\n"
"ds_read_b64_tr_b16 v[40:41], v10 offset:224\n"
"v_max_f32 v105, 0, v105\n"
"s_waitcnt lgkmcnt(9)\n"
"v_mfma_f32_16x16x32_bf16 v[168:171], v[224:227], a[0:3], 0\n"
"ds_read_b64_tr_b16 v[42:43], v10 offset:736\n"
"v_max_f32 v106, 0, v106\n"
"v_mfma_f32_16x16x32_bf16 v[168:171], v[228:231], a[8:11], v[168:171]\n"
"ds_read_b64_tr_b16 v[44:45], v10 offset:8672\n"
"v_max_f32 v107, 0, v107\n"
"v_mfma_f32_16x16x32_bf16 v[168:171], v[232:235], a[16:19], v[168:171]\n"
"ds_read_b64_tr_b16 v[46:47], v10 offset:9184\n"
"v_mul_f32 v104, v104, v120\n"
"v_mfma_f32_16x16x32_bf16 v[168:171], v[236:239], a[24:27], v[168:171]\n"
"ds_read_b64_tr_b16 v[48:49], v10 offset:17120\n"
"v_mul_f32 v105, v105, v121\n"
"v_mfma_f32_16x16x32_bf16 v[172:175], v[224:227], a[4:7], 0\n"
"ds_read_b64_tr_b16 v[50:51], v10 offset:17632\n"
"v_mul_f32 v106, v106, v122\n"
"v_mfma_f32_16x16x32_bf16 v[172:175], v[228:231], a[12:15], v[172:175]\n"
"ds_read_b64_tr_b16 v[52:53], v10 offset:25568\n"
"v_mul_f32 v107, v107, v123\n"
"v_mfma_f32_16x16x32_bf16 v[172:175], v[232:235], a[20:23], v[172:175]\n"
"ds_read_b64_tr_b16 v[54:55], v10 offset:26080\n"
"v_cvt_pk_bf16_f32 v74, v104, v105\n"
"v_mfma_f32_16x16x32_bf16 v[172:175], v[236:239], a[28:31], v[172:175]\n"
"ds_read_b64_tr_b16 v[224:225], v6 offset:0\n"
"v_cvt_pk_bf16_f32 v75, v106, v107\n"
"s_waitcnt lgkmcnt(9)\n"
"v_mfma_f32_16x16x32_bf16 v[176:179], v[24:27], a[0:3], 0\n"
"ds_read_b64_tr_b16 v[226:227], v6 offset:512\n"
"v_mfma_f32_16x16x32_bf16 v[176:179], v[28:31], a[8:11], v[176:179]\n"
"buffer_store_dwordx4 v[72:75], v8, s[36:39], s49 offen offset:2048 sc1\n"
"v_mfma_f32_16x16x32_bf16 v[176:179], v[32:35], a[16:19], v[176:179]\n"
"ds_read_b64_tr_b16 v[228:229], v6 offset:1056\n"
"ds_read_b64_tr_b16 v[230:231], v6 offset:1568\n"
"v_max_f32 v108, 0, v108\n"
"v_mfma_f32_16x16x32_bf16 v[176:179], v[36:39], a[24:27], v[176:179]\n"
"ds_read_b64_tr_b16 v[232:233], v6 offset:2112\n"
"v_max_f32 v109, 0, v109\n"
"v_mfma_f32_16x16x32_bf16 v[180:183], v[24:27], a[4:7], 0\n"
"ds_read_b64_tr_b16 v[234:235], v6 offset:2624\n"
"v_max_f32 v110, 0, v110\n"
"v_mfma_f32_16x16x32_bf16 v[180:183], v[28:31], a[12:15], v[180:183]\n"
"ds_read_b64_tr_b16 v[236:237], v6 offset:3168\n"
"v_max_f32 v111, 0, v111\n"
"v_mfma_f32_16x16x32_bf16 v[180:183], v[32:35], a[20:23], v[180:183]\n"
"ds_read_b64_tr_b16 v[238:239], v6 offset:3680\n"
"v_mul_f32 v108, v108, v124\n"
"v_mfma_f32_16x16x32_bf16 v[180:183], v[36:39], a[28:31], v[180:183]\n"
"s_waitcnt vmcnt(19) lgkmcnt(8)\n"
"s_barrier\n"
"v_mul_f32 v109, v109, v125\n"
"v_mfma_f32_16x16x32_bf16 v[184:187], v[40:43], a[0:3], 0\n"
"ds_read_b64_tr_b16 v[24:25], v10 offset:38016\n"
"ds_read_b64_tr_b16 v[26:27], v10 offset:38528\n"
"v_mul_f32 v110, v110, v126\n"
"v_mfma_f32_16x16x32_bf16 v[184:187], v[44:47], a[8:11], v[184:187]\n"
"buffer_store_dword v19, v18, s[36:39], 0 offen offset:48 sc1\n"
"v_mfma_f32_16x16x32_bf16 v[184:187], v[48:51], a[16:19], v[184:187]\n"
"ds_read_b64_tr_b16 v[28:29], v10 offset:46464\n"
"ds_read_b64_tr_b16 v[30:31], v10 offset:46976\n"
"v_mul_f32 v111, v111, v127\n"
"v_mfma_f32_16x16x32_bf16 v[184:187], v[52:55], a[24:27], v[184:187]\n"
"ds_read_b64_tr_b16 v[32:33], v10 offset:54912\n"
"s_add_u32 s35, s43, 73728\n"
"v_cvt_pk_bf16_f32 v78, v108, v109\n"
"v_mfma_f32_16x16x32_bf16 v[188:191], v[40:43], a[4:7], 0\n"
"ds_read_b64_tr_b16 v[34:35], v10 offset:55424\n"
"s_add_u32 m0, s46, 114048\n"
"v_cvt_pk_bf16_f32 v79, v110, v111\n"
"v_mfma_f32_16x16x32_bf16 v[188:191], v[44:47], a[12:15], v[188:191]\n"
"buffer_store_dwordx4 v[76:79], v8, s[36:39], s49 offen offset:3072 sc1\n"
"v_mfma_f32_16x16x32_bf16 v[188:191], v[48:51], a[20:23], v[188:191]\n"
"ds_read_b64_tr_b16 v[36:37], v10 offset:63360\n"
"ds_read_b64_tr_b16 v[38:39], v10 offset:63872\n"
"v_mfma_f32_16x16x32_bf16 v[188:191], v[52:55], a[28:31], v[188:191]\n"
"ds_read_b64_tr_b16 v[40:41], v10 offset:38048\n"
"s_waitcnt lgkmcnt(9)\n"
"v_mfma_f32_16x16x32_bf16 v[120:123], v[224:227], a[0:3], 0\n"
"s_barrier\n"
"ds_read_b64_tr_b16 v[42:43], v10 offset:38560\n"
"v_mfma_f32_16x16x32_bf16 v[120:123], v[228:231], a[8:11], v[120:123]\n"
"buffer_load_dwordx4 v1, s[28:31], s35 offen lds\n"
"v_mfma_f32_16x16x32_bf16 v[120:123], v[232:235], a[16:19], v[120:123]\n"
"ds_read_b64_tr_b16 v[44:45], v10 offset:46496\n"
"ds_read_b64_tr_b16 v[46:47], v10 offset:47008\n"
"v_mfma_f32_16x16x32_bf16 v[120:123], v[236:239], a[24:27], v[120:123]\n"
"ds_read_b64_tr_b16 v[48:49], v10 offset:54944\n"
"v_mfma_f32_16x16x32_bf16 v[124:127], v[224:227], a[4:7], 0\n"
"ds_read_b64_tr_b16 v[50:51], v10 offset:55456\n"
"v_mfma_f32_16x16x32_bf16 v[124:127], v[228:231], a[12:15], v[124:127]\n"
"buffer_load_dwordx4 v2, s[28:31], s35 offen offset:1056 lds\n"
"v_mfma_f32_16x16x32_bf16 v[124:127], v[232:235], a[20:23], v[124:127]\n"
"ds_read_b64_tr_b16 v[52:53], v10 offset:63392\n"
"ds_read_b64_tr_b16 v[54:55], v10 offset:63904\n"
"v_mfma_f32_16x16x32_bf16 v[124:127], v[236:239], a[28:31], v[124:127]\n"
"ds_read_b64_tr_b16 v[224:225], v10 offset:38080\n"
"s_waitcnt lgkmcnt(9)\n"
"v_mfma_f32_16x16x32_bf16 v[128:131], v[24:27], a[32:35], v[128:131]\n"
"ds_read_b64_tr_b16 v[226:227], v10 offset:38592\n"
"v_mfma_f32_16x16x32_bf16 v[128:131], v[28:31], a[40:43], v[128:131]\n"
"buffer_load_dwordx4 v3, s[28:31], s35 offen offset:2112 lds\n"
"v_mfma_f32_16x16x32_bf16 v[128:131], v[32:35], a[48:51], v[128:131]\n"
"ds_read_b64_tr_b16 v[228:229], v10 offset:46528\n"
"ds_read_b64_tr_b16 v[230:231], v10 offset:47040\n"
"v_mfma_f32_16x16x32_bf16 v[128:131], v[36:39], a[56:59], v[128:131]\n"
"ds_read_b64_tr_b16 v[232:233], v10 offset:54976\n"
"v_mfma_f32_16x16x32_bf16 v[132:135], v[24:27], a[36:39], v[132:135]\n"
"ds_read_b64_tr_b16 v[234:235], v10 offset:55488\n"
"v_mfma_f32_16x16x32_bf16 v[132:135], v[28:31], a[44:47], v[132:135]\n"
"ds_read_b64_tr_b16 v[236:237], v10 offset:63424\n"
"v_mfma_f32_16x16x32_bf16 v[132:135], v[32:35], a[52:55], v[132:135]\n"
"ds_read_b64_tr_b16 v[238:239], v10 offset:63936\n"
"v_mfma_f32_16x16x32_bf16 v[132:135], v[36:39], a[60:63], v[132:135]\n"
"ds_read_b64_tr_b16 v[24:25], v10 offset:38112\n"
"s_waitcnt lgkmcnt(9)\n"
"v_mfma_f32_16x16x32_bf16 v[136:139], v[40:43], a[32:35], v[136:139]\n"
"ds_read_b64_tr_b16 v[26:27], v10 offset:38624\n"
"s_add_u32 s75, s43, 78336\n"
"v_mfma_f32_16x16x32_bf16 v[136:139], v[44:47], a[40:43], v[136:139]\n"
"buffer_load_dwordx4 v4, s[28:31], s35 offen offset:3168 lds\n"
"v_mfma_f32_16x16x32_bf16 v[136:139], v[48:51], a[48:51], v[136:139]\n"
"ds_read_b64_tr_b16 v[28:29], v10 offset:46560\n"
"ds_read_b64_tr_b16 v[30:31], v10 offset:47072\n"
"v_mfma_f32_16x16x32_bf16 v[136:139], v[52:55], a[56:59], v[136:139]\n"
"ds_read_b64_tr_b16 v[32:33], v10 offset:55008\n"
"s_add_u32 s76, s61, 2048\n"
"v_mfma_f32_16x16x32_bf16 v[140:143], v[40:43], a[36:39], v[140:143]\n"
"ds_read_b64_tr_b16 v[34:35], v10 offset:55520\n"
"s_add_u32 m0, s18, 147840\n"
"v_mfma_f32_16x16x32_bf16 v[140:143], v[44:47], a[44:47], v[140:143]\n"
"buffer_load_dwordx4 v8, s[36:39], s76 offen sc1 lds\n"
"v_mfma_f32_16x16x32_bf16 v[140:143], v[48:51], a[52:55], v[140:143]\n"
"ds_read_b64_tr_b16 v[36:37], v10 offset:63456\n"
"ds_read_b64_tr_b16 v[38:39], v10 offset:63968\n"
"v_mfma_f32_16x16x32_bf16 v[140:143], v[52:55], a[60:63], v[140:143]\n"
"ds_read_b64_tr_b16 v[40:41], v10 offset:38144\n"
"s_waitcnt lgkmcnt(9)\n"
"v_mfma_f32_16x16x32_bf16 v[144:147], v[224:227], a[32:35], v[144:147]\n"
"ds_read_b64_tr_b16 v[42:43], v10 offset:38656\n"
"s_add_u32 m0, s46, 118272\n"
"v_mfma_f32_16x16x32_bf16 v[144:147], v[228:231], a[40:43], v[144:147]\n"
"buffer_load_dwordx4 v1, s[28:31], s75 offen lds\n"
"v_mfma_f32_16x16x32_bf16 v[144:147], v[232:235], a[48:51], v[144:147]\n"
"ds_read_b64_tr_b16 v[44:45], v10 offset:46592\n"
"ds_read_b64_tr_b16 v[46:47], v10 offset:47104\n"
"v_mfma_f32_16x16x32_bf16 v[144:147], v[236:239], a[56:59], v[144:147]\n"
"ds_read_b64_tr_b16 v[48:49], v10 offset:55040\n"
"v_mfma_f32_16x16x32_bf16 v[148:151], v[224:227], a[36:39], v[148:151]\n"
"ds_read_b64_tr_b16 v[50:51], v10 offset:55552\n"
"v_mfma_f32_16x16x32_bf16 v[148:151], v[228:231], a[44:47], v[148:151]\n"
"buffer_load_dwordx4 v2, s[28:31], s75 offen offset:1056 lds\n"
"v_mfma_f32_16x16x32_bf16 v[148:151], v[232:235], a[52:55], v[148:151]\n"
"ds_read_b64_tr_b16 v[52:53], v10 offset:63488\n"
"ds_read_b64_tr_b16 v[54:55], v10 offset:64000\n"
"v_mfma_f32_16x16x32_bf16 v[148:151], v[236:239], a[60:63], v[148:151]\n"
"ds_read_b64_tr_b16 v[224:225], v10 offset:38176\n"
"s_waitcnt lgkmcnt(9)\n"
"v_mfma_f32_16x16x32_bf16 v[152:155], v[24:27], a[32:35], v[152:155]\n"
"ds_read_b64_tr_b16 v[226:227], v10 offset:38688\n"
"v_mfma_f32_16x16x32_bf16 v[152:155], v[28:31], a[40:43], v[152:155]\n"
"buffer_load_dwordx4 v3, s[28:31], s75 offen offset:2112 lds\n"
"v_mfma_f32_16x16x32_bf16 v[152:155], v[32:35], a[48:51], v[152:155]\n"
"ds_read_b64_tr_b16 v[228:229], v10 offset:46624\n"
"ds_read_b64_tr_b16 v[230:231], v10 offset:47136\n"
"v_mfma_f32_16x16x32_bf16 v[152:155], v[36:39], a[56:59], v[152:155]\n"
"ds_read_b64_tr_b16 v[232:233], v10 offset:55072\n"
"v_mfma_f32_16x16x32_bf16 v[156:159], v[24:27], a[36:39], v[156:159]\n"
"ds_read_b64_tr_b16 v[234:235], v10 offset:55584\n"
"s_add_u32 s35, s43, 110592\n"
"v_mfma_f32_16x16x32_bf16 v[156:159], v[28:31], a[44:47], v[156:159]\n"
"buffer_load_dwordx4 v4, s[28:31], s75 offen offset:3168 lds\n"
"v_mfma_f32_16x16x32_bf16 v[156:159], v[32:35], a[52:55], v[156:159]\n"
"ds_read_b64_tr_b16 v[236:237], v10 offset:63520\n"
"ds_read_b64_tr_b16 v[238:239], v10 offset:64032\n"
"v_mfma_f32_16x16x32_bf16 v[156:159], v[36:39], a[60:63], v[156:159]\n"
"ds_read_b64_tr_b16 v[24:25], v10 offset:38208\n"
"s_waitcnt lgkmcnt(9)\n"
"v_mfma_f32_16x16x32_bf16 v[160:163], v[40:43], a[32:35], v[160:163]\n"
"ds_read_b64_tr_b16 v[26:27], v10 offset:38720\n"
"s_add_u32 m0, s46, 0\n"
"v_mfma_f32_16x16x32_bf16 v[160:163], v[44:47], a[40:43], v[160:163]\n"
"buffer_load_dwordx4 v1, s[28:31], s35 offen lds\n"
"v_mfma_f32_16x16x32_bf16 v[160:163], v[48:51], a[48:51], v[160:163]\n"
"ds_read_b64_tr_b16 v[28:29], v10 offset:46656\n"
"ds_read_b64_tr_b16 v[30:31], v10 offset:47168\n"
"v_mfma_f32_16x16x32_bf16 v[160:163], v[52:55], a[56:59], v[160:163]\n"
"ds_read_b64_tr_b16 v[32:33], v10 offset:55104\n"
"v_mfma_f32_16x16x32_bf16 v[164:167], v[40:43], a[36:39], v[164:167]\n"
"ds_read_b64_tr_b16 v[34:35], v10 offset:55616\n"
"v_mfma_f32_16x16x32_bf16 v[164:167], v[44:47], a[44:47], v[164:167]\n"
"buffer_load_dwordx4 v2, s[28:31], s35 offen offset:1056 lds\n"
"v_mfma_f32_16x16x32_bf16 v[164:167], v[48:51], a[52:55], v[164:167]\n"
"ds_read_b64_tr_b16 v[36:37], v10 offset:63552\n"
"ds_read_b64_tr_b16 v[38:39], v10 offset:64064\n"
"v_mfma_f32_16x16x32_bf16 v[164:167], v[52:55], a[60:63], v[164:167]\n"
"ds_read_b64_tr_b16 v[40:41], v10 offset:38240\n"
"s_waitcnt lgkmcnt(9)\n"
"v_mfma_f32_16x16x32_bf16 v[168:171], v[224:227], a[32:35], v[168:171]\n"
"ds_read_b64_tr_b16 v[42:43], v10 offset:38752\n"
"v_mfma_f32_16x16x32_bf16 v[168:171], v[228:231], a[40:43], v[168:171]\n"
"buffer_load_dwordx4 v3, s[28:31], s35 offen offset:2112 lds\n"
"v_mfma_f32_16x16x32_bf16 v[168:171], v[232:235], a[48:51], v[168:171]\n"
"ds_read_b64_tr_b16 v[44:45], v10 offset:46688\n"
"ds_read_b64_tr_b16 v[46:47], v10 offset:47200\n"
"v_mfma_f32_16x16x32_bf16 v[168:171], v[236:239], a[56:59], v[168:171]\n"
"ds_read_b64_tr_b16 v[48:49], v10 offset:55136\n"
"v_mfma_f32_16x16x32_bf16 v[172:175], v[224:227], a[36:39], v[172:175]\n"
"ds_read_b64_tr_b16 v[50:51], v10 offset:55648\n"
"v_mfma_f32_16x16x32_bf16 v[172:175], v[228:231], a[44:47], v[172:175]\n"
"buffer_load_dwordx4 v4, s[28:31], s35 offen offset:3168 lds\n"
"v_mfma_f32_16x16x32_bf16 v[172:175], v[232:235], a[52:55], v[172:175]\n"
"ds_read_b64_tr_b16 v[52:53], v10 offset:63584\n"
"ds_read_b64_tr_b16 v[54:55], v10 offset:64096\n"
"v_mfma_f32_16x16x32_bf16 v[172:175], v[236:239], a[60:63], v[172:175]\n"
"ds_read_b64_tr_b16 v[224:225], v6 offset:38016\n"
"s_waitcnt lgkmcnt(9)\n"
"v_mfma_f32_16x16x32_bf16 v[176:179], v[24:27], a[32:35], v[176:179]\n"
"ds_read_b64_tr_b16 v[226:227], v6 offset:38528\n"
"v_mfma_f32_16x16x32_bf16 v[176:179], v[28:31], a[40:43], v[176:179]\n"
"ds_read_b64_tr_b16 v[228:229], v6 offset:39072\n"
"v_mfma_f32_16x16x32_bf16 v[176:179], v[32:35], a[48:51], v[176:179]\n"
"ds_read_b64_tr_b16 v[230:231], v6 offset:39584\n"
"s_add_u32 s75, s43, 115200\n"
"v_mfma_f32_16x16x32_bf16 v[176:179], v[36:39], a[56:59], v[176:179]\n"
"ds_read_b64_tr_b16 v[232:233], v6 offset:40128\n"
"s_add_u32 s76, s61, 3072\n"
"v_mfma_f32_16x16x32_bf16 v[180:183], v[24:27], a[36:39], v[180:183]\n"
"ds_read_b64_tr_b16 v[234:235], v6 offset:40640\n"
"s_add_u32 m0, s18, 33792\n"
"v_mfma_f32_16x16x32_bf16 v[180:183], v[28:31], a[44:47], v[180:183]\n"
"buffer_load_dwordx4 v8, s[36:39], s76 offen sc1 lds\n"
"v_mfma_f32_16x16x32_bf16 v[180:183], v[32:35], a[52:55], v[180:183]\n"
"ds_read_b64_tr_b16 v[236:237], v6 offset:41184\n"
"ds_read_b64_tr_b16 v[238:239], v6 offset:41696\n"
"v_mfma_f32_16x16x32_bf16 v[180:183], v[36:39], a[60:63], v[180:183]\n"
"s_waitcnt vmcnt(27) lgkmcnt(8)\n"
"s_barrier\n"
"v_mfma_f32_16x16x32_bf16 v[184:187], v[40:43], a[32:35], v[184:187]\n"
"ds_read_b64_tr_b16 v[24:25], v11 offset:10496\n"
"ds_read_b64_tr_b16 v[26:27], v11 offset:11008\n"
"s_add_u32 m0, s46, 4224\n"
"v_mfma_f32_16x16x32_bf16 v[184:187], v[44:47], a[40:43], v[184:187]\n"
"buffer_load_dwordx4 v1, s[28:31], s75 offen lds\n"
"v_mfma_f32_16x16x32_bf16 v[184:187], v[48:51], a[48:51], v[184:187]\n"
"ds_read_b64_tr_b16 v[28:29], v11 offset:18944\n"
"ds_read_b64_tr_b16 v[30:31], v11 offset:19456\n"
"v_mfma_f32_16x16x32_bf16 v[184:187], v[52:55], a[56:59], v[184:187]\n"
"ds_read_b64_tr_b16 v[32:33], v11 offset:27392\n"
"v_mfma_f32_16x16x32_bf16 v[188:191], v[40:43], a[36:39], v[188:191]\n"
"ds_read_b64_tr_b16 v[34:35], v11 offset:27904\n"
"v_mfma_f32_16x16x32_bf16 v[188:191], v[44:47], a[44:47], v[188:191]\n"
"buffer_load_dwordx4 v2, s[28:31], s75 offen offset:1056 lds\n"
"v_mfma_f32_16x16x32_bf16 v[188:191], v[48:51], a[52:55], v[188:191]\n"
"ds_read_b64_tr_b16 v[36:37], v11 offset:35840\n"
"ds_read_b64_tr_b16 v[38:39], v11 offset:36352\n"
"v_mfma_f32_16x16x32_bf16 v[188:191], v[52:55], a[60:63], v[188:191]\n"
"ds_read_b64_tr_b16 v[40:41], v11 offset:10528\n"
"s_waitcnt lgkmcnt(9)\n"
"v_mfma_f32_16x16x32_bf16 v[120:123], v[224:227], a[32:35], v[120:123]\n"
"s_barrier\n"
"ds_read_b64_tr_b16 v[42:43], v11 offset:11040\n"
"v_mfma_f32_16x16x32_bf16 v[120:123], v[228:231], a[40:43], v[120:123]\n"
"buffer_load_dwordx4 v3, s[28:31], s75 offen offset:2112 lds\n"
"v_mfma_f32_16x16x32_bf16 v[120:123], v[232:235], a[48:51], v[120:123]\n"
"ds_read_b64_tr_b16 v[44:45], v11 offset:18976\n"
"ds_read_b64_tr_b16 v[46:47], v11 offset:19488\n"
"v_mfma_f32_16x16x32_bf16 v[120:123], v[236:239], a[56:59], v[120:123]\n"
"ds_read_b64_tr_b16 v[48:49], v11 offset:27424\n"
"v_mfma_f32_16x16x32_bf16 v[124:127], v[224:227], a[36:39], v[124:127]\n"
"ds_read_b64_tr_b16 v[50:51], v11 offset:27936\n"
"s_add_u32 s35, s43, 147456\n"
"v_mfma_f32_16x16x32_bf16 v[124:127], v[228:231], a[44:47], v[124:127]\n"
"buffer_load_dwordx4 v4, s[28:31], s75 offen offset:3168 lds\n"
"v_mfma_f32_16x16x32_bf16 v[124:127], v[232:235], a[52:55], v[124:127]\n"
"ds_read_b64_tr_b16 v[52:53], v11 offset:35872\n"
"ds_read_b64_tr_b16 v[54:55], v11 offset:36384\n"
"v_mfma_f32_16x16x32_bf16 v[124:127], v[236:239], a[60:63], v[124:127]\n"
"ds_read_b64_tr_b16 v[224:225], v11 offset:10560\n"
"s_waitcnt lgkmcnt(9)\n"
"v_mfma_f32_16x16x32_bf16 v[128:131], v[24:27], a[64:67], v[128:131]\n"
"ds_read_b64_tr_b16 v[226:227], v11 offset:11072\n"
"s_add_u32 m0, s46, 38016\n"
"v_mfma_f32_16x16x32_bf16 v[128:131], v[28:31], a[72:75], v[128:131]\n"
"buffer_load_dwordx4 v1, s[28:31], s35 offen lds\n"
"v_mfma_f32_16x16x32_bf16 v[128:131], v[32:35], a[80:83], v[128:131]\n"
"ds_read_b64_tr_b16 v[228:229], v11 offset:19008\n"
"ds_read_b64_tr_b16 v[230:231], v11 offset:19520\n"
"v_mfma_f32_16x16x32_bf16 v[128:131], v[36:39], a[88:91], v[128:131]\n"
"ds_read_b64_tr_b16 v[232:233], v11 offset:27456\n"
"v_mfma_f32_16x16x32_bf16 v[132:135], v[24:27], a[68:71], v[132:135]\n"
"ds_read_b64_tr_b16 v[234:235], v11 offset:27968\n"
"v_mfma_f32_16x16x32_bf16 v[132:135], v[28:31], a[76:79], v[132:135]\n"
"buffer_load_dwordx4 v[240:243], off, s[36:39], s59 offset:48 sc1\n"
"v_mfma_f32_16x16x32_bf16 v[132:135], v[32:35], a[84:87], v[132:135]\n"
"ds_read_b64_tr_b16 v[236:237], v11 offset:35904\n"
"ds_read_b64_tr_b16 v[238:239], v11 offset:36416\n"
"v_mfma_f32_16x16x32_bf16 v[132:135], v[36:39], a[92:95], v[132:135]\n"
"ds_read_b64_tr_b16 v[24:25], v11 offset:10592\n"
"s_waitcnt lgkmcnt(9)\n"
"v_mfma_f32_16x16x32_bf16 v[136:139], v[40:43], a[64:67], v[136:139]\n"
"ds_read_b64_tr_b16 v[26:27], v11 offset:11104\n"
"v_mfma_f32_16x16x32_bf16 v[136:139], v[44:47], a[72:75], v[136:139]\n"
"buffer_load_dwordx4 v2, s[28:31], s35 offen offset:1056 lds\n"
"v_mfma_f32_16x16x32_bf16 v[136:139], v[48:51], a[80:83], v[136:139]\n"
"ds_read_b64_tr_b16 v[28:29], v11 offset:19040\n"
"ds_read_b64_tr_b16 v[30:31], v11 offset:19552\n"
"v_mfma_f32_16x16x32_bf16 v[136:139], v[52:55], a[88:91], v[136:139]\n"
"ds_read_b64_tr_b16 v[32:33], v11 offset:27488\n"
"v_mfma_f32_16x16x32_bf16 v[140:143], v[40:43], a[68:71], v[140:143]\n"
"ds_read_b64_tr_b16 v[34:35], v11 offset:28000\n"
"v_mfma_f32_16x16x32_bf16 v[140:143], v[44:47], a[76:79], v[140:143]\n"
"buffer_load_dwordx4 v3, s[28:31], s35 offen offset:2112 lds\n"
"v_mfma_f32_16x16x32_bf16 v[140:143], v[48:51], a[84:87], v[140:143]\n"
"ds_read_b64_tr_b16 v[36:37], v11 offset:35936\n"
"ds_read_b64_tr_b16 v[38:39], v11 offset:36448\n"
"v_mfma_f32_16x16x32_bf16 v[140:143], v[52:55], a[92:95], v[140:143]\n"
"ds_read_b64_tr_b16 v[40:41], v11 offset:10624\n"
"s_waitcnt lgkmcnt(9)\n"
"v_mfma_f32_16x16x32_bf16 v[144:147], v[224:227], a[64:67], v[144:147]\n"
"ds_read_b64_tr_b16 v[42:43], v11 offset:11136\n"
"v_mfma_f32_16x16x32_bf16 v[144:147], v[228:231], a[72:75], v[144:147]\n"
"buffer_load_dwordx4 v4, s[28:31], s35 offen offset:3168 lds\n"
"v_mfma_f32_16x16x32_bf16 v[144:147], v[232:235], a[80:83], v[144:147]\n"
"ds_read_b64_tr_b16 v[44:45], v11 offset:19072\n"
"ds_read_b64_tr_b16 v[46:47], v11 offset:19584\n"
"v_mfma_f32_16x16x32_bf16 v[144:147], v[236:239], a[88:91], v[144:147]\n"
"ds_read_b64_tr_b16 v[48:49], v11 offset:27520\n"
"s_add_u32 s76, s61, 4096\n"
"v_mfma_f32_16x16x32_bf16 v[148:151], v[224:227], a[68:71], v[148:151]\n"
"ds_read_b64_tr_b16 v[50:51], v11 offset:28032\n"
"s_add_u32 m0, s18, 71808\n"
"v_mfma_f32_16x16x32_bf16 v[148:151], v[228:231], a[76:79], v[148:151]\n"
"buffer_load_dwordx4 v8, s[36:39], s76 offen sc1 lds\n"
"v_mfma_f32_16x16x32_bf16 v[148:151], v[232:235], a[84:87], v[148:151]\n"
"ds_read_b64_tr_b16 v[52:53], v11 offset:35968\n"
"ds_read_b64_tr_b16 v[54:55], v11 offset:36480\n"
"v_mfma_f32_16x16x32_bf16 v[148:151], v[236:239], a[92:95], v[148:151]\n"
"ds_read_b64_tr_b16 v[224:225], v11 offset:10656\n"
"s_waitcnt lgkmcnt(9)\n"
"v_mfma_f32_16x16x32_bf16 v[152:155], v[24:27], a[64:67], v[152:155]\n"
"ds_read_b64_tr_b16 v[226:227], v11 offset:11168\n"
"v_mfma_f32_16x16x32_bf16 v[152:155], v[28:31], a[72:75], v[152:155]\n"
"ds_read_b64_tr_b16 v[228:229], v11 offset:19104\n"
"v_mfma_f32_16x16x32_bf16 v[152:155], v[32:35], a[80:83], v[152:155]\n"
"ds_read_b64_tr_b16 v[230:231], v11 offset:19616\n"
"v_mfma_f32_16x16x32_bf16 v[152:155], v[36:39], a[88:91], v[152:155]\n"
"ds_read_b64_tr_b16 v[232:233], v11 offset:27552\n"
"s_add_u32 s75, s43, 152064\n"
"v_mfma_f32_16x16x32_bf16 v[156:159], v[24:27], a[68:71], v[156:159]\n"
"ds_read_b64_tr_b16 v[234:235], v11 offset:28064\n"
"s_add_u32 m0, s46, 42240\n"
"v_mfma_f32_16x16x32_bf16 v[156:159], v[28:31], a[76:79], v[156:159]\n"
"buffer_load_dwordx4 v1, s[28:31], s75 offen lds\n"
"v_mfma_f32_16x16x32_bf16 v[156:159], v[32:35], a[84:87], v[156:159]\n"
"ds_read_b64_tr_b16 v[236:237], v11 offset:36000\n"
"ds_read_b64_tr_b16 v[238:239], v11 offset:36512\n"
"v_mfma_f32_16x16x32_bf16 v[156:159], v[36:39], a[92:95], v[156:159]\n"
"ds_read_b64_tr_b16 v[24:25], v11 offset:10688\n"
"s_waitcnt lgkmcnt(9)\n"
"v_mfma_f32_16x16x32_bf16 v[160:163], v[40:43], a[64:67], v[160:163]\n"
"ds_read_b64_tr_b16 v[26:27], v11 offset:11200\n"
"v_mfma_f32_16x16x32_bf16 v[160:163], v[44:47], a[72:75], v[160:163]\n"
"buffer_load_dwordx4 v2, s[28:31], s75 offen offset:1056 lds\n"
"v_mfma_f32_16x16x32_bf16 v[160:163], v[48:51], a[80:83], v[160:163]\n"
"ds_read_b64_tr_b16 v[28:29], v11 offset:19136\n"
"ds_read_b64_tr_b16 v[30:31], v11 offset:19648\n"
"v_mfma_f32_16x16x32_bf16 v[160:163], v[52:55], a[88:91], v[160:163]\n"
"ds_read_b64_tr_b16 v[32:33], v11 offset:27584\n"
"v_mfma_f32_16x16x32_bf16 v[164:167], v[40:43], a[68:71], v[164:167]\n"
"ds_read_b64_tr_b16 v[34:35], v11 offset:28096\n"
"v_mfma_f32_16x16x32_bf16 v[164:167], v[44:47], a[76:79], v[164:167]\n"
"buffer_load_dwordx4 v3, s[28:31], s75 offen offset:2112 lds\n"
"v_mfma_f32_16x16x32_bf16 v[164:167], v[48:51], a[84:87], v[164:167]\n"
"ds_read_b64_tr_b16 v[36:37], v11 offset:36032\n"
"ds_read_b64_tr_b16 v[38:39], v11 offset:36544\n"
"v_mfma_f32_16x16x32_bf16 v[164:167], v[52:55], a[92:95], v[164:167]\n"
"ds_read_b64_tr_b16 v[40:41], v11 offset:10720\n"
"s_waitcnt lgkmcnt(9)\n"
"v_mfma_f32_16x16x32_bf16 v[168:171], v[224:227], a[64:67], v[168:171]\n"
"ds_read_b64_tr_b16 v[42:43], v11 offset:11232\n"
"v_mfma_f32_16x16x32_bf16 v[168:171], v[228:231], a[72:75], v[168:171]\n"
"buffer_load_dwordx4 v4, s[28:31], s75 offen offset:3168 lds\n"
"v_mfma_f32_16x16x32_bf16 v[168:171], v[232:235], a[80:83], v[168:171]\n"
"ds_read_b64_tr_b16 v[44:45], v11 offset:19168\n"
"ds_read_b64_tr_b16 v[46:47], v11 offset:19680\n"
"v_mfma_f32_16x16x32_bf16 v[168:171], v[236:239], a[88:91], v[168:171]\n"
"ds_read_b64_tr_b16 v[48:49], v11 offset:27616\n"
"v_mfma_f32_16x16x32_bf16 v[172:175], v[224:227], a[68:71], v[172:175]\n"
"ds_read_b64_tr_b16 v[50:51], v11 offset:28128\n"
"v_mfma_f32_16x16x32_bf16 v[172:175], v[228:231], a[76:79], v[172:175]\n"
"ds_read_b64_tr_b16 v[52:53], v11 offset:36064\n"
"v_mfma_f32_16x16x32_bf16 v[172:175], v[232:235], a[84:87], v[172:175]\n"
"ds_read_b64_tr_b16 v[54:55], v11 offset:36576\n"
"v_mfma_f32_16x16x32_bf16 v[172:175], v[236:239], a[92:95], v[172:175]\n"
"ds_read_b64_tr_b16 v[224:225], v9 offset:0\n"
"s_waitcnt lgkmcnt(9)\n"
"v_mfma_f32_16x16x32_bf16 v[176:179], v[24:27], a[64:67], v[176:179]\n"
"ds_read_b64_tr_b16 v[226:227], v9 offset:512\n"
"v_mfma_f32_16x16x32_bf16 v[176:179], v[28:31], a[72:75], v[176:179]\n"
"ds_read_b64_tr_b16 v[228:229], v9 offset:1056\n"
"v_mfma_f32_16x16x32_bf16 v[176:179], v[32:35], a[80:83], v[176:179]\n"
"ds_read_b64_tr_b16 v[230:231], v9 offset:1568\n"
"v_mfma_f32_16x16x32_bf16 v[176:179], v[36:39], a[88:91], v[176:179]\n"
"ds_read_b64_tr_b16 v[232:233], v9 offset:2112\n"
"v_mfma_f32_16x16x32_bf16 v[180:183], v[24:27], a[68:71], v[180:183]\n"
"ds_read_b64_tr_b16 v[234:235], v9 offset:2624\n"
"v_mfma_f32_16x16x32_bf16 v[180:183], v[28:31], a[76:79], v[180:183]\n"
"ds_read_b64_tr_b16 v[236:237], v9 offset:3168\n"
"v_mfma_f32_16x16x32_bf16 v[180:183], v[32:35], a[84:87], v[180:183]\n"
"ds_read_b64_tr_b16 v[238:239], v9 offset:3680\n"
"v_mfma_f32_16x16x32_bf16 v[180:183], v[36:39], a[92:95], v[180:183]\n"
"s_waitcnt vmcnt(19) lgkmcnt(8)\n"
"s_barrier\n"
"v_mfma_f32_16x16x32_bf16 v[184:187], v[40:43], a[64:67], v[184:187]\n"
"ds_read_b64_tr_b16 v[24:25], v11 offset:48512\n"
"ds_read_b64_tr_b16 v[26:27], v11 offset:49024\n"
"v_mfma_f32_16x16x32_bf16 v[184:187], v[44:47], a[72:75], v[184:187]\n"
"buffer_store_dword v19, v18, s[36:39], 0 offen offset:64 sc1\n"
"v_mfma_f32_16x16x32_bf16 v[184:187], v[48:51], a[80:83], v[184:187]\n"
"ds_read_b64_tr_b16 v[28:29], v11 offset:56960\n"
"ds_read_b64_tr_b16 v[30:31], v11 offset:57472\n"
"v_mfma_f32_16x16x32_bf16 v[184:187], v[52:55], a[88:91], v[184:187]\n"
"ds_read_b64_tr_b16 v[32:33], v11 offset:65408\n"
"v_mfma_f32_16x16x32_bf16 v[188:191], v[40:43], a[68:71], v[188:191]\n"
"ds_read_b64_tr_b16 v[34:35], v12 offset:384\n"
"v_mfma_f32_16x16x32_bf16 v[188:191], v[44:47], a[76:79], v[188:191]\n"
"ds_read_b64_tr_b16 v[36:37], v12 offset:8320\n"
"s_add_u32 s35, s43, 184320\n"
"v_mfma_f32_16x16x32_bf16 v[188:191], v[48:51], a[84:87], v[188:191]\n"
"ds_read_b64_tr_b16 v[38:39], v12 offset:8832\n"
"s_add_u32 m0, s46, 76032\n"
"v_mfma_f32_16x16x32_bf16 v[188:191], v[52:55], a[92:95], v[188:191]\n"
"ds_read_b64_tr_b16 v[40:41], v11 offset:48544\n"
"s_waitcnt lgkmcnt(9)\n"
"v_mfma_f32_16x16x32_bf16 v[120:123], v[224:227], a[64:67], v[120:123]\n"
"s_barrier\n"
"ds_read_b64_tr_b16 v[42:43], v11 offset:49056\n"
"v_mfma_f32_16x16x32_bf16 v[120:123], v[228:231], a[72:75], v[120:123]\n"
"buffer_load_dwordx4 v1, s[28:31], s35 offen lds\n"
"v_mfma_f32_16x16x32_bf16 v[120:123], v[232:235], a[80:83], v[120:123]\n"
"ds_read_b64_tr_b16 v[44:45], v11 offset:56992\n"
"ds_read_b64_tr_b16 v[46:47], v11 offset:57504\n"
"v_mfma_f32_16x16x32_bf16 v[120:123], v[236:239], a[88:91], v[120:123]\n"
"ds_read_b64_tr_b16 v[48:49], v11 offset:65440\n"
"v_mfma_f32_16x16x32_bf16 v[124:127], v[224:227], a[68:71], v[124:127]\n"
"ds_read_b64_tr_b16 v[50:51], v12 offset:416\n"
"v_mfma_f32_16x16x32_bf16 v[124:127], v[228:231], a[76:79], v[124:127]\n"
"buffer_load_dwordx4 v2, s[28:31], s35 offen offset:1056 lds\n"
"v_mfma_f32_16x16x32_bf16 v[124:127], v[232:235], a[84:87], v[124:127]\n"
"ds_read_b64_tr_b16 v[52:53], v12 offset:8352\n"
"ds_read_b64_tr_b16 v[54:55], v12 offset:8864\n"
"v_mfma_f32_16x16x32_bf16 v[124:127], v[236:239], a[92:95], v[124:127]\n"
"ds_read_b64_tr_b16 v[224:225], v11 offset:48576\n"
"s_waitcnt lgkmcnt(9)\n"
"v_mfma_f32_16x16x32_bf16 v[128:131], v[24:27], a[96:99], v[128:131]\n"
"ds_read_b64_tr_b16 v[226:227], v11 offset:49088\n"
"v_mfma_f32_16x16x32_bf16 v[128:131], v[28:31], a[104:107], v[128:131]\n"
"buffer_load_dwordx4 v3, s[28:31], s35 offen offset:2112 lds\n"
"v_mfma_f32_16x16x32_bf16 v[128:131], v[32:35], a[112:115], v[128:131]\n"
"ds_read_b64_tr_b16 v[228:229], v11 offset:57024\n"
"ds_read_b64_tr_b16 v[230:231], v11 offset:57536\n"
"v_mfma_f32_16x16x32_bf16 v[128:131], v[36:39], a[120:123], v[128:131]\n"
"ds_read_b64_tr_b16 v[232:233], v11 offset:65472\n"
"s_waitcnt vmcnt(12)\n"
"v_xor_b32 v240, s86, v240\n"
"v_mfma_f32_16x16x32_bf16 v[132:135], v[24:27], a[100:103], v[132:135]\n"
"ds_read_b64_tr_b16 v[234:235], v12 offset:448\n"
"s_add_u32 s76, s61, 5120\n"
"v_mfma_f32_16x16x32_bf16 v[132:135], v[28:31], a[108:111], v[132:135]\n"
"buffer_load_dwordx4 v4, s[28:31], s35 offen offset:3168 lds\n"
"v_mfma_f32_16x16x32_bf16 v[132:135], v[32:35], a[116:119], v[132:135]\n"
"ds_read_b64_tr_b16 v[236:237], v12 offset:8384\n"
"ds_read_b64_tr_b16 v[238:239], v12 offset:8896\n"
"v_bitop3_b32 v240, v240, v241, s86 bitop3:0xf6\n"
"v_mfma_f32_16x16x32_bf16 v[132:135], v[36:39], a[124:127], v[132:135]\n"
"ds_read_b64_tr_b16 v[24:25], v11 offset:48608\n"
"v_bitop3_b32 v240, v240, v242, s86 bitop3:0xf6\n"
"s_waitcnt lgkmcnt(9)\n"
"v_mfma_f32_16x16x32_bf16 v[136:139], v[40:43], a[96:99], v[136:139]\n"
"ds_read_b64_tr_b16 v[26:27], v11 offset:49120\n"
"s_add_u32 m0, s18, 109824\n"
"v_mfma_f32_16x16x32_bf16 v[136:139], v[44:47], a[104:107], v[136:139]\n"
"buffer_load_dwordx4 v8, s[36:39], s76 offen sc1 lds\n"
"v_mfma_f32_16x16x32_bf16 v[136:139], v[48:51], a[112:115], v[136:139]\n"
"ds_read_b64_tr_b16 v[28:29], v11 offset:57056\n"
"ds_read_b64_tr_b16 v[30:31], v11 offset:57568\n"
"v_bitop3_b32 v240, v240, v243, s86 bitop3:0xf6\n"
"v_mfma_f32_16x16x32_bf16 v[136:139], v[52:55], a[120:123], v[136:139]\n"
"ds_read_b64_tr_b16 v[32:33], v11 offset:65504\n"
"s_add_u32 s75, s43, 188928\n"
"v_cmp_ne_u32 vcc, 0, v240\n"
"v_mfma_f32_16x16x32_bf16 v[140:143], v[40:43], a[100:103], v[140:143]\n"
"ds_read_b64_tr_b16 v[34:35], v12 offset:480\n"
"s_add_u32 m0, s46, 80256\n"
"v_mfma_f32_16x16x32_bf16 v[140:143], v[44:47], a[108:111], v[140:143]\n"
"buffer_load_dwordx4 v1, s[28:31], s75 offen lds\n"
"v_mfma_f32_16x16x32_bf16 v[140:143], v[48:51], a[116:119], v[140:143]\n"
"ds_read_b64_tr_b16 v[36:37], v12 offset:8416\n"
"ds_read_b64_tr_b16 v[38:39], v12 offset:8928\n"
"s_cbranch_vccnz SLOW3_%=\n"
"BACK3_%=:\n"
"v_mfma_f32_16x16x32_bf16 v[140:143], v[52:55], a[124:127], v[140:143]\n"
"ds_read_b64_tr_b16 v[40:41], v11 offset:48640\n"
"s_waitcnt lgkmcnt(9)\n"
"v_mfma_f32_16x16x32_bf16 v[144:147], v[224:227], a[96:99], v[144:147]\n"
"ds_read_b64_tr_b16 v[42:43], v11 offset:49152\n"
"v_mfma_f32_16x16x32_bf16 v[144:147], v[228:231], a[104:107], v[144:147]\n"
"ds_read_b64_tr_b16 v[44:45], v11 offset:57088\n"
"v_mfma_f32_16x16x32_bf16 v[144:147], v[232:235], a[112:115], v[144:147]\n"
"ds_read_b64_tr_b16 v[46:47], v11 offset:57600\n"
"v_mfma_f32_16x16x32_bf16 v[144:147], v[236:239], a[120:123], v[144:147]\n"
"ds_read_b64_tr_b16 v[48:49], v12 offset:0\n"
"v_mfma_f32_16x16x32_bf16 v[148:151], v[224:227], a[100:103], v[148:151]\n"
"ds_read_b64_tr_b16 v[50:51], v12 offset:512\n"
"s_add_u32 s58, s57, 32768\n"
"v_mfma_f32_16x16x32_bf16 v[148:151], v[228:231], a[108:111], v[148:151]\n"
"buffer_load_dwordx4 a[160:163], v13, s[36:39], s58 offen sc1\n"
"v_mfma_f32_16x16x32_bf16 v[148:151], v[232:235], a[116:119], v[148:151]\n"
"ds_read_b64_tr_b16 v[52:53], v12 offset:8448\n"
"ds_read_b64_tr_b16 v[54:55], v12 offset:8960\n"
"v_mfma_f32_16x16x32_bf16 v[148:151], v[236:239], a[124:127], v[148:151]\n"
"ds_read_b64_tr_b16 v[224:225], v11 offset:48672\n"
"s_waitcnt lgkmcnt(9)\n"
"v_mfma_f32_16x16x32_bf16 v[152:155], v[24:27], a[96:99], v[152:155]\n"
"ds_read_b64_tr_b16 v[226:227], v11 offset:49184\n"
"v_mfma_f32_16x16x32_bf16 v[152:155], v[28:31], a[104:107], v[152:155]\n"
"buffer_load_dwordx4 a[164:167], v13, s[36:39], s58 offen offset:1024 sc1\n"
"v_mfma_f32_16x16x32_bf16 v[152:155], v[32:35], a[112:115], v[152:155]\n"
"ds_read_b64_tr_b16 v[228:229], v11 offset:57120\n"
"ds_read_b64_tr_b16 v[230:231], v11 offset:57632\n"
"v_mfma_f32_16x16x32_bf16 v[152:155], v[36:39], a[120:123], v[152:155]\n"
"ds_read_b64_tr_b16 v[232:233], v12 offset:32\n"
"v_mfma_f32_16x16x32_bf16 v[156:159], v[24:27], a[100:103], v[156:159]\n"
"ds_read_b64_tr_b16 v[234:235], v12 offset:544\n"
"v_mfma_f32_16x16x32_bf16 v[156:159], v[28:31], a[108:111], v[156:159]\n"
"buffer_load_dwordx4 a[168:171], v14, s[36:39], s58 offen sc1\n"
"v_mfma_f32_16x16x32_bf16 v[156:159], v[32:35], a[116:119], v[156:159]\n"
"ds_read_b64_tr_b16 v[236:237], v12 offset:8480\n"
"ds_read_b64_tr_b16 v[238:239], v12 offset:8992\n"
"v_mfma_f32_16x16x32_bf16 v[156:159], v[36:39], a[124:127], v[156:159]\n"
"ds_read_b64_tr_b16 v[24:25], v11 offset:48704\n"
"s_waitcnt lgkmcnt(9)\n"
"v_mfma_f32_16x16x32_bf16 v[160:163], v[40:43], a[96:99], v[160:163]\n"
"ds_read_b64_tr_b16 v[26:27], v11 offset:49216\n"
"v_mfma_f32_16x16x32_bf16 v[160:163], v[44:47], a[104:107], v[160:163]\n"
"buffer_load_dwordx4 a[172:175], v14, s[36:39], s58 offen offset:1024 sc1\n"
"v_mfma_f32_16x16x32_bf16 v[160:163], v[48:51], a[112:115], v[160:163]\n"
"ds_read_b64_tr_b16 v[28:29], v11 offset:57152\n"
"ds_read_b64_tr_b16 v[30:31], v11 offset:57664\n"
"v_mfma_f32_16x16x32_bf16 v[160:163], v[52:55], a[120:123], v[160:163]\n"
"ds_read_b64_tr_b16 v[32:33], v12 offset:64\n"
"v_mfma_f32_16x16x32_bf16 v[164:167], v[40:43], a[100:103], v[164:167]\n"
"ds_read_b64_tr_b16 v[34:35], v12 offset:576\n"
"v_mfma_f32_16x16x32_bf16 v[164:167], v[44:47], a[108:111], v[164:167]\n"
"buffer_load_dwordx4 a[176:179], v15, s[36:39], s58 offen sc1\n"
"v_mfma_f32_16x16x32_bf16 v[164:167], v[48:51], a[116:119], v[164:167]\n"
"ds_read_b64_tr_b16 v[36:37], v12 offset:8512\n"
"ds_read_b64_tr_b16 v[38:39], v12 offset:9024\n"
"v_mfma_f32_16x16x32_bf16 v[164:167], v[52:55], a[124:127], v[164:167]\n"
"ds_read_b64_tr_b16 v[40:41], v11 offset:48736\n"
"s_waitcnt lgkmcnt(9)\n"
"v_mfma_f32_16x16x32_bf16 v[168:171], v[224:227], a[96:99], v[168:171]\n"
"ds_read_b64_tr_b16 v[42:43], v11 offset:49248\n"
"v_mfma_f32_16x16x32_bf16 v[168:171], v[228:231], a[104:107], v[168:171]\n"
"buffer_load_dwordx4 a[180:183], v15, s[36:39], s58 offen offset:1024 sc1\n"
"v_mfma_f32_16x16x32_bf16 v[168:171], v[232:235], a[112:115], v[168:171]\n"
"ds_read_b64_tr_b16 v[44:45], v11 offset:57184\n"
"ds_read_b64_tr_b16 v[46:47], v11 offset:57696\n"
"v_mfma_f32_16x16x32_bf16 v[168:171], v[236:239], a[120:123], v[168:171]\n"
"ds_read_b64_tr_b16 v[48:49], v12 offset:96\n"
"v_mfma_f32_16x16x32_bf16 v[172:175], v[224:227], a[100:103], v[172:175]\n"
"ds_read_b64_tr_b16 v[50:51], v12 offset:608\n"
"v_mfma_f32_16x16x32_bf16 v[172:175], v[228:231], a[108:111], v[172:175]\n"
"buffer_load_dwordx4 a[184:187], v16, s[36:39], s58 offen sc1\n"
"v_mfma_f32_16x16x32_bf16 v[172:175], v[232:235], a[116:119], v[172:175]\n"
"ds_read_b64_tr_b16 v[52:53], v12 offset:8544\n"
"ds_read_b64_tr_b16 v[54:55], v12 offset:9056\n"
"v_mfma_f32_16x16x32_bf16 v[172:175], v[236:239], a[124:127], v[172:175]\n"
"ds_read_b64_tr_b16 v[224:225], v9 offset:38016\n"
"s_waitcnt lgkmcnt(9)\n"
"v_mfma_f32_16x16x32_bf16 v[176:179], v[24:27], a[96:99], v[176:179]\n"
"ds_read_b64_tr_b16 v[226:227], v9 offset:38528\n"
"v_mfma_f32_16x16x32_bf16 v[176:179], v[28:31], a[104:107], v[176:179]\n"
"buffer_load_dwordx4 a[188:191], v16, s[36:39], s58 offen offset:1024 sc1\n"
"v_mfma_f32_16x16x32_bf16 v[176:179], v[32:35], a[112:115], v[176:179]\n"
"ds_read_b64_tr_b16 v[228:229], v9 offset:39072\n"
"ds_read_b64_tr_b16 v[230:231], v9 offset:39584\n"
"v_mfma_f32_16x16x32_bf16 v[176:179], v[36:39], a[120:123], v[176:179]\n"
"ds_read_b64_tr_b16 v[232:233], v9 offset:40128\n"
"v_mfma_f32_16x16x32_bf16 v[180:183], v[24:27], a[100:103], v[180:183]\n"
"ds_read_b64_tr_b16 v[234:235], v9 offset:40640\n"
"s_add_u32 s58, s57, 40960\n"
"v_mfma_f32_16x16x32_bf16 v[180:183], v[28:31], a[108:111], v[180:183]\n"
"buffer_load_dwordx4 a[192:195], v13, s[36:39], s58 offen sc1\n"
"v_mfma_f32_16x16x32_bf16 v[180:183], v[32:35], a[116:119], v[180:183]\n"
"ds_read_b64_tr_b16 v[236:237], v9 offset:41184\n"
"ds_read_b64_tr_b16 v[238:239], v9 offset:41696\n"
"v_mfma_f32_16x16x32_bf16 v[180:183], v[36:39], a[124:127], v[180:183]\n"
"s_waitcnt lgkmcnt(8)\n"
"s_barrier\n"
"v_mfma_f32_16x16x32_bf16 v[184:187], v[40:43], a[96:99], v[184:187]\n"
"ds_read_b64_tr_b16 v[24:25], v10 offset:0\n"
"ds_read_b64_tr_b16 v[26:27], v10 offset:512\n"
"v_mfma_f32_16x16x32_bf16 v[184:187], v[44:47], a[104:107], v[184:187]\n"
"buffer_load_dwordx4 a[196:199], v13, s[36:39], s58 offen offset:1024 sc1\n"
"v_mfma_f32_16x16x32_bf16 v[184:187], v[48:51], a[112:115], v[184:187]\n"
"ds_read_b64_tr_b16 v[28:29], v10 offset:8448\n"
"ds_read_b64_tr_b16 v[30:31], v10 offset:8960\n"
"v_mfma_f32_16x16x32_bf16 v[184:187], v[52:55], a[120:123], v[184:187]\n"
"ds_read_b64_tr_b16 v[32:33], v10 offset:16896\n"
"v_mfma_f32_16x16x32_bf16 v[188:191], v[40:43], a[100:103], v[188:191]\n"
"ds_read_b64_tr_b16 v[34:35], v10 offset:17408\n"
"v_mfma_f32_16x16x32_bf16 v[188:191], v[44:47], a[108:111], v[188:191]\n"
"ds_read_b64_tr_b16 v[36:37], v10 offset:25344\n"
"v_mfma_f32_16x16x32_bf16 v[188:191], v[48:51], a[116:119], v[188:191]\n"
"ds_read_b64_tr_b16 v[38:39], v10 offset:25856\n"
"v_mfma_f32_16x16x32_bf16 v[188:191], v[52:55], a[124:127], v[188:191]\n"
"ds_read_b64_tr_b16 v[40:41], v10 offset:32\n"
"s_waitcnt lgkmcnt(9)\n"
"v_mfma_f32_16x16x32_bf16 v[120:123], v[224:227], a[96:99], v[120:123]\n"
"s_barrier\n"
"ds_read_b64_tr_b16 v[42:43], v10 offset:544\n"
"v_mfma_f32_16x16x32_bf16 v[120:123], v[228:231], a[104:107], v[120:123]\n"
"buffer_load_dwordx4 a[200:203], v14, s[36:39], s58 offen sc1\n"
"v_mfma_f32_16x16x32_bf16 v[120:123], v[232:235], a[112:115], v[120:123]\n"
"ds_read_b64_tr_b16 v[44:45], v10 offset:8480\n"
"ds_read_b64_tr_b16 v[46:47], v10 offset:8992\n"
"v_mfma_f32_16x16x32_bf16 v[120:123], v[236:239], a[120:123], v[120:123]\n"
"ds_read_b64_tr_b16 v[48:49], v10 offset:16928\n"
"v_mfma_f32_16x16x32_bf16 v[124:127], v[224:227], a[100:103], v[124:127]\n"
"ds_read_b64_tr_b16 v[50:51], v10 offset:17440\n"
"v_mfma_f32_16x16x32_bf16 v[124:127], v[228:231], a[108:111], v[124:127]\n"
"buffer_load_dwordx4 a[204:207], v14, s[36:39], s58 offen offset:1024 sc1\n"
"v_mfma_f32_16x16x32_bf16 v[124:127], v[232:235], a[116:119], v[124:127]\n"
"ds_read_b64_tr_b16 v[52:53], v10 offset:25376\n"
"ds_read_b64_tr_b16 v[54:55], v10 offset:25888\n"
"v_mfma_f32_16x16x32_bf16 v[124:127], v[236:239], a[124:127], v[124:127]\n"
"ds_read_b64_tr_b16 v[224:225], v10 offset:64\n"
"s_waitcnt lgkmcnt(9)\n"
"v_mfma_f32_16x16x32_bf16 v[128:131], v[24:27], a[128:131], v[128:131]\n"
"ds_read_b64_tr_b16 v[226:227], v10 offset:576\n"
"v_mfma_f32_16x16x32_bf16 v[128:131], v[28:31], a[136:139], v[128:131]\n"
"buffer_load_dwordx4 a[208:211], v15, s[36:39], s58 offen sc1\n"
"v_mfma_f32_16x16x32_bf16 v[128:131], v[32:35], a[144:147], v[128:131]\n"
"ds_read_b64_tr_b16 v[228:229], v10 offset:8512\n"
"ds_read_b64_tr_b16 v[230:231], v10 offset:9024\n"
"v_mfma_f32_16x16x32_bf16 v[128:131], v[36:39], a[152:155], v[128:131]\n"
"ds_read_b64_tr_b16 v[232:233], v10 offset:16960\n"
"v_mfma_f32_16x16x32_bf16 v[132:135], v[24:27], a[132:135], v[132:135]\n"
"ds_read_b64_tr_b16 v[234:235], v10 offset:17472\n"
"v_mfma_f32_16x16x32_bf16 v[132:135], v[28:31], a[140:143], v[132:135]\n"
"buffer_load_dwordx4 a[212:215], v15, s[36:39], s58 offen offset:1024 sc1\n"
"v_mfma_f32_16x16x32_bf16 v[132:135], v[32:35], a[148:151], v[132:135]\n"
"ds_read_b64_tr_b16 v[236:237], v10 offset:25408\n"
"ds_read_b64_tr_b16 v[238:239], v10 offset:25920\n"
"v_mfma_f32_16x16x32_bf16 v[132:135], v[36:39], a[156:159], v[132:135]\n"
"ds_read_b64_tr_b16 v[24:25], v10 offset:96\n"
"s_waitcnt lgkmcnt(9)\n"
"v_mfma_f32_16x16x32_bf16 v[136:139], v[40:43], a[128:131], v[136:139]\n"
"ds_read_b64_tr_b16 v[26:27], v10 offset:608\n"
"v_mfma_f32_16x16x32_bf16 v[136:139], v[44:47], a[136:139], v[136:139]\n"
"buffer_load_dwordx4 a[216:219], v16, s[36:39], s58 offen sc1\n"
"v_mfma_f32_16x16x32_bf16 v[136:139], v[48:51], a[144:147], v[136:139]\n"
"ds_read_b64_tr_b16 v[28:29], v10 offset:8544\n"
"ds_read_b64_tr_b16 v[30:31], v10 offset:9056\n"
"v_mfma_f32_16x16x32_bf16 v[136:139], v[52:55], a[152:155], v[136:139]\n"
"ds_read_b64_tr_b16 v[32:33], v10 offset:16992\n"
"v_mfma_f32_16x16x32_bf16 v[140:143], v[40:43], a[132:135], v[140:143]\n"
"ds_read_b64_tr_b16 v[34:35], v10 offset:17504\n"
"v_mfma_f32_16x16x32_bf16 v[140:143], v[44:47], a[140:143], v[140:143]\n"
"buffer_load_dwordx4 v[240:243], off, s[36:39], s59 offset:64 sc1\n"
"v_mfma_f32_16x16x32_bf16 v[140:143], v[48:51], a[148:151], v[140:143]\n"
"ds_read_b64_tr_b16 v[36:37], v10 offset:25440\n"
"ds_read_b64_tr_b16 v[38:39], v10 offset:25952\n"
"v_mfma_f32_16x16x32_bf16 v[140:143], v[52:55], a[156:159], v[140:143]\n"
"ds_read_b64_tr_b16 v[40:41], v10 offset:128\n"
"s_waitcnt lgkmcnt(9)\n"
"v_mfma_f32_16x16x32_bf16 v[144:147], v[224:227], a[128:131], v[144:147]\n"
"ds_read_b64_tr_b16 v[42:43], v10 offset:640\n"
"v_mfma_f32_16x16x32_bf16 v[144:147], v[228:231], a[136:139], v[144:147]\n"
"buffer_load_dwordx4 a[220:223], v16, s[36:39], s58 offen offset:1024 sc1\n"
"v_mfma_f32_16x16x32_bf16 v[144:147], v[232:235], a[144:147], v[144:147]\n"
"ds_read_b64_tr_b16 v[44:45], v10 offset:8576\n"
"ds_read_b64_tr_b16 v[46:47], v10 offset:9088\n"
"v_mfma_f32_16x16x32_bf16 v[144:147], v[236:239], a[152:155], v[144:147]\n"
"ds_read_b64_tr_b16 v[48:49], v10 offset:17024\n"
"v_mfma_f32_16x16x32_bf16 v[148:151], v[224:227], a[132:135], v[148:151]\n"
"ds_read_b64_tr_b16 v[50:51], v10 offset:17536\n"
"v_mfma_f32_16x16x32_bf16 v[148:151], v[228:231], a[140:143], v[148:151]\n"
"buffer_load_dwordx4 v2, s[28:31], s75 offen offset:1056 lds\n"
"v_mfma_f32_16x16x32_bf16 v[148:151], v[232:235], a[148:151], v[148:151]\n"
"ds_read_b64_tr_b16 v[52:53], v10 offset:25472\n"
"ds_read_b64_tr_b16 v[54:55], v10 offset:25984\n"
"v_mfma_f32_16x16x32_bf16 v[148:151], v[236:239], a[156:159], v[148:151]\n"
"ds_read_b64_tr_b16 v[224:225], v10 offset:160\n"
"s_waitcnt lgkmcnt(9)\n"
"v_mfma_f32_16x16x32_bf16 v[152:155], v[24:27], a[128:131], v[152:155]\n"
"ds_read_b64_tr_b16 v[226:227], v10 offset:672\n"
"v_mfma_f32_16x16x32_bf16 v[152:155], v[28:31], a[136:139], v[152:155]\n"
"buffer_load_dwordx4 v3, s[28:31], s75 offen offset:2112 lds\n"
"v_mfma_f32_16x16x32_bf16 v[152:155], v[32:35], a[144:147], v[152:155]\n"
"ds_read_b64_tr_b16 v[228:229], v10 offset:8608\n"
"ds_read_b64_tr_b16 v[230:231], v10 offset:9120\n"
"v_mfma_f32_16x16x32_bf16 v[152:155], v[36:39], a[152:155], v[152:155]\n"
"ds_read_b64_tr_b16 v[232:233], v10 offset:17056\n"
"v_mfma_f32_16x16x32_bf16 v[156:159], v[24:27], a[132:135], v[156:159]\n"
"ds_read_b64_tr_b16 v[234:235], v10 offset:17568\n"
"s_add_u32 s35, s43, 221184\n"
"v_mfma_f32_16x16x32_bf16 v[156:159], v[28:31], a[140:143], v[156:159]\n"
"buffer_load_dwordx4 v4, s[28:31], s75 offen offset:3168 lds\n"
"v_mfma_f32_16x16x32_bf16 v[156:159], v[32:35], a[148:151], v[156:159]\n"
"ds_read_b64_tr_b16 v[236:237], v10 offset:25504\n"
"ds_read_b64_tr_b16 v[238:239], v10 offset:26016\n"
"v_mfma_f32_16x16x32_bf16 v[156:159], v[36:39], a[156:159], v[156:159]\n"
"ds_read_b64_tr_b16 v[24:25], v10 offset:192\n"
"s_waitcnt lgkmcnt(9)\n"
"v_mfma_f32_16x16x32_bf16 v[160:163], v[40:43], a[128:131], v[160:163]\n"
"ds_read_b64_tr_b16 v[26:27], v10 offset:704\n"
"s_add_u32 m0, s46, 114048\n"
"v_mfma_f32_16x16x32_bf16 v[160:163], v[44:47], a[136:139], v[160:163]\n"
"buffer_load_dwordx4 v1, s[28:31], s35 offen lds\n"
"v_mfma_f32_16x16x32_bf16 v[160:163], v[48:51], a[144:147], v[160:163]\n"
"ds_read_b64_tr_b16 v[28:29], v10 offset:8640\n"
"ds_read_b64_tr_b16 v[30:31], v10 offset:9152\n"
"v_mfma_f32_16x16x32_bf16 v[160:163], v[52:55], a[152:155], v[160:163]\n"
"ds_read_b64_tr_b16 v[32:33], v10 offset:17088\n"
"v_mfma_f32_16x16x32_bf16 v[164:167], v[40:43], a[132:135], v[164:167]\n"
"ds_read_b64_tr_b16 v[34:35], v10 offset:17600\n"
"v_mfma_f32_16x16x32_bf16 v[164:167], v[44:47], a[140:143], v[164:167]\n"
"ds_read_b64_tr_b16 v[36:37], v10 offset:25536\n"
"v_mfma_f32_16x16x32_bf16 v[164:167], v[48:51], a[148:151], v[164:167]\n"
"ds_read_b64_tr_b16 v[38:39], v10 offset:26048\n"
"v_mfma_f32_16x16x32_bf16 v[164:167], v[52:55], a[156:159], v[164:167]\n"
"ds_read_b64_tr_b16 v[40:41], v10 offset:224\n"
"s_waitcnt lgkmcnt(9)\n"
"v_mfma_f32_16x16x32_bf16 v[168:171], v[224:227], a[128:131], v[168:171]\n"
"ds_read_b64_tr_b16 v[42:43], v10 offset:736\n"
"v_mfma_f32_16x16x32_bf16 v[168:171], v[228:231], a[136:139], v[168:171]\n"
"buffer_load_dwordx4 v2, s[28:31], s35 offen offset:1056 lds\n"
"v_mfma_f32_16x16x32_bf16 v[168:171], v[232:235], a[144:147], v[168:171]\n"
"ds_read_b64_tr_b16 v[44:45], v10 offset:8672\n"
"ds_read_b64_tr_b16 v[46:47], v10 offset:9184\n"
"v_mfma_f32_16x16x32_bf16 v[168:171], v[236:239], a[152:155], v[168:171]\n"
"ds_read_b64_tr_b16 v[48:49], v10 offset:17120\n"
"v_mfma_f32_16x16x32_bf16 v[172:175], v[224:227], a[132:135], v[172:175]\n"
"ds_read_b64_tr_b16 v[50:51], v10 offset:17632\n"
"v_mfma_f32_16x16x32_bf16 v[172:175], v[228:231], a[140:143], v[172:175]\n"
"buffer_load_dwordx4 v3, s[28:31], s35 offen offset:2112 lds\n"
"v_mfma_f32_16x16x32_bf16 v[172:175], v[232:235], a[148:151], v[172:175]\n"
"ds_read_b64_tr_b16 v[52:53], v10 offset:25568\n"
"ds_read_b64_tr_b16 v[54:55], v10 offset:26080\n"
"v_mfma_f32_16x16x32_bf16 v[172:175], v[236:239], a[156:159], v[172:175]\n"
"ds_read_b64_tr_b16 v[224:225], v6 offset:0\n"
"s_waitcnt lgkmcnt(9)\n"
"v_mfma_f32_16x16x32_bf16 v[176:179], v[24:27], a[128:131], v[176:179]\n"
"ds_read_b64_tr_b16 v[226:227], v6 offset:512\n"
"s_add_u32 s75, s43, 225792\n"
"v_mfma_f32_16x16x32_bf16 v[176:179], v[28:31], a[136:139], v[176:179]\n"
"buffer_load_dwordx4 v4, s[28:31], s35 offen offset:3168 lds\n"
"v_mfma_f32_16x16x32_bf16 v[176:179], v[32:35], a[144:147], v[176:179]\n"
"ds_read_b64_tr_b16 v[228:229], v6 offset:1056\n"
"ds_read_b64_tr_b16 v[230:231], v6 offset:1568\n"
"v_mfma_f32_16x16x32_bf16 v[176:179], v[36:39], a[152:155], v[176:179]\n"
"ds_read_b64_tr_b16 v[232:233], v6 offset:2112\n"
"s_add_u32 s76, s61, 6144\n"
"v_mfma_f32_16x16x32_bf16 v[180:183], v[24:27], a[132:135], v[180:183]\n"
"ds_read_b64_tr_b16 v[234:235], v6 offset:2624\n"
"s_add_u32 m0, s18, 147840\n"
"v_mfma_f32_16x16x32_bf16 v[180:183], v[28:31], a[140:143], v[180:183]\n"
"buffer_load_dwordx4 v8, s[36:39], s76 offen sc1 lds\n"
"v_mfma_f32_16x16x32_bf16 v[180:183], v[32:35], a[148:151], v[180:183]\n"
"ds_read_b64_tr_b16 v[236:237], v6 offset:3168\n"
"ds_read_b64_tr_b16 v[238:239], v6 offset:3680\n"
"v_mfma_f32_16x16x32_bf16 v[180:183], v[36:39], a[156:159], v[180:183]\n"
"s_waitcnt vmcnt(17) lgkmcnt(8)\n"
"s_barrier\n"
"v_mfma_f32_16x16x32_bf16 v[184:187], v[40:43], a[128:131], v[184:187]\n"
"ds_read_b64_tr_b16 v[24:25], v10 offset:38016\n"
"ds_read_b64_tr_b16 v[26:27], v10 offset:38528\n"
"s_add_u32 m0, s46, 118272\n"
"v_mfma_f32_16x16x32_bf16 v[184:187], v[44:47], a[136:139], v[184:187]\n"
"buffer_load_dwordx4 v1, s[28:31], s75 offen lds\n"
"v_mfma_f32_16x16x32_bf16 v[184:187], v[48:51], a[144:147], v[184:187]\n"
"ds_read_b64_tr_b16 v[28:29], v10 offset:46464\n"
"ds_read_b64_tr_b16 v[30:31], v10 offset:46976\n"
"v_mfma_f32_16x16x32_bf16 v[184:187], v[52:55], a[152:155], v[184:187]\n"
"ds_read_b64_tr_b16 v[32:33], v10 offset:54912\n"
"v_mfma_f32_16x16x32_bf16 v[188:191], v[40:43], a[132:135], v[188:191]\n"
"ds_read_b64_tr_b16 v[34:35], v10 offset:55424\n"
"v_mfma_f32_16x16x32_bf16 v[188:191], v[44:47], a[140:143], v[188:191]\n"
"buffer_load_dwordx4 v2, s[28:31], s75 offen offset:1056 lds\n"
"v_mfma_f32_16x16x32_bf16 v[188:191], v[48:51], a[148:151], v[188:191]\n"
"ds_read_b64_tr_b16 v[36:37], v10 offset:63360\n"
"ds_read_b64_tr_b16 v[38:39], v10 offset:63872\n"
"v_mfma_f32_16x16x32_bf16 v[188:191], v[52:55], a[156:159], v[188:191]\n"
"ds_read_b64_tr_b16 v[40:41], v10 offset:38048\n"
"s_waitcnt lgkmcnt(9)\n"
"v_mfma_f32_16x16x32_bf16 v[120:123], v[224:227], a[128:131], v[120:123]\n"
"s_barrier\n"
"ds_read_b64_tr_b16 v[42:43], v10 offset:38560\n"
"v_mfma_f32_16x16x32_bf16 v[120:123], v[228:231], a[136:139], v[120:123]\n"
"buffer_load_dwordx4 v3, s[28:31], s75 offen offset:2112 lds\n"
"v_mfma_f32_16x16x32_bf16 v[120:123], v[232:235], a[144:147], v[120:123]\n"
"ds_read_b64_tr_b16 v[44:45], v10 offset:46496\n"
"ds_read_b64_tr_b16 v[46:47], v10 offset:47008\n"
"v_mfma_f32_16x16x32_bf16 v[120:123], v[236:239], a[152:155], v[120:123]\n"
"ds_read_b64_tr_b16 v[48:49], v10 offset:54944\n"
"v_mfma_f32_16x16x32_bf16 v[124:127], v[224:227], a[132:135], v[124:127]\n"
"ds_read_b64_tr_b16 v[50:51], v10 offset:55456\n"
"s_add_u32 s35, s43, 258048\n"
"v_mfma_f32_16x16x32_bf16 v[124:127], v[228:231], a[140:143], v[124:127]\n"
"buffer_load_dwordx4 v4, s[28:31], s75 offen offset:3168 lds\n"
"v_mfma_f32_16x16x32_bf16 v[124:127], v[232:235], a[148:151], v[124:127]\n"
"ds_read_b64_tr_b16 v[52:53], v10 offset:63392\n"
"ds_read_b64_tr_b16 v[54:55], v10 offset:63904\n"
"v_mfma_f32_16x16x32_bf16 v[124:127], v[236:239], a[156:159], v[124:127]\n"
"ds_read_b64_tr_b16 v[224:225], v10 offset:38080\n"
"s_waitcnt lgkmcnt(9)\n"
"v_mfma_f32_16x16x32_bf16 v[128:131], v[24:27], a[160:163], v[128:131]\n"
"ds_read_b64_tr_b16 v[226:227], v10 offset:38592\n"
"s_add_u32 m0, s46, 0\n"
"v_mfma_f32_16x16x32_bf16 v[128:131], v[28:31], a[168:171], v[128:131]\n"
"buffer_load_dwordx4 v1, s[28:31], s35 offen lds\n"
"v_mfma_f32_16x16x32_bf16 v[128:131], v[32:35], a[176:179], v[128:131]\n"
"ds_read_b64_tr_b16 v[228:229], v10 offset:46528\n"
"ds_read_b64_tr_b16 v[230:231], v10 offset:47040\n"
"v_mfma_f32_16x16x32_bf16 v[128:131], v[36:39], a[184:187], v[128:131]\n"
"ds_read_b64_tr_b16 v[232:233], v10 offset:54976\n"
"v_mfma_f32_16x16x32_bf16 v[132:135], v[24:27], a[164:167], v[132:135]\n"
"ds_read_b64_tr_b16 v[234:235], v10 offset:55488\n"
"v_mfma_f32_16x16x32_bf16 v[132:135], v[28:31], a[172:175], v[132:135]\n"
"buffer_load_dwordx4 v2, s[28:31], s35 offen offset:1056 lds\n"
"v_mfma_f32_16x16x32_bf16 v[132:135], v[32:35], a[180:183], v[132:135]\n"
"ds_read_b64_tr_b16 v[236:237], v10 offset:63424\n"
"ds_read_b64_tr_b16 v[238:239], v10 offset:63936\n"
"v_mfma_f32_16x16x32_bf16 v[132:135], v[36:39], a[188:191], v[132:135]\n"
"ds_read_b64_tr_b16 v[24:25], v10 offset:38112\n"
"s_waitcnt lgkmcnt(9)\n"
"v_mfma_f32_16x16x32_bf16 v[136:139], v[40:43], a[160:163], v[136:139]\n"
"ds_read_b64_tr_b16 v[26:27], v10 offset:38624\n"
"v_mfma_f32_16x16x32_bf16 v[136:139], v[44:47], a[168:171], v[136:139]\n"
"ds_read_b64_tr_b16 v[28:29], v10 offset:46560\n"
"v_mfma_f32_16x16x32_bf16 v[136:139], v[48:51], a[176:179], v[136:139]\n"
"ds_read_b64_tr_b16 v[30:31], v10 offset:47072\n"
"v_mfma_f32_16x16x32_bf16 v[136:139], v[52:55], a[184:187], v[136:139]\n"
"ds_read_b64_tr_b16 v[32:33], v10 offset:55008\n"
"s_waitcnt vmcnt(15)\n"
"v_xor_b32 v240, s86, v240\n"
"v_mfma_f32_16x16x32_bf16 v[140:143], v[40:43], a[164:167], v[140:143]\n"
"ds_read_b64_tr_b16 v[34:35], v10 offset:55520\n"
"v_mfma_f32_16x16x32_bf16 v[140:143], v[44:47], a[172:175], v[140:143]\n"
"buffer_load_dwordx4 v3, s[28:31], s35 offen offset:2112 lds\n"
"v_mfma_f32_16x16x32_bf16 v[140:143], v[48:51], a[180:183], v[140:143]\n"
"ds_read_b64_tr_b16 v[36:37], v10 offset:63456\n"
"ds_read_b64_tr_b16 v[38:39], v10 offset:63968\n"
"v_bitop3_b32 v240, v240, v241, s86 bitop3:0xf6\n"
"v_mfma_f32_16x16x32_bf16 v[140:143], v[52:55], a[188:191], v[140:143]\n"
"ds_read_b64_tr_b16 v[40:41], v10 offset:38144\n"
"v_bitop3_b32 v240, v240, v242, s86 bitop3:0xf6\n"
"s_waitcnt lgkmcnt(9)\n"
"v_mfma_f32_16x16x32_bf16 v[144:147], v[224:227], a[160:163], v[144:147]\n"
"ds_read_b64_tr_b16 v[42:43], v10 offset:38656\n"
"v_mfma_f32_16x16x32_bf16 v[144:147], v[228:231], a[168:171], v[144:147]\n"
"buffer_load_dwordx4 v4, s[28:31], s35 offen offset:3168 lds\n"
"v_mfma_f32_16x16x32_bf16 v[144:147], v[232:235], a[176:179], v[144:147]\n"
"ds_read_b64_tr_b16 v[44:45], v10 offset:46592\n"
"ds_read_b64_tr_b16 v[46:47], v10 offset:47104\n"
"v_bitop3_b32 v240, v240, v243, s86 bitop3:0xf6\n"
"v_mfma_f32_16x16x32_bf16 v[144:147], v[236:239], a[184:187], v[144:147]\n"
"ds_read_b64_tr_b16 v[48:49], v10 offset:55040\n"
"s_add_u32 s76, s61, 7168\n"
"v_cmp_ne_u32 vcc, 0, v240\n"
"v_mfma_f32_16x16x32_bf16 v[148:151], v[224:227], a[164:167], v[148:151]\n"
"ds_read_b64_tr_b16 v[50:51], v10 offset:55552\n"
"s_add_u32 m0, s18, 33792\n"
"v_mfma_f32_16x16x32_bf16 v[148:151], v[228:231], a[172:175], v[148:151]\n"
"buffer_load_dwordx4 v8, s[36:39], s76 offen sc1 lds\n"
"v_mfma_f32_16x16x32_bf16 v[148:151], v[232:235], a[180:183], v[148:151]\n"
"ds_read_b64_tr_b16 v[52:53], v10 offset:63488\n"
"ds_read_b64_tr_b16 v[54:55], v10 offset:64000\n"
"s_cbranch_vccnz SLOW4_%=\n"
"BACK4_%=:\n"
"v_mfma_f32_16x16x32_bf16 v[148:151], v[236:239], a[188:191], v[148:151]\n"
"ds_read_b64_tr_b16 v[224:225], v10 offset:38176\n"
"s_waitcnt lgkmcnt(9)\n"
"v_mfma_f32_16x16x32_bf16 v[152:155], v[24:27], a[160:163], v[152:155]\n"
"ds_read_b64_tr_b16 v[226:227], v10 offset:38688\n"
"s_add_u32 s58, s57, 49152\n"
"v_mfma_f32_16x16x32_bf16 v[152:155], v[28:31], a[168:171], v[152:155]\n"
"buffer_load_dwordx4 a[224:227], v13, s[36:39], s58 offen sc1\n"
"v_mfma_f32_16x16x32_bf16 v[152:155], v[32:35], a[176:179], v[152:155]\n"
"ds_read_b64_tr_b16 v[228:229], v10 offset:46624\n"
"ds_read_b64_tr_b16 v[230:231], v10 offset:47136\n"
"v_mfma_f32_16x16x32_bf16 v[152:155], v[36:39], a[184:187], v[152:155]\n"
"ds_read_b64_tr_b16 v[232:233], v10 offset:55072\n"
"v_mfma_f32_16x16x32_bf16 v[156:159], v[24:27], a[164:167], v[156:159]\n"
"ds_read_b64_tr_b16 v[234:235], v10 offset:55584\n"
"v_mfma_f32_16x16x32_bf16 v[156:159], v[28:31], a[172:175], v[156:159]\n"
"buffer_load_dwordx4 a[228:231], v13, s[36:39], s58 offen offset:1024 sc1\n"
"v_mfma_f32_16x16x32_bf16 v[156:159], v[32:35], a[180:183], v[156:159]\n"
"ds_read_b64_tr_b16 v[236:237], v10 offset:63520\n"
"ds_read_b64_tr_b16 v[238:239], v10 offset:64032\n"
"v_mfma_f32_16x16x32_bf16 v[156:159], v[36:39], a[188:191], v[156:159]\n"
"ds_read_b64_tr_b16 v[24:25], v6 offset:38016\n"
"s_waitcnt lgkmcnt(9)\n"
"v_mfma_f32_16x16x32_bf16 v[160:163], v[40:43], a[160:163], v[160:163]\n"
"ds_read_b64_tr_b16 v[26:27], v6 offset:38528\n"
"v_mfma_f32_16x16x32_bf16 v[160:163], v[44:47], a[168:171], v[160:163]\n"
"buffer_load_dwordx4 a[232:235], v14, s[36:39], s58 offen sc1\n"
"v_mfma_f32_16x16x32_bf16 v[160:163], v[48:51], a[176:179], v[160:163]\n"
"ds_read_b64_tr_b16 v[28:29], v6 offset:39072\n"
"ds_read_b64_tr_b16 v[30:31], v6 offset:39584\n"
"v_mfma_f32_16x16x32_bf16 v[160:163], v[52:55], a[184:187], v[160:163]\n"
"ds_read_b64_tr_b16 v[32:33], v6 offset:40128\n"
"v_mfma_f32_16x16x32_bf16 v[164:167], v[40:43], a[164:167], v[164:167]\n"
"ds_read_b64_tr_b16 v[34:35], v6 offset:40640\n"
"v_mfma_f32_16x16x32_bf16 v[164:167], v[44:47], a[172:175], v[164:167]\n"
"buffer_load_dwordx4 a[236:239], v14, s[36:39], s58 offen offset:1024 sc1\n"
"v_mfma_f32_16x16x32_bf16 v[164:167], v[48:51], a[180:183], v[164:167]\n"
"ds_read_b64_tr_b16 v[36:37], v6 offset:41184\n"
"ds_read_b64_tr_b16 v[38:39], v6 offset:41696\n"
"v_mfma_f32_16x16x32_bf16 v[164:167], v[52:55], a[188:191], v[164:167]\n"
"s_waitcnt vmcnt(18) lgkmcnt(8)\n"
"s_barrier\n"
"v_mfma_f32_16x16x32_bf16 v[168:171], v[224:227], a[160:163], v[168:171]\n"
"ds_read_b64_tr_b16 v[40:41], v11 offset:10496\n"
"ds_read_b64_tr_b16 v[42:43], v11 offset:11008\n"
"v_mfma_f32_16x16x32_bf16 v[168:171], v[228:231], a[168:171], v[168:171]\n"
"buffer_load_dwordx4 a[240:243], v15, s[36:39], s58 offen sc1\n"
"v_mfma_f32_16x16x32_bf16 v[168:171], v[232:235], a[176:179], v[168:171]\n"
"ds_read_b64_tr_b16 v[44:45], v11 offset:18944\n"
"ds_read_b64_tr_b16 v[46:47], v11 offset:19456\n"
"v_mfma_f32_16x16x32_bf16 v[168:171], v[236:239], a[184:187], v[168:171]\n"
"ds_read_b64_tr_b16 v[48:49], v11 offset:27392\n"
"v_mfma_f32_16x16x32_bf16 v[172:175], v[224:227], a[164:167], v[172:175]\n"
"ds_read_b64_tr_b16 v[50:51], v11 offset:27904\n"
"v_mfma_f32_16x16x32_bf16 v[172:175], v[228:231], a[172:175], v[172:175]\n"
"buffer_load_dwordx4 a[244:247], v15, s[36:39], s58 offen offset:1024 sc1\n"
"v_mfma_f32_16x16x32_bf16 v[172:175], v[232:235], a[180:183], v[172:175]\n"
"ds_read_b64_tr_b16 v[52:53], v11 offset:35840\n"
"ds_read_b64_tr_b16 v[54:55], v11 offset:36352\n"
"v_mfma_f32_16x16x32_bf16 v[172:175], v[236:239], a[188:191], v[172:175]\n"
"ds_read_b64_tr_b16 v[224:225], v11 offset:10528\n"
"s_waitcnt lgkmcnt(9)\n"
"v_mfma_f32_16x16x32_bf16 v[120:123], v[24:27], a[160:163], v[120:123]\n"
"ds_read_b64_tr_b16 v[226:227], v11 offset:11040\n"
"v_mfma_f32_16x16x32_bf16 v[120:123], v[28:31], a[168:171], v[120:123]\n"
"buffer_load_dwordx4 a[248:251], v16, s[36:39], s58 offen sc1\n"
"v_mfma_f32_16x16x32_bf16 v[120:123], v[32:35], a[176:179], v[120:123]\n"
"ds_read_b64_tr_b16 v[228:229], v11 offset:18976\n"
"ds_read_b64_tr_b16 v[230:231], v11 offset:19488\n"
"v_mfma_f32_16x16x32_bf16 v[120:123], v[36:39], a[184:187], v[120:123]\n"
"ds_read_b64_tr_b16 v[232:233], v11 offset:27424\n"
"v_mfma_f32_16x16x32_bf16 v[124:127], v[24:27], a[164:167], v[124:127]\n"
"ds_read_b64_tr_b16 v[234:235], v11 offset:27936\n"
"v_mfma_f32_16x16x32_bf16 v[124:127], v[28:31], a[172:175], v[124:127]\n"
"ds_read_b64_tr_b16 v[236:237], v11 offset:35872\n"
"v_mfma_f32_16x16x32_bf16 v[124:127], v[32:35], a[180:183], v[124:127]\n"
"ds_read_b64_tr_b16 v[238:239], v11 offset:36384\n"
"v_mfma_f32_16x16x32_bf16 v[124:127], v[36:39], a[188:191], v[124:127]\n"
"ds_read_b64_tr_b16 v[24:25], v11 offset:10560\n"
"s_waitcnt lgkmcnt(9)\n"
"v_mfma_f32_16x16x32_bf16 v[128:131], v[40:43], a[192:195], v[128:131]\n"
"ds_read_b64_tr_b16 v[26:27], v11 offset:11072\n"
"v_mfma_f32_16x16x32_bf16 v[128:131], v[44:47], a[200:203], v[128:131]\n"
"buffer_load_dwordx4 a[252:255], v16, s[36:39], s58 offen offset:1024 sc1\n"
"v_mfma_f32_16x16x32_bf16 v[128:131], v[48:51], a[208:211], v[128:131]\n"
"ds_read_b64_tr_b16 v[28:29], v11 offset:19008\n"
"ds_read_b64_tr_b16 v[30:31], v11 offset:19520\n"
"v_mfma_f32_16x16x32_bf16 v[128:131], v[52:55], a[216:219], v[128:131]\n"
"ds_read_b64_tr_b16 v[32:33], v11 offset:27456\n"
"v_mfma_f32_16x16x32_bf16 v[132:135], v[40:43], a[196:199], v[132:135]\n"
"ds_read_b64_tr_b16 v[34:35], v11 offset:27968\n"
"s_add_u32 s58, s57, 57344\n"
"v_mfma_f32_16x16x32_bf16 v[132:135], v[44:47], a[204:207], v[132:135]\n"
"buffer_load_dwordx4 v[192:195], v13, s[36:39], s58 offen sc1\n"
"v_mfma_f32_16x16x32_bf16 v[132:135], v[48:51], a[212:215], v[132:135]\n"
"ds_read_b64_tr_b16 v[36:37], v11 offset:35904\n"
"ds_read_b64_tr_b16 v[38:39], v11 offset:36416\n"
"v_mfma_f32_16x16x32_bf16 v[132:135], v[52:55], a[220:223], v[132:135]\n"
"ds_read_b64_tr_b16 v[40:41], v11 offset:10592\n"
"s_waitcnt lgkmcnt(9)\n"
"v_mfma_f32_16x16x32_bf16 v[136:139], v[224:227], a[192:195], v[136:139]\n"
"ds_read_b64_tr_b16 v[42:43], v11 offset:11104\n"
"v_mfma_f32_16x16x32_bf16 v[136:139], v[228:231], a[200:203], v[136:139]\n"
"buffer_load_dwordx4 v[196:199], v13, s[36:39], s58 offen offset:1024 sc1\n"
"v_mfma_f32_16x16x32_bf16 v[136:139], v[232:235], a[208:211], v[136:139]\n"
"ds_read_b64_tr_b16 v[44:45], v11 offset:19040\n"
"ds_read_b64_tr_b16 v[46:47], v11 offset:19552\n"
"v_mfma_f32_16x16x32_bf16 v[136:139], v[236:239], a[216:219], v[136:139]\n"
"ds_read_b64_tr_b16 v[48:49], v11 offset:27488\n"
"v_mfma_f32_16x16x32_bf16 v[140:143], v[224:227], a[196:199], v[140:143]\n"
"ds_read_b64_tr_b16 v[50:51], v11 offset:28000\n"
"v_mfma_f32_16x16x32_bf16 v[140:143], v[228:231], a[204:207], v[140:143]\n"
"buffer_load_dwordx4 v[200:203], v14, s[36:39], s58 offen sc1\n"
"v_mfma_f32_16x16x32_bf16 v[140:143], v[232:235], a[212:215], v[140:143]\n"
"ds_read_b64_tr_b16 v[52:53], v11 offset:35936\n"
"ds_read_b64_tr_b16 v[54:55], v11 offset:36448\n"
"v_mfma_f32_16x16x32_bf16 v[140:143], v[236:239], a[220:223], v[140:143]\n"
"ds_read_b64_tr_b16 v[224:225], v11 offset:10624\n"
"s_waitcnt lgkmcnt(9)\n"
"v_mfma_f32_16x16x32_bf16 v[144:147], v[24:27], a[192:195], v[144:147]\n"
"ds_read_b64_tr_b16 v[226:227], v11 offset:11136\n"
"v_mfma_f32_16x16x32_bf16 v[144:147], v[28:31], a[200:203], v[144:147]\n"
"buffer_load_dwordx4 v[204:207], v14, s[36:39], s58 offen offset:1024 sc1\n"
"v_mfma_f32_16x16x32_bf16 v[144:147], v[32:35], a[208:211], v[144:147]\n"
"ds_read_b64_tr_b16 v[228:229], v11 offset:19072\n"
"ds_read_b64_tr_b16 v[230:231], v11 offset:19584\n"
"v_mfma_f32_16x16x32_bf16 v[144:147], v[36:39], a[216:219], v[144:147]\n"
"ds_read_b64_tr_b16 v[232:233], v11 offset:27520\n"
"v_mfma_f32_16x16x32_bf16 v[148:151], v[24:27], a[196:199], v[148:151]\n"
"ds_read_b64_tr_b16 v[234:235], v11 offset:28032\n"
"v_mfma_f32_16x16x32_bf16 v[148:151], v[28:31], a[204:207], v[148:151]\n"
"buffer_load_dwordx4 v[208:211], v15, s[36:39], s58 offen sc1\n"
"v_mfma_f32_16x16x32_bf16 v[148:151], v[32:35], a[212:215], v[148:151]\n"
"ds_read_b64_tr_b16 v[236:237], v11 offset:35968\n"
"ds_read_b64_tr_b16 v[238:239], v11 offset:36480\n"
"v_mfma_f32_16x16x32_bf16 v[148:151], v[36:39], a[220:223], v[148:151]\n"
"ds_read_b64_tr_b16 v[24:25], v11 offset:10656\n"
"s_waitcnt lgkmcnt(9)\n"
"v_mfma_f32_16x16x32_bf16 v[152:155], v[40:43], a[192:195], v[152:155]\n"
"ds_read_b64_tr_b16 v[26:27], v11 offset:11168\n"
"v_mfma_f32_16x16x32_bf16 v[152:155], v[44:47], a[200:203], v[152:155]\n"
"buffer_load_dwordx4 v[212:215], v15, s[36:39], s58 offen offset:1024 sc1\n"
"v_mfma_f32_16x16x32_bf16 v[152:155], v[48:51], a[208:211], v[152:155]\n"
"ds_read_b64_tr_b16 v[28:29], v11 offset:19104\n"
"ds_read_b64_tr_b16 v[30:31], v11 offset:19616\n"
"v_mfma_f32_16x16x32_bf16 v[152:155], v[52:55], a[216:219], v[152:155]\n"
"ds_read_b64_tr_b16 v[32:33], v11 offset:27552\n"
"v_mfma_f32_16x16x32_bf16 v[156:159], v[40:43], a[196:199], v[156:159]\n"
"ds_read_b64_tr_b16 v[34:35], v11 offset:28064\n"
"v_mfma_f32_16x16x32_bf16 v[156:159], v[44:47], a[204:207], v[156:159]\n"
"buffer_load_dwordx4 v[216:219], v16, s[36:39], s58 offen sc1\n"
"v_mfma_f32_16x16x32_bf16 v[156:159], v[48:51], a[212:215], v[156:159]\n"
"ds_read_b64_tr_b16 v[36:37], v11 offset:36000\n"
"ds_read_b64_tr_b16 v[38:39], v11 offset:36512\n"
"v_mfma_f32_16x16x32_bf16 v[156:159], v[52:55], a[220:223], v[156:159]\n"
"ds_read_b64_tr_b16 v[40:41], v9 offset:0\n"
"s_waitcnt lgkmcnt(9)\n"
"v_mfma_f32_16x16x32_bf16 v[160:163], v[224:227], a[192:195], v[160:163]\n"
"ds_read_b64_tr_b16 v[42:43], v9 offset:512\n"
"v_mfma_f32_16x16x32_bf16 v[160:163], v[228:231], a[200:203], v[160:163]\n"
"buffer_load_dwordx4 v[220:223], v16, s[36:39], s58 offen offset:1024 sc1\n"
"v_mfma_f32_16x16x32_bf16 v[160:163], v[232:235], a[208:211], v[160:163]\n"
"ds_read_b64_tr_b16 v[44:45], v9 offset:1056\n"
"ds_read_b64_tr_b16 v[46:47], v9 offset:1568\n"
"v_mfma_f32_16x16x32_bf16 v[160:163], v[236:239], a[216:219], v[160:163]\n"
"ds_read_b64_tr_b16 v[48:49], v9 offset:2112\n"
"s_add_u32 s75, s43, 262656\n"
"v_mfma_f32_16x16x32_bf16 v[164:167], v[224:227], a[196:199], v[164:167]\n"
"ds_read_b64_tr_b16 v[50:51], v9 offset:2624\n"
"s_add_u32 m0, s46, 4224\n"
"v_mfma_f32_16x16x32_bf16 v[164:167], v[228:231], a[204:207], v[164:167]\n"
"buffer_load_dwordx4 v1, s[28:31], s75 offen lds\n"
"v_mfma_f32_16x16x32_bf16 v[164:167], v[232:235], a[212:215], v[164:167]\n"
"ds_read_b64_tr_b16 v[52:53], v9 offset:3168\n"
"ds_read_b64_tr_b16 v[54:55], v9 offset:3680\n"
"v_mfma_f32_16x16x32_bf16 v[164:167], v[236:239], a[220:223], v[164:167]\n"
"s_waitcnt vmcnt(9) lgkmcnt(8)\n"
"s_barrier\n"
"v_mfma_f32_16x16x32_bf16 v[168:171], v[24:27], a[192:195], v[168:171]\n"
"ds_read_b64_tr_b16 v[224:225], v11 offset:48512\n"
"v_mfma_f32_16x16x32_bf16 v[168:171], v[28:31], a[200:203], v[168:171]\n"
"ds_read_b64_tr_b16 v[226:227], v11 offset:49024\n"
"v_mfma_f32_16x16x32_bf16 v[168:171], v[32:35], a[208:211], v[168:171]\n"
"ds_read_b64_tr_b16 v[228:229], v11 offset:56960\n"
"v_mfma_f32_16x16x32_bf16 v[168:171], v[36:39], a[216:219], v[168:171]\n"
"ds_read_b64_tr_b16 v[230:231], v11 offset:57472\n"
"v_mfma_f32_16x16x32_bf16 v[172:175], v[24:27], a[196:199], v[172:175]\n"
"ds_read_b64_tr_b16 v[232:233], v11 offset:65408\n"
"ds_read_b64_tr_b16 v[234:235], v12 offset:384\n"
"v_mfma_f32_16x16x32_bf16 v[172:175], v[28:31], a[204:207], v[172:175]\n"
"buffer_load_dwordx4 v2, s[28:31], s75 offen offset:1056 lds\n"
"v_mfma_f32_16x16x32_bf16 v[172:175], v[32:35], a[212:215], v[172:175]\n"
"ds_read_b64_tr_b16 v[236:237], v12 offset:8320\n"
"ds_read_b64_tr_b16 v[238:239], v12 offset:8832\n"
"v_mfma_f32_16x16x32_bf16 v[172:175], v[36:39], a[220:223], v[172:175]\n"
"ds_read_b64_tr_b16 v[24:25], v11 offset:48544\n"
"s_waitcnt lgkmcnt(9)\n"
"v_mfma_f32_16x16x32_bf16 v[120:123], v[40:43], a[192:195], v[120:123]\n"
"ds_read_b64_tr_b16 v[26:27], v11 offset:49056\n"
"v_mfma_f32_16x16x32_bf16 v[120:123], v[44:47], a[200:203], v[120:123]\n"
"buffer_load_dwordx4 v3, s[28:31], s75 offen offset:2112 lds\n"
"v_mfma_f32_16x16x32_bf16 v[120:123], v[48:51], a[208:211], v[120:123]\n"
"ds_read_b64_tr_b16 v[28:29], v11 offset:56992\n"
"ds_read_b64_tr_b16 v[30:31], v11 offset:57504\n"
"v_mfma_f32_16x16x32_bf16 v[120:123], v[52:55], a[216:219], v[120:123]\n"
"ds_read_b64_tr_b16 v[32:33], v11 offset:65440\n"
"v_mfma_f32_16x16x32_bf16 v[124:127], v[40:43], a[196:199], v[124:127]\n"
"ds_read_b64_tr_b16 v[34:35], v12 offset:416\n"
"v_mfma_f32_16x16x32_bf16 v[124:127], v[44:47], a[204:207], v[124:127]\n"
"buffer_load_dwordx4 v4, s[28:31], s75 offen offset:3168 lds\n"
"v_mfma_f32_16x16x32_bf16 v[124:127], v[48:51], a[212:215], v[124:127]\n"
"ds_read_b64_tr_b16 v[36:37], v12 offset:8352\n"
"ds_read_b64_tr_b16 v[38:39], v12 offset:8864\n"
"v_mfma_f32_16x16x32_bf16 v[124:127], v[52:55], a[220:223], v[124:127]\n"
"ds_read_b64_tr_b16 v[40:41], v11 offset:48576\n"
"s_waitcnt lgkmcnt(9)\n"
"v_mfma_f32_16x16x32_bf16 v[128:131], v[224:227], a[224:227], v[128:131]\n"
"ds_read_b64_tr_b16 v[42:43], v11 offset:49088\n"
"v_mfma_f32_16x16x32_bf16 v[128:131], v[228:231], a[232:235], v[128:131]\n"
"ds_read_b64_tr_b16 v[44:45], v11 offset:57024\n"
"v_mfma_f32_16x16x32_bf16 v[128:131], v[232:235], a[240:243], v[128:131]\n"
"ds_read_b64_tr_b16 v[46:47], v11 offset:57536\n"
"v_mfma_f32_16x16x32_bf16 v[128:131], v[236:239], a[248:251], v[128:131]\n"
"ds_read_b64_tr_b16 v[48:49], v11 offset:65472\n"
"v_mfma_f32_16x16x32_bf16 v[132:135], v[224:227], a[228:231], v[132:135]\n"
"ds_read_b64_tr_b16 v[50:51], v12 offset:448\n"
"v_mfma_f32_16x16x32_bf16 v[132:135], v[228:231], a[236:239], v[132:135]\n"
"ds_read_b64_tr_b16 v[52:53], v12 offset:8384\n"
"v_mfma_f32_16x16x32_bf16 v[132:135], v[232:235], a[244:247], v[132:135]\n"
"ds_read_b64_tr_b16 v[54:55], v12 offset:8896\n"
"v_mfma_f32_16x16x32_bf16 v[132:135], v[236:239], a[252:255], v[132:135]\n"
"ds_read_b64_tr_b16 v[224:225], v11 offset:48608\n"
"s_waitcnt lgkmcnt(9)\n"
"v_mfma_f32_16x16x32_bf16 v[136:139], v[24:27], a[224:227], v[136:139]\n"
"ds_read_b64_tr_b16 v[226:227], v11 offset:49120\n"
"v_mfma_f32_16x16x32_bf16 v[136:139], v[28:31], a[232:235], v[136:139]\n"
"ds_read_b64_tr_b16 v[228:229], v11 offset:57056\n"
"v_mfma_f32_16x16x32_bf16 v[136:139], v[32:35], a[240:243], v[136:139]\n"
"ds_read_b64_tr_b16 v[230:231], v11 offset:57568\n"
"v_mfma_f32_16x16x32_bf16 v[136:139], v[36:39], a[248:251], v[136:139]\n"
"ds_read_b64_tr_b16 v[232:233], v11 offset:65504\n"
"v_mfma_f32_16x16x32_bf16 v[140:143], v[24:27], a[228:231], v[140:143]\n"
"ds_read_b64_tr_b16 v[234:235], v12 offset:480\n"
"v_mfma_f32_16x16x32_bf16 v[140:143], v[28:31], a[236:239], v[140:143]\n"
"ds_read_b64_tr_b16 v[236:237], v12 offset:8416\n"
"v_mfma_f32_16x16x32_bf16 v[140:143], v[32:35], a[244:247], v[140:143]\n"
"ds_read_b64_tr_b16 v[238:239], v12 offset:8928\n"
"v_mfma_f32_16x16x32_bf16 v[140:143], v[36:39], a[252:255], v[140:143]\n"
"ds_read_b64_tr_b16 v[24:25], v11 offset:48640\n"
"s_waitcnt lgkmcnt(9)\n"
"v_mfma_f32_16x16x32_bf16 v[144:147], v[40:43], a[224:227], v[144:147]\n"
"ds_read_b64_tr_b16 v[26:27], v11 offset:49152\n"
"v_mfma_f32_16x16x32_bf16 v[144:147], v[44:47], a[232:235], v[144:147]\n"
"ds_read_b64_tr_b16 v[28:29], v11 offset:57088\n"
"v_mfma_f32_16x16x32_bf16 v[144:147], v[48:51], a[240:243], v[144:147]\n"
"ds_read_b64_tr_b16 v[30:31], v11 offset:57600\n"
"v_mfma_f32_16x16x32_bf16 v[144:147], v[52:55], a[248:251], v[144:147]\n"
"ds_read_b64_tr_b16 v[32:33], v12 offset:0\n"
"v_mfma_f32_16x16x32_bf16 v[148:151], v[40:43], a[228:231], v[148:151]\n"
"ds_read_b64_tr_b16 v[34:35], v12 offset:512\n"
"v_mfma_f32_16x16x32_bf16 v[148:151], v[44:47], a[236:239], v[148:151]\n"
"ds_read_b64_tr_b16 v[36:37], v12 offset:8448\n"
"v_mfma_f32_16x16x32_bf16 v[148:151], v[48:51], a[244:247], v[148:151]\n"
"ds_read_b64_tr_b16 v[38:39], v12 offset:8960\n"
"v_mfma_f32_16x16x32_bf16 v[148:151], v[52:55], a[252:255], v[148:151]\n"
"ds_read_b64_tr_b16 v[40:41], v11 offset:48672\n"
"s_waitcnt lgkmcnt(9)\n"
"v_mfma_f32_16x16x32_bf16 v[152:155], v[224:227], a[224:227], v[152:155]\n"
"ds_read_b64_tr_b16 v[42:43], v11 offset:49184\n"
"v_mfma_f32_16x16x32_bf16 v[152:155], v[228:231], a[232:235], v[152:155]\n"
"ds_read_b64_tr_b16 v[44:45], v11 offset:57120\n"
"v_mfma_f32_16x16x32_bf16 v[152:155], v[232:235], a[240:243], v[152:155]\n"
"ds_read_b64_tr_b16 v[46:47], v11 offset:57632\n"
"v_mfma_f32_16x16x32_bf16 v[152:155], v[236:239], a[248:251], v[152:155]\n"
"ds_read_b64_tr_b16 v[48:49], v12 offset:32\n"
"v_mfma_f32_16x16x32_bf16 v[156:159], v[224:227], a[228:231], v[156:159]\n"
"ds_read_b64_tr_b16 v[50:51], v12 offset:544\n"
"v_mfma_f32_16x16x32_bf16 v[156:159], v[228:231], a[236:239], v[156:159]\n"
"ds_read_b64_tr_b16 v[52:53], v12 offset:8480\n"
"v_mfma_f32_16x16x32_bf16 v[156:159], v[232:235], a[244:247], v[156:159]\n"
"ds_read_b64_tr_b16 v[54:55], v12 offset:8992\n"
"v_mfma_f32_16x16x32_bf16 v[156:159], v[236:239], a[252:255], v[156:159]\n"
"ds_read_b64_tr_b16 v[224:225], v9 offset:38016\n"
"s_waitcnt lgkmcnt(9)\n"
"v_mfma_f32_16x16x32_bf16 v[160:163], v[24:27], a[224:227], v[160:163]\n"
"ds_read_b64_tr_b16 v[226:227], v9 offset:38528\n"
"v_mfma_f32_16x16x32_bf16 v[160:163], v[28:31], a[232:235], v[160:163]\n"
"ds_read_b64_tr_b16 v[228:229], v9 offset:39072\n"
"v_mfma_f32_16x16x32_bf16 v[160:163], v[32:35], a[240:243], v[160:163]\n"
"ds_read_b64_tr_b16 v[230:231], v9 offset:39584\n"
"v_mfma_f32_16x16x32_bf16 v[160:163], v[36:39], a[248:251], v[160:163]\n"
"ds_read_b64_tr_b16 v[232:233], v9 offset:40128\n"
"v_mfma_f32_16x16x32_bf16 v[164:167], v[24:27], a[228:231], v[164:167]\n"
"ds_read_b64_tr_b16 v[234:235], v9 offset:40640\n"
"v_mfma_f32_16x16x32_bf16 v[164:167], v[28:31], a[236:239], v[164:167]\n"
"ds_read_b64_tr_b16 v[236:237], v9 offset:41184\n"
"v_mfma_f32_16x16x32_bf16 v[164:167], v[32:35], a[244:247], v[164:167]\n"
"ds_read_b64_tr_b16 v[238:239], v9 offset:41696\n"
"v_mfma_f32_16x16x32_bf16 v[164:167], v[36:39], a[252:255], v[164:167]\n"
"s_waitcnt vmcnt(0) lgkmcnt(8)\n"
"s_barrier\n"
"v_mfma_f32_16x16x32_bf16 v[168:171], v[40:43], a[224:227], v[168:171]\n"
"ds_read_b64_tr_b16 v[24:25], v6 offset:0\n"
"ds_read_b64_tr_b16 v[26:27], v6 offset:512\n"
"v_mfma_f32_16x16x32_bf16 v[168:171], v[44:47], a[232:235], v[168:171]\n"
"ds_read_b64_tr_b16 v[28:29], v6 offset:1056\n"
"v_mfma_f32_16x16x32_bf16 v[168:171], v[48:51], a[240:243], v[168:171]\n"
"ds_read_b64_tr_b16 v[30:31], v6 offset:1568\n"
"v_mfma_f32_16x16x32_bf16 v[168:171], v[52:55], a[248:251], v[168:171]\n"
"ds_read_b64_tr_b16 v[32:33], v6 offset:2112\n"
"v_mfma_f32_16x16x32_bf16 v[172:175], v[40:43], a[228:231], v[172:175]\n"
"ds_read_b64_tr_b16 v[34:35], v6 offset:2624\n"
"v_mfma_f32_16x16x32_bf16 v[172:175], v[44:47], a[236:239], v[172:175]\n"
"ds_read_b64_tr_b16 v[36:37], v6 offset:3168\n"
"v_mfma_f32_16x16x32_bf16 v[172:175], v[48:51], a[244:247], v[172:175]\n"
"ds_read_b64_tr_b16 v[38:39], v6 offset:3680\n"
"v_mfma_f32_16x16x32_bf16 v[172:175], v[52:55], a[252:255], v[172:175]\n"
"ds_read_b64_tr_b16 v[40:41], v10 offset:0\n"
"s_waitcnt lgkmcnt(9)\n"
"v_mfma_f32_16x16x32_bf16 v[120:123], v[224:227], a[224:227], v[120:123]\n"
"ds_read_b64_tr_b16 v[42:43], v10 offset:512\n"
"v_mfma_f32_16x16x32_bf16 v[120:123], v[228:231], a[232:235], v[120:123]\n"
"ds_read_b64_tr_b16 v[44:45], v10 offset:8448\n"
"v_mfma_f32_16x16x32_bf16 v[120:123], v[232:235], a[240:243], v[120:123]\n"
"ds_read_b64_tr_b16 v[46:47], v10 offset:8960\n"
"v_mfma_f32_16x16x32_bf16 v[120:123], v[236:239], a[248:251], v[120:123]\n"
"ds_read_b64_tr_b16 v[48:49], v10 offset:16896\n"
"v_mfma_f32_16x16x32_bf16 v[124:127], v[224:227], a[228:231], v[124:127]\n"
"ds_read_b64_tr_b16 v[50:51], v10 offset:17408\n"
"v_mfma_f32_16x16x32_bf16 v[124:127], v[228:231], a[236:239], v[124:127]\n"
"ds_read_b64_tr_b16 v[52:53], v10 offset:25344\n"
"v_mfma_f32_16x16x32_bf16 v[124:127], v[232:235], a[244:247], v[124:127]\n"
"ds_read_b64_tr_b16 v[54:55], v10 offset:25856\n"
"v_mfma_f32_16x16x32_bf16 v[124:127], v[236:239], a[252:255], v[124:127]\n"
"ds_read_b64_tr_b16 v[224:225], v10 offset:32\n"
"s_waitcnt lgkmcnt(9)\n"
"v_mfma_f32_16x16x32_bf16 v[120:123], v[24:27], v[192:195], v[120:123]\n"
"ds_read_b64_tr_b16 v[226:227], v10 offset:544\n"
"v_mfma_f32_16x16x32_bf16 v[120:123], v[28:31], v[200:203], v[120:123]\n"
"ds_read_b64_tr_b16 v[228:229], v10 offset:8480\n"
"v_mfma_f32_16x16x32_bf16 v[120:123], v[32:35], v[208:211], v[120:123]\n"
"ds_read_b64_tr_b16 v[230:231], v10 offset:8992\n"
"v_mfma_f32_16x16x32_bf16 v[120:123], v[36:39], v[216:219], v[120:123]\n"
"ds_read_b64_tr_b16 v[232:233], v10 offset:16928\n"
"v_mfma_f32_16x16x32_bf16 v[124:127], v[24:27], v[196:199], v[124:127]\n"
"ds_read_b64_tr_b16 v[234:235], v10 offset:17440\n"
"v_mfma_f32_16x16x32_bf16 v[124:127], v[28:31], v[204:207], v[124:127]\n"
"ds_read_b64_tr_b16 v[236:237], v10 offset:25376\n"
"v_mfma_f32_16x16x32_bf16 v[124:127], v[32:35], v[212:215], v[124:127]\n"
"ds_read_b64_tr_b16 v[238:239], v10 offset:25888\n"
"v_mfma_f32_16x16x32_bf16 v[124:127], v[36:39], v[220:223], v[124:127]\n"
"ds_read_b64_tr_b16 v[24:25], v10 offset:64\n"
"s_waitcnt lgkmcnt(9)\n"
"v_mfma_f32_16x16x32_bf16 v[128:131], v[40:43], v[192:195], v[128:131]\n"
"ds_read_b64_tr_b16 v[26:27], v10 offset:576\n"
"v_mfma_f32_16x16x32_bf16 v[128:131], v[44:47], v[200:203], v[128:131]\n"
"ds_read_b64_tr_b16 v[28:29], v10 offset:8512\n"
"v_mfma_f32_16x16x32_bf16 v[128:131], v[48:51], v[208:211], v[128:131]\n"
"ds_read_b64_tr_b16 v[30:31], v10 offset:9024\n"
"v_cvt_pk_bf16_f32 v112, v120, v121\n"
"v_mfma_f32_16x16x32_bf16 v[128:131], v[52:55], v[216:219], v[128:131]\n"
"ds_read_b64_tr_b16 v[32:33], v10 offset:16960\n"
"v_cvt_pk_bf16_f32 v113, v122, v123\n"
"v_mfma_f32_16x16x32_bf16 v[132:135], v[40:43], v[196:199], v[132:135]\n"
"ds_read_b64_tr_b16 v[34:35], v10 offset:17472\n"
"v_mfma_f32_16x16x32_bf16 v[132:135], v[44:47], v[204:207], v[132:135]\n"
"ds_read_b64_tr_b16 v[36:37], v10 offset:25408\n"
"v_cvt_pk_bf16_f32 v114, v124, v125\n"
"v_mfma_f32_16x16x32_bf16 v[132:135], v[48:51], v[212:215], v[132:135]\n"
"ds_read_b64_tr_b16 v[38:39], v10 offset:25920\n"
"v_mfma_f32_16x16x32_bf16 v[132:135], v[52:55], v[220:223], v[132:135]\n"
"ds_read_b64_tr_b16 v[40:41], v10 offset:96\n"
"s_waitcnt lgkmcnt(9)\n"
"v_mfma_f32_16x16x32_bf16 v[136:139], v[224:227], v[192:195], v[136:139]\n"
"ds_read_b64_tr_b16 v[42:43], v10 offset:608\n"
"v_cvt_pk_bf16_f32 v115, v126, v127\n"
"v_mfma_f32_16x16x32_bf16 v[136:139], v[228:231], v[200:203], v[136:139]\n"
"buffer_store_dwordx4 v[112:115], v8, s[36:39], s19 offen sc1\n"
"v_mfma_f32_16x16x32_bf16 v[136:139], v[232:235], v[208:211], v[136:139]\n"
"ds_read_b64_tr_b16 v[44:45], v10 offset:8544\n"
"ds_read_b64_tr_b16 v[46:47], v10 offset:9056\n"
"v_mfma_f32_16x16x32_bf16 v[136:139], v[236:239], v[216:219], v[136:139]\n"
"ds_read_b64_tr_b16 v[48:49], v10 offset:16992\n"
"v_cvt_pk_bf16_f32 v128, v128, v129\n"
"v_mfma_f32_16x16x32_bf16 v[140:143], v[224:227], v[196:199], v[140:143]\n"
"ds_read_b64_tr_b16 v[50:51], v10 offset:17504\n"
"v_cvt_pk_bf16_f32 v129, v130, v131\n"
"v_mfma_f32_16x16x32_bf16 v[140:143], v[228:231], v[204:207], v[140:143]\n"
"ds_read_b64_tr_b16 v[52:53], v10 offset:25440\n"
"v_cvt_pk_bf16_f32 v130, v132, v133\n"
"v_mfma_f32_16x16x32_bf16 v[140:143], v[232:235], v[212:215], v[140:143]\n"
"ds_read_b64_tr_b16 v[54:55], v10 offset:25952\n"
"v_mfma_f32_16x16x32_bf16 v[140:143], v[236:239], v[220:223], v[140:143]\n"
"ds_read_b64_tr_b16 v[224:225], v10 offset:128\n"
"s_waitcnt lgkmcnt(9)\n"
"v_mfma_f32_16x16x32_bf16 v[144:147], v[24:27], v[192:195], v[144:147]\n"
"ds_read_b64_tr_b16 v[226:227], v10 offset:640\n"
"v_cvt_pk_bf16_f32 v131, v134, v135\n"
"v_mfma_f32_16x16x32_bf16 v[144:147], v[28:31], v[200:203], v[144:147]\n"
"buffer_store_dwordx4 v[128:131], v8, s[36:39], s45 offen sc1\n"
"v_mfma_f32_16x16x32_bf16 v[144:147], v[32:35], v[208:211], v[144:147]\n"
"ds_read_b64_tr_b16 v[228:229], v10 offset:8576\n"
"ds_read_b64_tr_b16 v[230:231], v10 offset:9088\n"
"v_mfma_f32_16x16x32_bf16 v[144:147], v[36:39], v[216:219], v[144:147]\n"
"ds_read_b64_tr_b16 v[232:233], v10 offset:17024\n"
"v_cvt_pk_bf16_f32 v136, v136, v137\n"
"v_mfma_f32_16x16x32_bf16 v[148:151], v[24:27], v[196:199], v[148:151]\n"
"ds_read_b64_tr_b16 v[234:235], v10 offset:17536\n"
"v_cvt_pk_bf16_f32 v137, v138, v139\n"
"v_mfma_f32_16x16x32_bf16 v[148:151], v[28:31], v[204:207], v[148:151]\n"
"ds_read_b64_tr_b16 v[236:237], v10 offset:25472\n"
"v_cvt_pk_bf16_f32 v138, v140, v141\n"
"v_mfma_f32_16x16x32_bf16 v[148:151], v[32:35], v[212:215], v[148:151]\n"
"ds_read_b64_tr_b16 v[238:239], v10 offset:25984\n"
"v_mfma_f32_16x16x32_bf16 v[148:151], v[36:39], v[220:223], v[148:151]\n"
"ds_read_b64_tr_b16 v[24:25], v10 offset:160\n"
"s_waitcnt lgkmcnt(9)\n"
"v_mfma_f32_16x16x32_bf16 v[152:155], v[40:43], v[192:195], v[152:155]\n"
"ds_read_b64_tr_b16 v[26:27], v10 offset:672\n"
"v_cvt_pk_bf16_f32 v139, v142, v143\n"
"v_mfma_f32_16x16x32_bf16 v[152:155], v[44:47], v[200:203], v[152:155]\n"
"buffer_store_dwordx4 v[136:139], v8, s[36:39], s45 offen offset:1024 sc1\n"
"v_mfma_f32_16x16x32_bf16 v[152:155], v[48:51], v[208:211], v[152:155]\n"
"ds_read_b64_tr_b16 v[28:29], v10 offset:8608\n"
"ds_read_b64_tr_b16 v[30:31], v10 offset:9120\n"
"v_mfma_f32_16x16x32_bf16 v[152:155], v[52:55], v[216:219], v[152:155]\n"
"ds_read_b64_tr_b16 v[32:33], v10 offset:17056\n"
"v_cvt_pk_bf16_f32 v144, v144, v145\n"
"v_mfma_f32_16x16x32_bf16 v[156:159], v[40:43], v[196:199], v[156:159]\n"
"ds_read_b64_tr_b16 v[34:35], v10 offset:17568\n"
"v_cvt_pk_bf16_f32 v145, v146, v147\n"
"v_mfma_f32_16x16x32_bf16 v[156:159], v[44:47], v[204:207], v[156:159]\n"
"ds_read_b64_tr_b16 v[36:37], v10 offset:25504\n"
"v_cvt_pk_bf16_f32 v146, v148, v149\n"
"v_mfma_f32_16x16x32_bf16 v[156:159], v[48:51], v[212:215], v[156:159]\n"
"ds_read_b64_tr_b16 v[38:39], v10 offset:26016\n"
"v_mfma_f32_16x16x32_bf16 v[156:159], v[52:55], v[220:223], v[156:159]\n"
"ds_read_b64_tr_b16 v[40:41], v10 offset:38208\n"
"s_waitcnt lgkmcnt(9)\n"
"v_mfma_f32_16x16x32_bf16 v[160:163], v[224:227], v[192:195], v[160:163]\n"
"ds_read_b64_tr_b16 v[42:43], v10 offset:38720\n"
"v_cvt_pk_bf16_f32 v147, v150, v151\n"
"v_mfma_f32_16x16x32_bf16 v[160:163], v[228:231], v[200:203], v[160:163]\n"
"buffer_store_dwordx4 v[144:147], v8, s[36:39], s87 offen sc1\n"
"v_mfma_f32_16x16x32_bf16 v[160:163], v[232:235], v[208:211], v[160:163]\n"
"ds_read_b64_tr_b16 v[44:45], v10 offset:46656\n"
"ds_read_b64_tr_b16 v[46:47], v10 offset:47168\n"
"v_mfma_f32_16x16x32_bf16 v[160:163], v[236:239], v[216:219], v[160:163]\n"
"ds_read_b64_tr_b16 v[48:49], v10 offset:55104\n"
"v_cvt_pk_bf16_f32 v152, v152, v153\n"
"v_mfma_f32_16x16x32_bf16 v[164:167], v[224:227], v[196:199], v[164:167]\n"
"ds_read_b64_tr_b16 v[50:51], v10 offset:55616\n"
"v_cvt_pk_bf16_f32 v153, v154, v155\n"
"v_mfma_f32_16x16x32_bf16 v[164:167], v[228:231], v[204:207], v[164:167]\n"
"ds_read_b64_tr_b16 v[52:53], v10 offset:63552\n"
"v_cvt_pk_bf16_f32 v154, v156, v157\n"
"v_mfma_f32_16x16x32_bf16 v[164:167], v[232:235], v[212:215], v[164:167]\n"
"ds_read_b64_tr_b16 v[54:55], v10 offset:64064\n"
"v_mfma_f32_16x16x32_bf16 v[164:167], v[236:239], v[220:223], v[164:167]\n"
"ds_read_b64_tr_b16 v[224:225], v11 offset:10688\n"
"s_waitcnt lgkmcnt(9)\n"
"v_mfma_f32_16x16x32_bf16 v[168:171], v[24:27], v[192:195], v[168:171]\n"
"ds_read_b64_tr_b16 v[226:227], v11 offset:11200\n"
"v_cvt_pk_bf16_f32 v155, v158, v159\n"
"v_mfma_f32_16x16x32_bf16 v[168:171], v[28:31], v[200:203], v[168:171]\n"
"buffer_store_dwordx4 v[152:155], v8, s[36:39], s87 offen offset:1024 sc1\n"
"v_mfma_f32_16x16x32_bf16 v[168:171], v[32:35], v[208:211], v[168:171]\n"
"ds_read_b64_tr_b16 v[228:229], v11 offset:19136\n"
"ds_read_b64_tr_b16 v[230:231], v11 offset:19648\n"
"v_mfma_f32_16x16x32_bf16 v[168:171], v[36:39], v[216:219], v[168:171]\n"
"ds_read_b64_tr_b16 v[232:233], v11 offset:27584\n"
"v_cvt_pk_bf16_f32 v160, v160, v161\n"
"v_mfma_f32_16x16x32_bf16 v[172:175], v[24:27], v[196:199], v[172:175]\n"
"ds_read_b64_tr_b16 v[234:235], v11 offset:28096\n"
"v_cvt_pk_bf16_f32 v161, v162, v163\n"
"v_mfma_f32_16x16x32_bf16 v[172:175], v[28:31], v[204:207], v[172:175]\n"
"ds_read_b64_tr_b16 v[236:237], v11 offset:36032\n"
"v_cvt_pk_bf16_f32 v162, v164, v165\n"
"v_mfma_f32_16x16x32_bf16 v[172:175], v[32:35], v[212:215], v[172:175]\n"
"ds_read_b64_tr_b16 v[238:239], v11 offset:36544\n"
"v_mfma_f32_16x16x32_bf16 v[172:175], v[36:39], v[220:223], v[172:175]\n"
"ds_read_b64_tr_b16 v[24:25], v11 offset:48704\n"
"s_waitcnt lgkmcnt(9)\n"
"v_mfma_f32_16x16x32_bf16 v[176:179], v[40:43], a[160:163], v[176:179]\n"
"ds_read_b64_tr_b16 v[26:27], v11 offset:49216\n"
"v_cvt_pk_bf16_f32 v163, v166, v167\n"
"v_mfma_f32_16x16x32_bf16 v[176:179], v[44:47], a[168:171], v[176:179]\n"
"buffer_store_dwordx4 v[160:163], v8, s[36:39], s82 offen sc1\n"
"v_mfma_f32_16x16x32_bf16 v[176:179], v[48:51], a[176:179], v[176:179]\n"
"ds_read_b64_tr_b16 v[28:29], v11 offset:57152\n"
"ds_read_b64_tr_b16 v[30:31], v11 offset:57664\n"
"v_mfma_f32_16x16x32_bf16 v[176:179], v[52:55], a[184:187], v[176:179]\n"
"ds_read_b64_tr_b16 v[32:33], v12 offset:64\n"
"v_cvt_pk_bf16_f32 v168, v168, v169\n"
"v_mfma_f32_16x16x32_bf16 v[180:183], v[40:43], a[164:167], v[180:183]\n"
"ds_read_b64_tr_b16 v[34:35], v12 offset:576\n"
"v_cvt_pk_bf16_f32 v169, v170, v171\n"
"v_mfma_f32_16x16x32_bf16 v[180:183], v[44:47], a[172:175], v[180:183]\n"
"ds_read_b64_tr_b16 v[36:37], v12 offset:8512\n"
"v_cvt_pk_bf16_f32 v170, v172, v173\n"
"v_mfma_f32_16x16x32_bf16 v[180:183], v[48:51], a[180:183], v[180:183]\n"
"ds_read_b64_tr_b16 v[38:39], v12 offset:9024\n"
"v_mfma_f32_16x16x32_bf16 v[180:183], v[52:55], a[188:191], v[180:183]\n"
"ds_read_b64_tr_b16 v[40:41], v10 offset:192\n"
"s_waitcnt lgkmcnt(9)\n"
"v_mfma_f32_16x16x32_bf16 v[176:179], v[224:227], a[192:195], v[176:179]\n"
"ds_read_b64_tr_b16 v[42:43], v10 offset:704\n"
"v_cvt_pk_bf16_f32 v171, v174, v175\n"
"v_mfma_f32_16x16x32_bf16 v[176:179], v[228:231], a[200:203], v[176:179]\n"
"buffer_store_dwordx4 v[168:171], v8, s[36:39], s82 offen offset:1024 sc1\n"
"v_mfma_f32_16x16x32_bf16 v[176:179], v[232:235], a[208:211], v[176:179]\n"
"ds_read_b64_tr_b16 v[44:45], v10 offset:8640\n"
"ds_read_b64_tr_b16 v[46:47], v10 offset:9152\n"
"v_mfma_f32_16x16x32_bf16 v[176:179], v[236:239], a[216:219], v[176:179]\n"
"ds_read_b64_tr_b16 v[48:49], v10 offset:17088\n"
"v_mfma_f32_16x16x32_bf16 v[180:183], v[224:227], a[196:199], v[180:183]\n"
"ds_read_b64_tr_b16 v[50:51], v10 offset:17600\n"
"v_mfma_f32_16x16x32_bf16 v[180:183], v[228:231], a[204:207], v[180:183]\n"
"ds_read_b64_tr_b16 v[52:53], v10 offset:25536\n"
"v_mfma_f32_16x16x32_bf16 v[180:183], v[232:235], a[212:215], v[180:183]\n"
"ds_read_b64_tr_b16 v[54:55], v10 offset:26048\n"
"v_mfma_f32_16x16x32_bf16 v[180:183], v[236:239], a[220:223], v[180:183]\n"
"ds_read_b64_tr_b16 v[224:225], v10 offset:38240\n"
"s_waitcnt lgkmcnt(9)\n"
"v_mfma_f32_16x16x32_bf16 v[176:179], v[24:27], a[224:227], v[176:179]\n"
"ds_read_b64_tr_b16 v[226:227], v10 offset:38752\n"
"v_mfma_f32_16x16x32_bf16 v[176:179], v[28:31], a[232:235], v[176:179]\n"
"ds_read_b64_tr_b16 v[228:229], v10 offset:46688\n"
"v_mfma_f32_16x16x32_bf16 v[176:179], v[32:35], a[240:243], v[176:179]\n"
"ds_read_b64_tr_b16 v[230:231], v10 offset:47200\n"
"v_mfma_f32_16x16x32_bf16 v[176:179], v[36:39], a[248:251], v[176:179]\n"
"ds_read_b64_tr_b16 v[232:233], v10 offset:55136\n"
"v_mfma_f32_16x16x32_bf16 v[180:183], v[24:27], a[228:231], v[180:183]\n"
"ds_read_b64_tr_b16 v[234:235], v10 offset:55648\n"
"v_mfma_f32_16x16x32_bf16 v[180:183], v[28:31], a[236:239], v[180:183]\n"
"ds_read_b64_tr_b16 v[236:237], v10 offset:63584\n"
"v_mfma_f32_16x16x32_bf16 v[180:183], v[32:35], a[244:247], v[180:183]\n"
"ds_read_b64_tr_b16 v[238:239], v10 offset:64096\n"
"v_mfma_f32_16x16x32_bf16 v[180:183], v[36:39], a[252:255], v[180:183]\n"
"ds_read_b64_tr_b16 v[24:25], v11 offset:10720\n"
"s_waitcnt lgkmcnt(9)\n"
"v_mfma_f32_16x16x32_bf16 v[176:179], v[40:43], v[192:195], v[176:179]\n"
"ds_read_b64_tr_b16 v[26:27], v11 offset:11232\n"
"v_mfma_f32_16x16x32_bf16 v[176:179], v[44:47], v[200:203], v[176:179]\n"
"buffer_load_dwordx4 v[64:67], v8, s[36:39], s70 offen sc1\n"
"v_mfma_f32_16x16x32_bf16 v[176:179], v[48:51], v[208:211], v[176:179]\n"
"ds_read_b64_tr_b16 v[28:29], v11 offset:19168\n"
"ds_read_b64_tr_b16 v[30:31], v11 offset:19680\n"
"v_mfma_f32_16x16x32_bf16 v[176:179], v[52:55], v[216:219], v[176:179]\n"
"ds_read_b64_tr_b16 v[32:33], v11 offset:27616\n"
"v_mfma_f32_16x16x32_bf16 v[180:183], v[40:43], v[196:199], v[180:183]\n"
"ds_read_b64_tr_b16 v[34:35], v11 offset:28128\n"
"v_mfma_f32_16x16x32_bf16 v[180:183], v[44:47], v[204:207], v[180:183]\n"
"buffer_load_dwordx4 v[68:71], v8, s[36:39], s71 offen sc1\n"
"v_mfma_f32_16x16x32_bf16 v[180:183], v[48:51], v[212:215], v[180:183]\n"
"ds_read_b64_tr_b16 v[36:37], v11 offset:36064\n"
"ds_read_b64_tr_b16 v[38:39], v11 offset:36576\n"
"v_mfma_f32_16x16x32_bf16 v[180:183], v[52:55], v[220:223], v[180:183]\n"
"ds_read_b64_tr_b16 v[40:41], v11 offset:48736\n"
"s_waitcnt lgkmcnt(9)\n"
"v_mfma_f32_16x16x32_bf16 v[184:187], v[224:227], a[160:163], v[184:187]\n"
"ds_read_b64_tr_b16 v[42:43], v11 offset:49248\n"
"v_mfma_f32_16x16x32_bf16 v[184:187], v[228:231], a[168:171], v[184:187]\n"
"buffer_load_dwordx4 v[72:75], v8, s[36:39], s74 offen sc1\n"
"v_mfma_f32_16x16x32_bf16 v[184:187], v[232:235], a[176:179], v[184:187]\n"
"ds_read_b64_tr_b16 v[44:45], v11 offset:57184\n"
"ds_read_b64_tr_b16 v[46:47], v11 offset:57696\n"
"v_mfma_f32_16x16x32_bf16 v[184:187], v[236:239], a[184:187], v[184:187]\n"
"ds_read_b64_tr_b16 v[48:49], v12 offset:96\n"
"v_mfma_f32_16x16x32_bf16 v[188:191], v[224:227], a[164:167], v[188:191]\n"
"ds_read_b64_tr_b16 v[50:51], v12 offset:608\n"
"v_mfma_f32_16x16x32_bf16 v[188:191], v[228:231], a[172:175], v[188:191]\n"
"buffer_load_dwordx4 v[80:83], v8, s[36:39], s68 offen sc1\n"
"v_mfma_f32_16x16x32_bf16 v[188:191], v[232:235], a[180:183], v[188:191]\n"
"ds_read_b64_tr_b16 v[52:53], v12 offset:8544\n"
"ds_read_b64_tr_b16 v[54:55], v12 offset:9056\n"
"v_mfma_f32_16x16x32_bf16 v[188:191], v[236:239], a[188:191], v[188:191]\n"
"ds_read_b64_tr_b16 v[224:225], v10 offset:224\n"
"s_waitcnt lgkmcnt(9)\n"
"v_mfma_f32_16x16x32_bf16 v[184:187], v[24:27], a[192:195], v[184:187]\n"
"ds_read_b64_tr_b16 v[226:227], v10 offset:736\n"
"v_mfma_f32_16x16x32_bf16 v[184:187], v[28:31], a[200:203], v[184:187]\n"
"buffer_load_dwordx4 v[84:87], v8, s[36:39], s68 offen offset:1024 sc1\n"
"v_mfma_f32_16x16x32_bf16 v[184:187], v[32:35], a[208:211], v[184:187]\n"
"ds_read_b64_tr_b16 v[228:229], v10 offset:8672\n"
"ds_read_b64_tr_b16 v[230:231], v10 offset:9184\n"
"v_mfma_f32_16x16x32_bf16 v[184:187], v[36:39], a[216:219], v[184:187]\n"
"ds_read_b64_tr_b16 v[232:233], v10 offset:17120\n"
"v_mfma_f32_16x16x32_bf16 v[188:191], v[24:27], a[196:199], v[188:191]\n"
"ds_read_b64_tr_b16 v[234:235], v10 offset:17632\n"
"v_mfma_f32_16x16x32_bf16 v[188:191], v[28:31], a[204:207], v[188:191]\n"
"buffer_load_dwordx4 v[88:91], v8, s[36:39], s68 offen offset:2048 sc1\n"
"v_mfma_f32_16x16x32_bf16 v[188:191], v[32:35], a[212:215], v[188:191]\n"
"ds_read_b64_tr_b16 v[236:237], v10 offset:25568\n"
"ds_read_b64_tr_b16 v[238:239], v10 offset:26080\n"
"v_mfma_f32_16x16x32_bf16 v[188:191], v[36:39], a[220:223], v[188:191]\n"
"s_waitcnt lgkmcnt(8)\n"
"v_mfma_f32_16x16x32_bf16 v[184:187], v[40:43], a[224:227], v[184:187]\n"
"v_mfma_f32_16x16x32_bf16 v[184:187], v[44:47], a[232:235], v[184:187]\n"
"buffer_load_dwordx4 v[92:95], v8, s[36:39], s68 offen offset:3072 sc1\n"
"v_mfma_f32_16x16x32_bf16 v[184:187], v[48:51], a[240:243], v[184:187]\n"
"v_mfma_f32_16x16x32_bf16 v[184:187], v[52:55], a[248:251], v[184:187]\n"
"v_mfma_f32_16x16x32_bf16 v[188:191], v[40:43], a[228:231], v[188:191]\n"
"v_mfma_f32_16x16x32_bf16 v[188:191], v[44:47], a[236:239], v[188:191]\n"
"buffer_load_dwordx4 v[96:99], v8, s[36:39], s69 offen sc1\n"
"v_mfma_f32_16x16x32_bf16 v[188:191], v[48:51], a[244:247], v[188:191]\n"
"v_mfma_f32_16x16x32_bf16 v[188:191], v[52:55], a[252:255], v[188:191]\n"
"s_waitcnt lgkmcnt(0)\n"
"v_mfma_f32_16x16x32_bf16 v[184:187], v[224:227], v[192:195], v[184:187]\n"
"v_mfma_f32_16x16x32_bf16 v[184:187], v[228:231], v[200:203], v[184:187]\n"
"buffer_load_dwordx4 v[100:103], v8, s[36:39], s69 offen offset:1024 sc1\n"
"v_mfma_f32_16x16x32_bf16 v[184:187], v[232:235], v[208:211], v[184:187]\n"
"v_mfma_f32_16x16x32_bf16 v[184:187], v[236:239], v[216:219], v[184:187]\n"
"v_mfma_f32_16x16x32_bf16 v[188:191], v[224:227], v[196:199], v[188:191]\n"
"v_mfma_f32_16x16x32_bf16 v[188:191], v[228:231], v[204:207], v[188:191]\n"
"v_mfma_f32_16x16x32_bf16 v[188:191], v[232:235], v[212:215], v[188:191]\n"
"v_mfma_f32_16x16x32_bf16 v[188:191], v[236:239], v[220:223], v[188:191]\n"
"v_and_b32 v25, 15, %[tid]\n"
"v_bfe_u32 v24, %[tid], 4, 2\n"
"s_lshr_b32 s50, s84, 2\n"
"s_and_b32 s51, s84, 3\n"
"v_bfe_u32 v31, %[tid], 2, 4\n"
"v_lshlrev_b32 v31, 6, v31\n"
"v_and_b32 v32, 3, %[tid]\n"
"v_lshl_add_u32 v31, v32, 3, v31\n"
"s_add_u32 s55, s77, 152576\n"
"v_add_u32 v31, s55, v31\n"
"s_lshl_b32 s55, s50, 7\n"
"s_add_u32 s55, s55, 161792\n"
"v_lshl_add_u32 v32, v24, 4, s55\n"
"s_lshl_b32 s55, s33, 7\n"
"s_add_u32 s55, s55, 162560\n"
"v_lshl_add_u32 v33, v25, 2, s55\n"
"ds_read_b64_tr_b16 v[40:41], v31\n"
"ds_read_b64_tr_b16 v[42:43], v31 offset:32\n"
"ds_read_b128 v[48:51], v32\n"
"ds_read_b64_tr_b16 v[44:45], v31 offset:1024\n"
"ds_read_b64_tr_b16 v[46:47], v31 offset:1056\n"
"ds_read_b128 v[52:55], v32 offset:64\n"
"ds_read_b32 v56, v33\n"
"ds_read_b32 v57, v33 offset:64\n"
"v_subrev_u32 v31, s77, v31\n"
"s_lshl_b32 s55, s51, 6\n"
"s_add_u32 s55, s55, 162304\n"
"v_lshl_add_u32 v32, v24, 4, s55\n"
"ds_read_b64_tr_b16 v[26:27], v31 offset:8192\n"
"ds_read_b64_tr_b16 v[28:29], v31 offset:8224\n"
"ds_read_b128 v[36:39], v32\n"
"s_lshl_b32 s54, s85, 7\n"
"s_lshl_b32 s55, s33, 5\n"
"s_add_u32 s54, s54, s55\n"
"v_add_u32 v33, s54, v25\n"
"v_mul_u32_u24 v33, 1152, v33\n"
"s_lshl_b32 s54, s51, 5\n"
"s_add_u32 s54, s54, 1024\n"
"v_lshl_add_u32 v30, v24, 3, s54\n"
"v_add_u32 v30, v30, v33\n"
"v_add_u32 v31, 18432, v30\n"
"s_lshl_b32 s54, s51, 8\n"
"s_lshl_b32 s55, s50, 6\n"
"s_add_u32 s54, s54, s55\n"
"v_add_u32 v34, s54, v33\n"
"v_and_b32 v35, 1, v24\n"
"v_lshl_add_u32 v34, v35, 5, v34\n"
"v_lshrrev_b32 v35, 1, v24\n"
"v_lshl_add_u32 v34, v35, 4, v34\n"
"v_add_u32 v35, 18432, v34\n"
"s_waitcnt lgkmcnt(10)\n"
"v_lshlrev_b32 v128, 16, v40\n"
"s_waitcnt lgkmcnt(4)\n"
"v_mul_f32 v144, v48, v56\n"
"v_and_b32 v129, 0xffff0000, v40\n"
"v_mul_f32 v145, v49, v56\n"
"v_lshlrev_b32 v130, 16, v41\n"
"v_mul_f32 v146, v50, v56\n"
"v_and_b32 v131, 0xffff0000, v41\n"
"v_mul_f32 v147, v51, v56\n"
"v_lshlrev_b32 v132, 16, v42\n"
"s_waitcnt lgkmcnt(3)\n"
"v_mul_f32 v148, v48, v57\n"
"v_and_b32 v133, 0xffff0000, v42\n"
"v_mul_f32 v149, v49, v57\n"
"v_lshlrev_b32 v134, 16, v43\n"
"v_mul_f32 v150, v50, v57\n"
"v_and_b32 v135, 0xffff0000, v43\n"
"v_mul_f32 v151, v51, v57\n"
"v_lshlrev_b32 v136, 16, v44\n"
"v_mul_f32 v152, v52, v56\n"
"v_and_b32 v137, 0xffff0000, v44\n"
"v_mul_f32 v153, v53, v56\n"
"v_lshlrev_b32 v138, 16, v45\n"
"v_mul_f32 v154, v54, v56\n"
"v_and_b32 v139, 0xffff0000, v45\n"
"v_mul_f32 v155, v55, v56\n"
"v_lshlrev_b32 v140, 16, v46\n"
"v_mul_f32 v156, v52, v57\n"
"v_and_b32 v141, 0xffff0000, v46\n"
"v_mul_f32 v157, v53, v57\n"
"v_lshlrev_b32 v142, 16, v47\n"
"v_mul_f32 v158, v54, v57\n"
"v_and_b32 v143, 0xffff0000, v47\n"
"v_mul_f32 v159, v55, v57\n"
"s_waitcnt lgkmcnt(2)\n"
"v_lshlrev_b32 v104, 16, v26\n"
"s_waitcnt lgkmcnt(0)\n"
"v_mul_f32 v112, v36, v56\n"
"v_and_b32 v105, 0xffff0000, v26\n"
"v_mul_f32 v113, v37, v56\n"
"v_lshlrev_b32 v106, 16, v27\n"
"v_mul_f32 v114, v38, v56\n"
"v_and_b32 v107, 0xffff0000, v27\n"
"v_mul_f32 v115, v39, v56\n"
"v_lshlrev_b32 v108, 16, v28\n"
"v_mul_f32 v116, v36, v57\n"
"v_and_b32 v109, 0xffff0000, v28\n"
"v_mul_f32 v117, v37, v57\n"
"v_lshlrev_b32 v110, 16, v29\n"
"v_mul_f32 v118, v38, v57\n"
"v_and_b32 v111, 0xffff0000, v29\n"
"v_mul_f32 v119, v39, v57\n"
"s_mov_b32 s46, 0x3f80\n"
"s_mov_b32 s47, 0x3f800000\n"
"s_cmp_eq_u32 s50, s33\n"
"s_cselect_b64 s[44:45], -1, 0\n"
"s_waitcnt vmcnt(8)\n"
"v_max3_u32 v171, v64, v65, v66\n"
"s_waitcnt vmcnt(7)\n"
"v_max3_u32 v171, v171, v67, v68\n"
"v_max3_u32 v171, v171, v69, v70\n"
"s_waitcnt vmcnt(6)\n"
"v_max3_u32 v171, v171, v71, v72\n"
"v_max3_u32 v171, v171, v73, v74\n"
"v_max3_u32 v171, v171, v75, v75\n"
"s_waitcnt vmcnt(5)\n"
"v_max3_u32 v168, v80, v81, v82\n"
"s_waitcnt vmcnt(4)\n"
"v_max3_u32 v168, v168, v83, v84\n"
"v_max3_u32 v168, v168, v85, v86\n"
"v_max3_u32 v168, v168, v87, v87\n"
"s_waitcnt vmcnt(3)\n"
"v_max3_u32 v169, v88, v89, v90\n"
"s_waitcnt vmcnt(2)\n"
"v_max3_u32 v169, v169, v91, v92\n"
"v_max3_u32 v169, v169, v93, v94\n"
"v_max3_u32 v169, v169, v95, v95\n"
"s_waitcnt vmcnt(1)\n"
"v_max3_u32 v170, v96, v97, v98\n"
"s_waitcnt vmcnt(0)\n"
"v_max3_u32 v170, v170, v99, v100\n"
"v_max3_u32 v170, v170, v101, v102\n"
"v_max3_u32 v170, v170, v103, v103\n"
"v_max3_u32 v172, v168, v169, v170\n"
"v_max_u32 v172, v172, v171\n"
"v_cmp_eq_u32 vcc, -1, v172\n"
"s_nop 1\n"
"s_cbranch_vccnz SLOWR_%=\n"
"BACKR_%=:\n"
"v_dot2c_f32_bf16 v176, s46, v80\n"
"v_dot2c_f32_bf16 v177, s47, v80\n"
"v_dot2c_f32_bf16 v178, s46, v81\n"
"v_dot2c_f32_bf16 v179, s47, v81\n"
"v_dot2c_f32_bf16 v180, s46, v82\n"
"v_dot2c_f32_bf16 v181, s47, v82\n"
"v_dot2c_f32_bf16 v182, s46, v83\n"
"v_dot2c_f32_bf16 v183, s47, v83\n"
"v_dot2c_f32_bf16 v176, s46, v88\n"
"v_dot2c_f32_bf16 v177, s47, v88\n"
"v_dot2c_f32_bf16 v178, s46, v89\n"
"v_dot2c_f32_bf16 v179, s47, v89\n"
"v_dot2c_f32_bf16 v180, s46, v90\n"
"v_dot2c_f32_bf16 v181, s47, v90\n"
"v_dot2c_f32_bf16 v182, s46, v91\n"
"v_dot2c_f32_bf16 v183, s47, v91\n"
"v_dot2c_f32_bf16 v176, s46, v96\n"
"v_dot2c_f32_bf16 v177, s47, v96\n"
"v_dot2c_f32_bf16 v178, s46, v97\n"
"v_dot2c_f32_bf16 v179, s47, v97\n"
"v_dot2c_f32_bf16 v180, s46, v98\n"
"v_dot2c_f32_bf16 v181, s47, v98\n"
"v_dot2c_f32_bf16 v182, s46, v99\n"
"v_dot2c_f32_bf16 v183, s47, v99\n"
"v_dot2c_f32_bf16 v184, s46, v84\n"
"v_dot2c_f32_bf16 v185, s47, v84\n"
"v_dot2c_f32_bf16 v186, s46, v85\n"
"v_dot2c_f32_bf16 v187, s47, v85\n"
"v_dot2c_f32_bf16 v188, s46, v86\n"
"v_dot2c_f32_bf16 v189, s47, v86\n"
"v_dot2c_f32_bf16 v190, s46, v87\n"
"v_dot2c_f32_bf16 v191, s47, v87\n"
"v_dot2c_f32_bf16 v184, s46, v92\n"
"v_dot2c_f32_bf16 v185, s47, v92\n"
"v_dot2c_f32_bf16 v186, s46, v93\n"
"v_dot2c_f32_bf16 v187, s47, v93\n"
"v_dot2c_f32_bf16 v188, s46, v94\n"
"v_dot2c_f32_bf16 v189, s47, v94\n"
"v_dot2c_f32_bf16 v190, s46, v95\n"
"v_dot2c_f32_bf16 v191, s47, v95\n"
"v_dot2c_f32_bf16 v184, s46, v100\n"
"v_dot2c_f32_bf16 v185, s47, v100\n"
"v_dot2c_f32_bf16 v186, s46, v101\n"
"v_dot2c_f32_bf16 v187, s47, v101\n"
"v_dot2c_f32_bf16 v188, s46, v102\n"
"v_dot2c_f32_bf16 v189, s47, v102\n"
"v_dot2c_f32_bf16 v190, s46, v103\n"
"v_dot2c_f32_bf16 v191, s47, v103\n"
"v_fma_f32 v128, v176, v144, v128\n"
"v_fma_f32 v129, v177, v145, v129\n"
"v_fma_f32 v130, v178, v146, v130\n"
"v_fma_f32 v131, v179, v147, v131\n"
"v_fma_f32 v132, v180, v148, v132\n"
"v_fma_f32 v133, v181, v149, v133\n"
"v_fma_f32 v134, v182, v150, v134\n"
"v_fma_f32 v135, v183, v151, v135\n"
"v_fma_f32 v136, v184, v152, v136\n"
"v_fma_f32 v137, v185, v153, v137\n"
"v_fma_f32 v138, v186, v154, v138\n"
"v_fma_f32 v139, v187, v155, v139\n"
"v_fma_f32 v140, v188, v156, v140\n"
"v_fma_f32 v141, v189, v157, v141\n"
"v_fma_f32 v142, v190, v158, v142\n"
"v_fma_f32 v143, v191, v159, v143\n"
"v_cvt_pk_bf16_f32 v40, v128, v129\n"
"v_cvt_pk_bf16_f32 v41, v130, v131\n"
"v_cvt_pk_bf16_f32 v42, v136, v137\n"
"v_cvt_pk_bf16_f32 v43, v138, v139\n"
"v_cvt_pk_bf16_f32 v44, v132, v133\n"
"v_cvt_pk_bf16_f32 v45, v134, v135\n"
"v_cvt_pk_bf16_f32 v46, v140, v141\n"
"v_cvt_pk_bf16_f32 v47, v142, v143\n"
"v_permlane16_swap_b32 v40, v42\n"
"v_permlane16_swap_b32 v41, v43\n"
"v_permlane16_swap_b32 v44, v46\n"
"v_permlane16_swap_b32 v45, v47\n"
"global_store_dwordx4 v34, v[40:43], s[98:99]\n"
"global_store_dwordx4 v35, v[44:47], s[98:99]\n"
"s_mov_b64 exec, s[44:45]\n"
"s_cbranch_execz ZNE_%=\n"
"v_dot2c_f32_bf16 v120, s46, v64\n"
"v_dot2c_f32_bf16 v121, s47, v64\n"
"v_dot2c_f32_bf16 v122, s46, v65\n"
"v_dot2c_f32_bf16 v123, s47, v65\n"
"v_dot2c_f32_bf16 v124, s46, v66\n"
"v_dot2c_f32_bf16 v125, s47, v66\n"
"v_dot2c_f32_bf16 v126, s46, v67\n"
"v_dot2c_f32_bf16 v127, s47, v67\n"
"v_dot2c_f32_bf16 v120, s46, v68\n"
"v_dot2c_f32_bf16 v121, s47, v68\n"
"v_dot2c_f32_bf16 v122, s46, v69\n"
"v_dot2c_f32_bf16 v123, s47, v69\n"
"v_dot2c_f32_bf16 v124, s46, v70\n"
"v_dot2c_f32_bf16 v125, s47, v70\n"
"v_dot2c_f32_bf16 v126, s46, v71\n"
"v_dot2c_f32_bf16 v127, s47, v71\n"
"v_dot2c_f32_bf16 v120, s46, v72\n"
"v_dot2c_f32_bf16 v121, s47, v72\n"
"v_dot2c_f32_bf16 v122, s46, v73\n"
"v_dot2c_f32_bf16 v123, s47, v73\n"
"v_dot2c_f32_bf16 v124, s46, v74\n"
"v_dot2c_f32_bf16 v125, s47, v74\n"
"v_dot2c_f32_bf16 v126, s46, v75\n"
"v_dot2c_f32_bf16 v127, s47, v75\n"
"v_fma_f32 v104, v120, v112, v104\n"
"v_fma_f32 v105, v121, v113, v105\n"
"v_fma_f32 v106, v122, v114, v106\n"
"v_fma_f32 v107, v123, v115, v107\n"
"v_fma_f32 v108, v124, v116, v108\n"
"v_fma_f32 v109, v125, v117, v109\n"
"v_fma_f32 v110, v126, v118, v110\n"
"v_fma_f32 v111, v127, v119, v111\n"
"v_cvt_pk_bf16_f32 v26, v104, v105\n"
"v_cvt_pk_bf16_f32 v27, v106, v107\n"
"v_cvt_pk_bf16_f32 v28, v108, v109\n"
"v_cvt_pk_bf16_f32 v29, v110, v111\n"
"global_store_dwordx2 v30, v[26:27], s[98:99]\n"
"global_store_dwordx2 v31, v[28:29], s[98:99]\n"
"ZNE_%=:\n"
"s_mov_b64 exec, -1\n"
"s_branch END_%=\n"
"SLOWU_%=:\n"
"s_mov_b32 s60, 0\n"
"LOOPU_%=:\n"
"buffer_load_dword v176, v184, s[36:39], 0 offen sc1\n"
"s_add_u32 s60, s60, 1\n"
"s_waitcnt vmcnt(0)\n"
"v_cmp_ne_u32 vcc, s86, v176\n"
"s_cmp_gt_u32 s60, 2000000\n"
"s_cbranch_scc1 BACKU_%=\n"
"s_cbranch_vccnz LOOPU_%=\n"
"s_branch BACKU_%=\n"
"SLOWS_%=:\n"
"s_mov_b32 s60, 0\n"
"LOOPS_%=:\n"
"buffer_load_dwordx4 v[160:163], v181, s[36:39], 0 offen sc1\n"
"buffer_load_dwordx4 v[164:167], v181, s[36:39], 0 offen offset:256 sc1\n"
"buffer_load_dwordx4 v[168:171], v181, s[36:39], 0 offen offset:2048 sc1\n"
"buffer_load_dwordx4 v[172:175], v181, s[36:39], 0 offen offset:2304 sc1\n"
"s_add_u32 s60, s60, 1\n"
"s_waitcnt vmcnt(0)\n"
"v_xor_b32 v177, s86, v161\n"
"v_bitop3_b32 v177, v177, v163, s86 bitop3:0xf6\n"
"v_bitop3_b32 v177, v177, v165, s86 bitop3:0xf6\n"
"v_bitop3_b32 v177, v177, v167, s86 bitop3:0xf6\n"
"v_bitop3_b32 v177, v177, v169, s86 bitop3:0xf6\n"
"v_bitop3_b32 v177, v177, v171, s86 bitop3:0xf6\n"
"v_bitop3_b32 v177, v177, v173, s86 bitop3:0xf6\n"
"v_bitop3_b32 v177, v177, v175, s86 bitop3:0xf6\n"
"v_cmp_ne_u32 vcc, 0, v177\n"
"s_cmp_gt_u32 s60, 2000000\n"
"s_cbranch_scc1 BACKS_%=\n"
"s_cbranch_vccnz LOOPS_%=\n"
"s_branch BACKS_%=\n"
"SLOW1_%=:\n"
"s_mov_b32 s60, 0\n"
"LOOP1_%=:\n"
"buffer_load_dwordx4 v[24:27], off, s[36:39], s59 offset:16 sc1\n"
"s_add_u32 s60, s60, 1\n"
"s_waitcnt vmcnt(0)\n"
"v_xor_b32 v24, s86, v24\n"
"v_bitop3_b32 v24, v24, v25, s86 bitop3:0xf6\n"
"v_bitop3_b32 v24, v24, v26, s86 bitop3:0xf6\n"
"v_bitop3_b32 v24, v24, v27, s86 bitop3:0xf6\n"
"v_cmp_ne_u32 vcc, 0, v24\n"
"s_cmp_gt_u32 s60, 2000000\n"
"s_cbranch_scc1 BACK1_%=\n"
"s_cbranch_vccnz LOOP1_%=\n"
"s_branch BACK1_%=\n"
"SLOW2_%=:\n"
"s_mov_b32 s60, 0\n"
"LOOP2_%=:\n"
"buffer_load_dwordx4 v[24:27], off, s[36:39], s59 offset:32 sc1\n"
"s_add_u32 s60, s60, 1\n"
"s_waitcnt vmcnt(0)\n"
"v_xor_b32 v24, s86, v24\n"
"v_bitop3_b32 v24, v24, v25, s86 bitop3:0xf6\n"
"v_bitop3_b32 v24, v24, v26, s86 bitop3:0xf6\n"
"v_bitop3_b32 v24, v24, v27, s86 bitop3:0xf6\n"
"v_cmp_ne_u32 vcc, 0, v24\n"
"s_cmp_gt_u32 s60, 2000000\n"
"s_cbranch_scc1 BACK2_%=\n"
"s_cbranch_vccnz LOOP2_%=\n"
"s_branch BACK2_%=\n"
"SLOW3_%=:\n"
"s_mov_b32 s60, 0\n"
"LOOP3_%=:\n"
"buffer_load_dwordx4 v[240:243], off, s[36:39], s59 offset:48 sc1\n"
"s_add_u32 s60, s60, 1\n"
"s_waitcnt vmcnt(0)\n"
"v_xor_b32 v240, s86, v240\n"
"v_bitop3_b32 v240, v240, v241, s86 bitop3:0xf6\n"
"v_bitop3_b32 v240, v240, v242, s86 bitop3:0xf6\n"
"v_bitop3_b32 v240, v240, v243, s86 bitop3:0xf6\n"
"v_cmp_ne_u32 vcc, 0, v240\n"
"s_cmp_gt_u32 s60, 2000000\n"
"s_cbranch_scc1 BACK3_%=\n"
"s_cbranch_vccnz LOOP3_%=\n"
"s_branch BACK3_%=\n"
"SLOW4_%=:\n"
"s_mov_b32 s60, 0\n"
"LOOP4_%=:\n"
"buffer_load_dwordx4 v[240:243], off, s[36:39], s59 offset:64 sc1\n"
"s_add_u32 s60, s60, 1\n"
"s_waitcnt vmcnt(0)\n"
"v_xor_b32 v240, s86, v240\n"
"v_bitop3_b32 v240, v240, v241, s86 bitop3:0xf6\n"
"v_bitop3_b32 v240, v240, v242, s86 bitop3:0xf6\n"
"v_bitop3_b32 v240, v240, v243, s86 bitop3:0xf6\n"
"v_cmp_ne_u32 vcc, 0, v240\n"
"s_cmp_gt_u32 s60, 2000000\n"
"s_cbranch_scc1 BACK4_%=\n"
"s_cbranch_vccnz LOOP4_%=\n"
"s_branch BACK4_%=\n"
"SLOWC_%=:\n"
"s_mov_b32 s60, 0\n"
"LOOPC_%=:\n"
"buffer_load_dword v29, v17, s[36:39], 0 offen sc1\n"
"s_add_u32 s60, s60, 1\n"
"s_waitcnt vmcnt(0)\n"
"v_cmp_ne_u32 vcc, s86, v29\n"
"s_cmp_gt_u32 s60, 2000000\n"
"s_cbranch_scc1 BACKC_%=\n"
"s_cbranch_vccnz LOOPC_%=\n"
"s_branch BACKC_%=\n"
"SLOWX_%=:\n"
"s_mov_b32 s60, 0\n"
"LOOPX_%=:\n"
"s_mov_b64 exec, vcc\n"
"buffer_load_dword v82, v70, s[36:39], 0 offen sc1\n"
"s_add_u32 s60, s60, 1\n"
"s_waitcnt vmcnt(0)\n"
"v_cmp_ne_u32 vcc, s86, v82\n"
"s_nop 1\n"
"s_cmp_gt_u32 s60, 2000000\n"
"s_cbranch_scc1 OUTX_%=\n"
"s_cbranch_vccnz LOOPX_%=\n"
"OUTX_%=:\n"
"s_mov_b64 exec, -1\n"
"s_branch BACKX_%=\n"
"SLOWG_%=:\n"
"s_mov_b32 s60, 0\n"
"LOOPG_%=:\n"
"buffer_load_dwordx2 v[76:77], v73, s[36:39], 0 offen sc1\n"
"buffer_load_dwordx2 v[78:79], v73, s[36:39], s62 offen sc1\n"
"buffer_load_dwordx2 v[80:81], v73, s[36:39], s63 offen sc1\n"
"s_add_u32 s60, s60, 1\n"
"s_waitcnt vmcnt(0)\n"
"v_xor_b32 v75, s86, v77\n"
"v_bitop3_b32 v75, v75, v79, s86 bitop3:0xf6\n"
"v_bitop3_b32 v75, v75, v81, s86 bitop3:0xf6\n"
"v_cmp_ne_u32 vcc, 0, v75\n"
"s_cmp_gt_u32 s60, 2000000\n"
"s_cbranch_scc1 BACKG_%=\n"
"s_cbranch_vccnz LOOPG_%=\n"
"s_branch BACKG_%=\n"
"SLOWY_%=:\n"
"s_mov_b32 s60, 0\n"
"LOOPY_%=:\n"
"s_mov_b64 exec, vcc\n"
"buffer_load_dword v83, v70, s[36:39], s76 offen sc1\n"
"buffer_load_dword v84, v70, s[36:39], s16 offen sc1\n"
"s_add_u32 s60, s60, 1\n"
"s_waitcnt vmcnt(0)\n"
"v_xor_b32 v71, s86, v83\n"
"v_bitop3_b32 v71, v71, v84, s86 bitop3:0xf6\n"
"v_cmp_ne_u32 vcc, 0, v71\n"
"s_nop 1\n"
"s_cmp_gt_u32 s60, 2000000\n"
"s_cbranch_scc1 OUTY_%=\n"
"s_cbranch_vccnz LOOPY_%=\n"
"OUTY_%=:\n"
"s_mov_b64 exec, -1\n"
"s_branch BACKY_%=\n"
"SLOWR_%=:\n"
"s_mov_b32 s60, 0\n"
"LOOPR_%=:\n"
"v_cmp_eq_u32 vcc, -1, v171\n"
"s_nop 1\n"
"s_mov_b64 exec, vcc\n"
"buffer_load_dwordx4 v[64:67], v8, s[36:39], s70 offen sc1\n"
"buffer_load_dwordx4 v[68:71], v8, s[36:39], s71 offen sc1\n"
"buffer_load_dwordx4 v[72:75], v8, s[36:39], s74 offen sc1\n"
"s_mov_b64 exec, -1\n"
"v_cmp_eq_u32 vcc, -1, v168\n"
"s_nop 1\n"
"s_mov_b64 exec, vcc\n"
"buffer_load_dwordx4 v[80:83], v8, s[36:39], s68 offen sc1\n"
"buffer_load_dwordx4 v[84:87], v8, s[36:39], s68 offen offset:1024 sc1\n"
"s_mov_b64 exec, -1\n"
"v_cmp_eq_u32 vcc, -1, v169\n"
"s_nop 1\n"
"s_mov_b64 exec, vcc\n"
"buffer_load_dwordx4 v[88:91], v8, s[36:39], s68 offen offset:2048 sc1\n"
"buffer_load_dwordx4 v[92:95], v8, s[36:39], s68 offen offset:3072 sc1\n"
"s_mov_b64 exec, -1\n"
"v_cmp_eq_u32 vcc, -1, v170\n"
"s_nop 1\n"
"s_mov_b64 exec, vcc\n"
"buffer_load_dwordx4 v[96:99], v8, s[36:39], s69 offen sc1\n"
"buffer_load_dwordx4 v[100:103], v8, s[36:39], s69 offen offset:1024 sc1\n"
"s_mov_b64 exec, -1\n"
"s_add_u32 s60, s60, 1\n"
"s_waitcnt vmcnt(0)\n"
"v_max3_u32 v171, v64, v65, v66\n"
"v_max3_u32 v171, v171, v67, v68\n"
"v_max3_u32 v171, v171, v69, v70\n"
"v_max3_u32 v171, v171, v71, v72\n"
"v_max3_u32 v171, v171, v73, v74\n"
"v_max3_u32 v171, v171, v75, v75\n"
"v_max3_u32 v168, v80, v81, v82\n"
"v_max3_u32 v168, v168, v83, v84\n"
"v_max3_u32 v168, v168, v85, v86\n"
"v_max3_u32 v168, v168, v87, v87\n"
"v_max3_u32 v169, v88, v89, v90\n"
"v_max3_u32 v169, v169, v91, v92\n"
"v_max3_u32 v169, v169, v93, v94\n"
"v_max3_u32 v169, v169, v95, v95\n"
"v_max3_u32 v170, v96, v97, v98\n"
"v_max3_u32 v170, v170, v99, v100\n"
"v_max3_u32 v170, v170, v101, v102\n"
"v_max3_u32 v170, v170, v103, v103\n"
"v_max3_u32 v172, v168, v169, v170\n"
"v_max_u32 v172, v172, v171\n"
"v_cmp_eq_u32 vcc, -1, v172\n"
"s_cmp_gt_u32 s60, 2000000\n"
"s_cbranch_scc1 BACKR_%=\n"
"s_cbranch_vccnz LOOPR_%=\n"
"s_branch BACKR_%=\n"
"END_%=:\n"
        :
        : [tid] "v"(threadIdx.x), [blk] "s"(blockIdx.x), [ka] "s"(__builtin_amdgcn_kernarg_segment_ptr()), [kt] "s"(k_pos)
        : "v1", "v2", "v3", "v4", "v5", "v6", "v7", "v8", "v9", "v10", "v11", "v12", "v13", "v14", "v15", "v16", "v17", "v18", "v19", "v20", "v21", "v22", "v23", "v24", "v25", "v26", "v27", "v28", "v29", "v30", "v31", "v32", "v33", "v34", "v35", "v36", "v37", "v38", "v39", "v40", "v41", "v42", "v43", "v44", "v45", "v46", "v47", "v48", "v49", "v50", "v51", "v52", "v53", "v54", "v55", "v56", "v57", "v58", "v59", "v60", "v61", "v62", "v63", "v64", "v65", "v66", "v67", "v68", "v69", "v70", "v71", "v72", "v73", "v74", "v75", "v76", "v77", "v78", "v79", "v80", "v81", "v82", "v83", "v84", "v85", "v86", "v87", "v88", "v89", "v90", "v91", "v92", "v93", "v94", "v95", "v96", "v97", "v98", "v99", "v100", "v101", "v102", "v103", "v104", "v105", "v106", "v107", "v108", "v109", "v110", "v111", "v112", "v113", "v114", "v115", "v116", "v117", "v118", "v119", "v120", "v121", "v122", "v123", "v124", "v125", "v126", "v127", "v128", "v129", "v130", "v131", "v132", "v133", "v134", "v135", "v136", "v137", "v138", "v139", "v140", "v141", "v142", "v143", "v144", "v145", "v146", "v147", "v148", "v149", "v150", "v151", "v152", "v153", "v154", "v155", "v156", "v157", "v158", "v159", "v160", "v161", "v162", "v163", "v164", "v165", "v166", "v167", "v168", "v169", "v170", "v171", "v172", "v173", "v174", "v175", "v176", "v177", "v178", "v179", "v180", "v181", "v182", "v183", "v184", "v185", "v186", "v187", "v188", "v189", "v190", "v191", "v192", "v193", "v194", "v195", "v196", "v197", "v198", "v199", "v200", "v201", "v202", "v203", "v204", "v205", "v206", "v207", "v208", "v209", "v210", "v211", "v212", "v213", "v214", "v215", "v216", "v217", "v218", "v219", "v220", "v221", "v222", "v223", "v224", "v225", "v226", "v227", "v228", "v229", "v230", "v231", "v232", "v233", "v234", "v235", "v236", "v237", "v238", "v239", "v240", "v241", "v242", "v243", "v244", "v245", "v246", "v247", "v248", "v249", "v250", "v251", "v252", "v253", "v254", "v255", "a0", "a1", "a2", "a3", "a4", "a5", "a6", "a7", "a8", "a9", "a10", "a11", "a12", "a13", "a14", "a15", "a16", "a17", "a18", "a19", "a20", "a21", "a22", "a23", "a24", "a25", "a26", "a27", "a28", "a29", "a30", "a31", "a32", "a33", "a34", "a35", "a36", "a37", "a38", "a39", "a40", "a41", "a42", "a43", "a44", "a45", "a46", "a47", "a48", "a49", "a50", "a51", "a52", "a53", "a54", "a55", "a56", "a57", "a58", "a59", "a60", "a61", "a62", "a63", "a64", "a65", "a66", "a67", "a68", "a69", "a70", "a71", "a72", "a73", "a74", "a75", "a76", "a77", "a78", "a79", "a80", "a81", "a82", "a83", "a84", "a85", "a86", "a87", "a88", "a89", "a90", "a91", "a92", "a93", "a94", "a95", "a96", "a97", "a98", "a99", "a100", "a101", "a102", "a103", "a104", "a105", "a106", "a107", "a108", "a109", "a110", "a111", "a112", "a113", "a114", "a115", "a116", "a117", "a118", "a119", "a120", "a121", "a122", "a123", "a124", "a125", "a126", "a127", "a128", "a129", "a130", "a131", "a132", "a133", "a134", "a135", "a136", "a137", "a138", "a139", "a140", "a141", "a142", "a143", "a144", "a145", "a146", "a147", "a148", "a149", "a150", "a151", "a152", "a153", "a154", "a155", "a156", "a157", "a158", "a159", "a160", "a161", "a162", "a163", "a164", "a165", "a166", "a167", "a168", "a169", "a170", "a171", "a172", "a173", "a174", "a175", "a176", "a177", "a178", "a179", "a180", "a181", "a182", "a183", "a184", "a185", "a186", "a187", "a188", "a189", "a190", "a191", "a192", "a193", "a194", "a195", "a196", "a197", "a198", "a199", "a200", "a201", "a202", "a203", "a204", "a205", "a206", "a207", "a208", "a209", "a210", "a211", "a212", "a213", "a214", "a215", "a216", "a217", "a218", "a219", "a220", "a221", "a222", "a223", "a224", "a225", "a226", "a227", "a228", "a229", "a230", "a231", "a232", "a233", "a234", "a235", "a236", "a237", "a238", "a239", "a240", "a241", "a242", "a243", "a244", "a245", "a246", "a247", "a248", "a249", "a250", "a251", "a252", "a253", "a254", "a255", "s16", "s17", "s18", "s19", "s20", "s21", "s22", "s23", "s24", "s25", "s26", "s27", "s28", "s29", "s30", "s31", "s33", "s34", "s35", "s36", "s37", "s38", "s39", "s40", "s41", "s42", "s43", "s44", "s45", "s46", "s47", "s48", "s49", "s50", "s51", "s52", "s53", "s54", "s55", "s56", "s57", "s58", "s59", "s60", "s61", "s62", "s63", "s64", "s65", "s66", "s67", "s68", "s69", "s70", "s71", "s72", "s73", "s74", "s75", "s76", "s77", "s78", "s79", "s80", "s81", "s82", "s83", "s84", "s85", "s86", "s87", "s88", "s89", "s90", "s91", "s92", "s93", "s94", "s95", "s96", "s97", "s98", "s99", "vcc", "scc", "memory");
}

extern "C" void kernel_launch(void* const* d_in, const int* in_sizes, int n_in, void* d_out, int out_size, void* d_ws, size_t ws_size, hipStream_t stream) {
    (void)hipFuncSetAttribute((const void*)k_block, hipFuncAttributeMaxDynamicSharedMemorySize, 163840);
    k_block<<<256, 256, 163840, stream>>>(d_in[0], d_in[1], d_in[2], d_in[3], d_in[4], d_in[5], d_in[6], d_in[7], d_in[8], d_in[13], d_in[14], d_in[10], d_in[11], d_in[12], d_ws, d_out);
}
```
